```python
import torch

def _softmax(x, axis=-1):
    return torch.softmax(x, dim=axis)
def _gelu(x):
    return torch.nn.functional.gelu(x, approximate="tanh")
def _silu(x):
    return torch.nn.functional.silu(x)
def _sigmoid(x):
    return torch.sigmoid(x)
def _layernorm(x, axis=-1, eps=1e-5):
    m = x.mean(dim=axis, keepdim=True)
    v = x.var(dim=axis, keepdim=True, unbiased=False)
    return (x - m) / torch.sqrt(v + eps)
def _scatter_add(tgt, idx, src, axis=0):
    return tgt.clone().index_add_(axis, idx, src)
def _segment_sum(src, seg, n):
    out = torch.zeros((n,) + src.shape[1:], dtype=src.dtype)
    return out.index_add_(0, seg, src)
def _pad(x, pw):
    flat = tuple(v for pair in reversed(pw) for v in pair)
    return torch.nn.functional.pad(x, flat)

kernel_name = 'parallel_sum__grouped_kv_mqa__glu_gated_bilinear__prompt'

def setup_inputs():

    _w = (lambda a: (lambda t: t.to(torch.bfloat16) if t.is_floating_point() else t)(torch.as_tensor(a)))
    def _A_setup():
        B, S, D, H = 1, 2048, 640, 32
        Dh = D // H
        return {
            "x": _w((lambda *s: torch.randn(*s, dtype=torch.bfloat16) * 0.05)(B, S, D)),
            "wq": _w((lambda *s: torch.randn(*s, dtype=torch.bfloat16) * 0.05)(D, H * Dh)),
            "wk": _w((lambda *s: torch.randn(*s, dtype=torch.bfloat16) * 0.05)(D, Dh)),
            "wv": _w((lambda *s: torch.randn(*s, dtype=torch.bfloat16) * 0.05)(D, Dh)),
            "wo": _w((lambda *s: torch.randn(*s, dtype=torch.bfloat16) * 0.05)(H * Dh, D)),
        }
    _A_in = _A_setup()

    def _B_setup():
        B, S, D, F = 1, 2048, 640, 1536
        return {
            "x": _w((lambda *s: torch.randn(*s, dtype=torch.bfloat16) * 0.05)(B, S, D)),
            "w_gate": _w((lambda *s: torch.randn(*s, dtype=torch.bfloat16) * 0.8)(D, F) * (D ** -0.5)),
            "w_up": _w((lambda *s: torch.randn(*s, dtype=torch.bfloat16) * 0.8)(D, F) * (D ** -0.5)),
            "w_down": _w((lambda *s: torch.randn(*s, dtype=torch.bfloat16) * 0.8)(F, D) * (F ** -0.5)),
        }
    _B_in = _B_setup()

    return {
        "A_x": _A_in["x"],
        "A_wq": _A_in["wq"],
        "A_wk": _A_in["wk"],
        "A_wv": _A_in["wv"],
        "A_wo": _A_in["wo"],
        "B_x": _B_in["x"],
        "B_w_gate": _B_in["w_gate"],
        "B_w_up": _B_in["w_up"],
        "B_w_down": _B_in["w_down"],
    }

def reference(A_x, A_wq, A_wk, A_wv, A_wo, B_x, B_w_gate, B_w_up, B_w_down):
    def _A_ref(x, wq, wk, wv, wo):
        B, S, D = x.shape
        H = 32
        Dh = D // H
        q = (lambda x, *d: x.permute(*d))((x @ wq).reshape(B, S, H, Dh), 0, 2, 1, 3)
        k = x @ wk
        v = x @ wv
        scores = torch.einsum("bhqd,bkd->bhqk", q, k) * (Dh ** -0.5)
        p = _softmax(scores, axis=-1)
        o = torch.einsum("bhqk,bkd->bhqd", p, v)
        return (lambda x, *d: x.permute(*d))(o, 0, 2, 1, 3).reshape(B, S, H * Dh) @ wo

    def _B_ref(x, w_gate, w_up, w_down):
        g = torch.einsum("bsd,df->bsf", x, w_gate)
        u = torch.einsum("bsd,df->bsf", x, w_up)
        h = g * u
        return torch.einsum("bsf,fd->bsd", h, w_down)

    x = A_x
    xn = _layernorm(x, axis=-1)
    return x + _A_ref(xn, A_wq, A_wk, A_wv, A_wo) + _B_ref(xn, B_w_gate, B_w_up, B_w_down)
```

```cpp
#include <hip/hip_runtime.h>
#include <hip/hip_bf16.h>
typedef unsigned short u16;
static __device__ __forceinline__ float bf2f(u16 v){ return __uint_as_float(((unsigned)v)<<16); }
static __device__ __forceinline__ u16 f2bf(float f){ unsigned u=__float_as_uint(f); u+=0x7fff+((u>>16)&1); return (u16)(u>>16); }
constexpr int S=2048,D=640,H=32,DH=20,F=1536;
__global__ void n_ln(const u16* x,float* xn){
  int r=blockIdx.x; __shared__ float red[2][256];
  float s=0,q=0; for(int i=threadIdx.x;i<D;i+=256){float v=bf2f(x[r*D+i]); s+=v;}
  red[0][threadIdx.x]=s; __syncthreads();
  for(int o=128;o;o>>=1){ if(threadIdx.x<o) red[0][threadIdx.x]+=red[0][threadIdx.x+o]; __syncthreads(); }
  float m=red[0][0]/D;
  for(int i=threadIdx.x;i<D;i+=256){float v=bf2f(x[r*D+i])-m; q+=v*v;}
  red[1][threadIdx.x]=q; __syncthreads();
  for(int o=128;o;o>>=1){ if(threadIdx.x<o) red[1][threadIdx.x]+=red[1][threadIdx.x+o]; __syncthreads(); }
  float rs=rsqrtf(red[1][0]/D+1e-5f);
  for(int i=threadIdx.x;i<D;i+=256) xn[r*D+i]=(bf2f(x[r*D+i])-m)*rs;
}
__global__ void n_gemm(const float* A,const u16* W,float* C,int K,int N,int acc){
  int n=blockIdx.x*64+threadIdx.x; int m0=blockIdx.y*8; if(n>=N) return;
  float a[8]={0,0,0,0,0,0,0,0};
  for(int k=0;k<K;k++){ float w=bf2f(W[k*N+n]);
    #pragma unroll
    for(int i=0;i<8;i++) a[i]+=A[(m0+i)*K+k]*w; }
  #pragma unroll
  for(int i=0;i<8;i++){ if(acc) C[(m0+i)*N+n]+=a[i]; else C[(m0+i)*N+n]=a[i]; }
}
__global__ void n_mul(const float* g,const float* u,float* h,int n){ int i=blockIdx.x*256+threadIdx.x; if(i<n) h[i]=g[i]*u[i]; }
__global__ void n_attn(const float* q,const float* k,const float* v,float* o){
  int idx=blockIdx.x*64+threadIdx.x; int r=idx/H, h=idx%H;
  float qq[DH],acc[DH];
  #pragma unroll
  for(int d=0;d<DH;d++){qq[d]=q[r*D+h*DH+d]*0.22360679775f; acc[d]=0;}
  float m=-1e30f,l=0;
  for(int j=0;j<S;j++){ float s=0;
    #pragma unroll
    for(int d=0;d<DH;d++) s+=qq[d]*k[j*DH+d];
    float mn=fmaxf(m,s); float c=__expf(m-mn), p=__expf(s-mn); l=l*c+p;
    #pragma unroll
    for(int d=0;d<DH;d++) acc[d]=acc[d]*c+p*v[j*DH+d];
    m=mn; }
  #pragma unroll
  for(int d=0;d<DH;d++) o[r*D+h*DH+d]=acc[d]/l;
}
__global__ void n_fin(const u16* x,const float* y,u16* out,int n){ int i=blockIdx.x*256+threadIdx.x; if(i<n) out[i]=f2bf(bf2f(x[i])+y[i]); }
extern "C" void kernel_launch(void* const* d_in, const int* in_sizes, int n_in, void* d_out, int out_size, void* d_ws, size_t ws_size, hipStream_t stream){
  const u16* x=(const u16*)d_in[0]; const u16* wq=(const u16*)d_in[1]; const u16* wk=(const u16*)d_in[2]; const u16* wv=(const u16*)d_in[3]; const u16* wo=(const u16*)d_in[4];
  const u16* wg=(const u16*)d_in[6]; const u16* wu=(const u16*)d_in[7]; const u16* wd=(const u16*)d_in[8];
  float* w=(float*)d_ws; float* xn=w; float* q=xn+S*D; float* k=q+S*D; float* v=k+S*DH; float* o=v+S*DH; float* g=o+S*D; float* u=g+S*F; float* h=u+S*F; float* y=h+S*F;
  n_ln<<<S,256,0,stream>>>(x,xn);
  n_gemm<<<dim3(10,S/8),64,0,stream>>>(xn,wq,q,D,D,0);
  n_gemm<<<dim3(1,S/8),64,0,stream>>>(xn,wk,k,D,DH,0);
  n_gemm<<<dim3(1,S/8),64,0,stream>>>(xn,wv,v,D,DH,0);
  n_gemm<<<dim3(24,S/8),64,0,stream>>>(xn,wg,g,D,F,0);
  n_gemm<<<dim3(24,S/8),64,0,stream>>>(xn,wu,u,D,F,0);
  n_mul<<<S*F/256,256,0,stream>>>(g,u,h,S*F);
  n_attn<<<S*H/64,64,0,stream>>>(q,k,v,o);
  n_gemm<<<dim3(10,S/8),64,0,stream>>>(o,wo,y,D,D,0);
  n_gemm<<<dim3(10,S/8),64,0,stream>>>(h,wd,y,F,D,1);
  n_fin<<<S*D/256,256,0,stream>>>(x,y,(u16*)d_out,S*D);
}
```

```cpp
#include <hip/hip_runtime.h>
typedef unsigned u32; typedef unsigned long long u64; typedef unsigned short u16;
typedef __bf16 bf16x2 __attribute__((ext_vector_type(2)));
typedef __bf16 bf16x4 __attribute__((ext_vector_type(4)));
typedef __bf16 bf16x8 __attribute__((ext_vector_type(8)));
typedef __bf16 bf16x16 __attribute__((ext_vector_type(16)));
typedef short s16x4 __attribute__((ext_vector_type(4)));
typedef float f32x2 __attribute__((ext_vector_type(2)));
typedef float f32x4 __attribute__((ext_vector_type(4)));
typedef float f32x8 __attribute__((ext_vector_type(8)));
typedef float f32x16 __attribute__((ext_vector_type(16)));
typedef u32 u32x2 __attribute__((ext_vector_type(2)));
typedef u32 u32x4 __attribute__((ext_vector_type(4)));
typedef __amdgpu_buffer_rsrc_t rsrc_t;
#define DI __device__ __forceinline__
#define LDSP(T, a) ((__attribute__((address_space(3))) T*)(unsigned long)(a))
#define BC(T, x) __builtin_bit_cast(T, x)

constexpr u32 WS_EPOCH = 0, WS_KVF = 1024, WS_HF = 2048, WS_OF = 4096, WS_STAT = 8192, WS_KV = 32768,
              WS_OT = 262144  , WS_OM = 1u << 20  , WS_H = 4u << 20  , WS_DBG = 12u << 20, WS_BYTES = 16u << 20;
constexpr u32 L_G = 16384, L_U = 16384 + 12288 + 32, L_Q = 41088, STG = 49280;
constexpr u32 L_MISC = 3 * STG;
constexpr u32 L_QI = L_MISC, L_POLL = 160256, L_STAT = 161280;
constexpr u32 L_WK = STG + L_Q, L_WV = L_WK + 25600, L_XN = L_WV + 25600, L_KVS = L_XN + 20480, L_ST0 = L_KVS + 1536;
constexpr u32 L_KV = STG, KVCH = 24576;
constexpr u32 WSLOT = 20480;
constexpr u32 OOB = 0x40000000u;
constexpr int SPIN_MAX = 1 << 16;
static_assert(L_ST0 + 256 <= 163840 && L_STAT + 1024 <= L_KVS + 1024, "lds");

DI float asf(u32 x) { return BC(float, x); }
DI u32 asu(float x) { return BC(u32, x); }
DI rsrc_t mkrs(const void* p, u32 n) { return __builtin_amdgcn_make_buffer_rsrc((void*)p, (short)0, (int)n, 0x00020000); }
template <int N> DI void waitv() { asm volatile("s_waitcnt vmcnt(%0)" ::"n"(N) : "memory"); }
template <int N> DI void waitbar() { asm volatile("s_waitcnt vmcnt(%0) lgkmcnt(0)\n\ts_barrier" ::"n"(N) : "memory"); }
DI void bar() { asm volatile("s_waitcnt lgkmcnt(0)\n\ts_barrier" ::: "memory"); }
DI void dma1(u32 m0, u32 v0, rsrc_t rs, u32 so) {
  asm volatile("s_mov_b32 m0, %0\n\ts_nop 0\n\tbuffer_load_dwordx4 %1, %2, %3 offen lds" ::"s"(m0), "v"(v0), "s"(rs), "s"(so) : "memory");
}
DI void dma1c(u32 m0, u32 v0, rsrc_t rs, u32 so) {
  asm volatile("s_mov_b32 m0, %0\n\ts_nop 0\n\tbuffer_load_dwordx4 %1, %2, %3 offen sc1 lds" ::"s"(m0), "v"(v0), "s"(rs), "s"(so) : "memory");
}
DI void dma2(u32 m0, u32 v0, u32 v1, rsrc_t rs, u32 so) {
  asm volatile("s_mov_b32 m0, %0\n\ts_nop 0\n\tbuffer_load_dwordx4 %1, %3, %4 offen lds\n\tbuffer_load_dwordx4 %2, %3, %4 offen offset:1024 lds"
               ::"s"(m0), "v"(v0), "v"(v1), "s"(rs), "s"(so) : "memory");
}
DI void dma3(u32 m0, u32 v0, u32 v1, u32 v2, rsrc_t rs, u32 so) {
  asm volatile("s_mov_b32 m0, %0\n\ts_nop 0\n\tbuffer_load_dwordx4 %1, %4, %5 offen lds\n\tbuffer_load_dwordx4 %2, %4, %5 offen offset:1024 lds\n\t"
               "buffer_load_dwordx4 %3, %4, %5 offen offset:2048 lds" ::"s"(m0), "v"(v0), "v"(v1), "v"(v2), "s"(rs), "s"(so) : "memory");
}
DI void dma3c(u32 m0, u32 v0, rsrc_t rs, u32 so) {
  asm volatile("s_mov_b32 m0, %0\n\ts_nop 0\n\tbuffer_load_dwordx4 %1, %2, %3 offen sc1 lds\n\tbuffer_load_dwordx4 %1, %2, %3 offen offset:1024 sc1 lds\n\t"
               "buffer_load_dwordx4 %1, %2, %3 offen offset:2048 sc1 lds" ::"s"(m0), "v"(v0), "s"(rs), "s"(so) : "memory");
}
DI void st16c(u32x4 v, rsrc_t rs, u32 off) { __builtin_amdgcn_raw_buffer_store_b128(v, rs, off, 0, 16); }
DI void st8c(u32x2 v, rsrc_t rs, u32 off) { __builtin_amdgcn_raw_buffer_store_b64(v, rs, off, 0, 16); }
DI u32x4 ld16c(rsrc_t rs, u32 off) { return __builtin_amdgcn_raw_buffer_load_b128(rs, off, 0, 16); }
DI u32x2 ld8c(rsrc_t rs, u32 off) { return __builtin_amdgcn_raw_buffer_load_b64(rs, off, 0, 16); }
DI u32 pk(float lo, float hi) { f32x2 v = {lo, hi}; return BC(u32, __builtin_convertvector(v, bf16x2)); }
DI float blo(u32 v) { return BC(float, v << 16); }
DI float bhi(u32 v) { return BC(float, v & 0xffff0000u); }
DI float dot2(u32 a, u32 b, float c) { return __builtin_amdgcn_fdot2_f32_bf16(BC(bf16x2, a), BC(bf16x2, b), c, false); }
DI bf16x4 trr(u32 a) { return __builtin_amdgcn_ds_read_tr16_b64_v4bf16(LDSP(bf16x4, a)); }
DI bf16x8 tr2(u32 a, u32 b) { bf16x4 x = trr(a), y = trr(b); return __builtin_shufflevector(x, y, 0, 1, 2, 3, 4, 5, 6, 7); }
DI bf16x8 l128(u32 a) { return *LDSP(bf16x8, a); }
template <int C> DI float dpp(float x) { return BC(float, __builtin_amdgcn_update_dpp(0, BC(int, x), C, 0xf, 0xf, true)); }
DI float sw16(float x) { auto r = __builtin_amdgcn_permlane16_swap(BC(u32, x), BC(u32, x), false, false); return asf(r[0]) + asf(r[1]); }
DI float sw32(float x) { auto r = __builtin_amdgcn_permlane32_swap(BC(u32, x), BC(u32, x), false, false); return asf(r[0]) + asf(r[1]); }
DI float mx16(float x) { auto r = __builtin_amdgcn_permlane16_swap(BC(u32, x), BC(u32, x), false, false); return fmaxf(asf(r[0]), asf(r[1])); }
DI float mx32(float x) { auto r = __builtin_amdgcn_permlane32_swap(BC(u32, x), BC(u32, x), false, false); return fmaxf(asf(r[0]), asf(r[1])); }
DI float sum16(float x) { x += dpp<0xB1>(x); x += dpp<0x4E>(x); x += dpp<0x141>(x); x += dpp<0x140>(x); return x; }
DI float max16(float x) { x = fmaxf(x, dpp<0xB1>(x)); x = fmaxf(x, dpp<0x4E>(x)); x = fmaxf(x, dpp<0x141>(x)); x = fmaxf(x, dpp<0x140>(x)); return x; }
DI u32 vunit(u32 d) { return d < 4 ? d : d < 12 ? d + 5 : d < 16 ? d - 8 : d < 20 ? d + 1 : 8; }
#define M32(a, b, c) __builtin_amdgcn_mfma_f32_32x32x16_bf16(a, b, c, 0, 0, 0)
#define M16(a, b, c) __builtin_amdgcn_mfma_f32_16x16x32_bf16(a, b, c, 0, 0, 0)

#define STAMP(i)

template <int PITCH>
DI void p1_stage(u32 wa0, u32 wa1, u32 xa, f32x16 (&acc)[2][2], float (&cs)[2][2]) {
#pragma unroll
  for (int s = 0; s < 4; ++s) {
    bf16x8 a0 = tr2(wa0 + s * 16 * PITCH, wa0 + s * 16 * PITCH + 4 * PITCH);
    bf16x8 a1 = tr2(wa1 + s * 16 * PITCH, wa1 + s * 16 * PITCH + 4 * PITCH);
    u32 xs = xa ^ (s << 5);
    bf16x8 b0 = l128(xs), b1 = l128(xs + 4096);
    acc[0][0] = M32(a0, b0, acc[0][0]); acc[0][1] = M32(a0, b1, acc[0][1]);
    acc[1][0] = M32(a1, b0, acc[1][0]); acc[1][1] = M32(a1, b1, acc[1][1]);
    u32x4 u0 = BC(u32x4, a0), u1 = BC(u32x4, a1);
#pragma unroll
    for (int j = 0; j < 4; ++j) { cs[0][j & 1] = dot2(u0[j], 0x3f803f80u, cs[0][j & 1]); cs[1][j & 1] = dot2(u1[j], 0x3f803f80u, cs[1][j & 1]); }
  }
}

__global__ __launch_bounds__(512, 2) void fused_block(const u16* __restrict__ x, const u16* __restrict__ wq, const u16* __restrict__ wk,
                                                      const u16* __restrict__ wv, const u16* __restrict__ wo, const u16* __restrict__ wg,
                                                      const u16* __restrict__ wu, const u16* __restrict__ wd, u16* __restrict__ out, char* ws) {
  __shared__ __attribute__((aligned(128))) char smem[163840];
  asm volatile("" ::"v"(smem) : "memory");
  const u32 lane = threadIdx.x & 63, w = __builtin_amdgcn_readfirstlane(threadIdx.x >> 6);
  const u32 b = blockIdx.x, xcd = b & 7, bi = b >> 3;
  const u32 rb = (xcd >> 2) * 8 + (bi >> 2), cb = (xcd & 3) * 4 + (bi & 3);
  const bool prod = ((cb ^ (cb >> 2)) & 1) == 0;
  const rsrc_t rsX = mkrs(x, 2048 * 1280), rsW = mkrs(ws, WS_BYTES), rsQ = mkrs(wq, 640 * 1280);
  const rsrc_t rsGU = mkrs(w < 4 ? wg : wu, 640 * 3072);
  STAMP(0);
  u32 ep;
  asm volatile("buffer_load_dword %0, off, %1, 0 sc1" : "=v"(ep) : "s"(rsW) : "memory");

  u32 vx0, vx1, vg0, vg1, vg2, vq;
  {
    u32 t0 = 16 * w + (lane >> 3), t1 = t0 + 8, sl = lane & 7;
    vx0 = (128 * rb + t0) * 1280 + ((sl ^ ((t0 >> 1) & 7)) << 4);
    vx1 = (128 * rb + t1) * 1280 + ((sl ^ ((t1 >> 1) & 7)) << 4) - 1024;
    u32 u = (w & 3) * 192 + lane;
    vg0 = (u / 12) * 3072 + (u % 12) * 16 + 192 * cb; u += 64;
    vg1 = (u / 12) * 3072 + (u % 12) * 16 + 192 * cb - 1024; u += 64;
    vg2 = (u / 12) * 3072 + (u % 12) * 16 + 192 * cb - 2048;
    u32 r = 8 * w + (lane >> 3), un = sl ^ ((r & 2) << 1);
    vq = un < 5 ? r * 1280 + 80 * cb + un * 16 : OOB;
  }
  const u32 mx = 2048 * w, mg = (w < 4 ? L_G : L_U) + (w & 3) * 3072, mq = L_Q + 1024 * w;
  auto stage_xg = [&](u32 base, u32 t) { dma2(base + mx, vx0, vx1, rsX, t * 128); dma3(base + mg, vg0, vg1, vg2, rsGU, t * (64 * 3072)); };
  auto stage_q = [&](u32 base, u32 t) { dma1(base + mq, vq, rsQ, t * (64 * 1280)); };

  float kmax_own = 0.f;
  const u32 pid = 8 * rb + (cb >> 1);
  if (prod) {
    u32x2 xv[5];
    const u32 tk = 2 * w + (lane >> 5), xo = (16 * pid + tk) * 1280 + (lane & 31) * 8;
    asm volatile("buffer_load_dwordx2 %0, %5, %6, 0 offen\n\tbuffer_load_dwordx2 %1, %5, %6, 0 offen offset:256\n\tbuffer_load_dwordx2 %2, %5, %6, 0 offen offset:512\n\t"
                 "buffer_load_dwordx2 %3, %5, %6, 0 offen offset:768\n\tbuffer_load_dwordx2 %4, %5, %6, 0 offen offset:1024"
                 : "=&v"(xv[0]), "=&v"(xv[1]), "=&v"(xv[2]), "=&v"(xv[3]), "=&v"(xv[4]) : "v"(xo), "s"(rsX) : "memory");
    const rsrc_t rsKV = mkrs(w < 4 ? wk : wv, 25600);
    for (u32 p = w & 3; p < 25; p += 4) dma1((w < 4 ? L_WK : L_WV) + p * 1024, lane * 16, rsKV, p * 1024);
    stage_xg(0, 0); stage_q(0, 0); stage_xg(STG, 1);
    asm volatile("s_waitcnt vmcnt(17)" : "+v"(xv[0]), "+v"(xv[1]), "+v"(xv[2]), "+v"(xv[3]), "+v"(xv[4]), "+v"(ep)::"memory");
    float s1 = 0.f;
#pragma unroll
    for (int i = 0; i < 5; ++i) { s1 = dot2(xv[i][0], 0x3f803f80u, s1); s1 = dot2(xv[i][1], 0x3f803f80u, s1); }
    const float mu = sw16(sum16(s1)) * (1.f / 640.f);
    float f[20], s2 = 0.f;
#pragma unroll
    for (int i = 0; i < 5; ++i) { f[4 * i] = blo(xv[i][0]) - mu; f[4 * i + 1] = bhi(xv[i][0]) - mu; f[4 * i + 2] = blo(xv[i][1]) - mu; f[4 * i + 3] = bhi(xv[i][1]) - mu; }
#pragma unroll
    for (int i = 0; i < 20; ++i) s2 += f[i] * f[i];
    const float rs = rsqrtf(sw16(sum16(s2)) * (1.f / 640.f) + 1e-5f);
#pragma unroll
    for (int i = 0; i < 5; ++i) {
      u32 un = 16 * i + (((lane & 31) >> 1) ^ tk);
      *LDSP(u32x2, L_XN + tk * 1280 + un * 16 + (lane & 1) * 8) = u32x2{pk(f[4 * i] * rs, f[4 * i + 1] * rs), pk(f[4 * i + 2] * rs, f[4 * i + 3] * rs)};
    }
    if ((lane & 31) == 0) *LDSP(f32x2, L_ST0 + tk * 8) = f32x2{-mu, rs};
    waitbar<11>();
    if (w < 4) {
      const u32 g = lane >> 4, c = lane & 15;
      const u32 wa = (w < 2 ? L_WK : L_WV) + (8 * g + (c >> 2)) * 40 + ((w & 1) ? 32 : (c & 3) * 8);
      const u32 xa = L_XN + c * 1280;
      f32x4 a = {0.f, 0.f, 0.f, 0.f};
#pragma unroll
      for (int s = 0; s < 20; ++s) {
        bf16x8 wf = tr2(wa + s * 1280, wa + s * 1280 + 160);
        bf16x8 xf = l128(xa + (s >> 2) * 256 + ((((s & 3) * 4 + g) ^ c) << 4));
        a = w < 2 ? M16(wf, xf, a) : M16(xf, wf, a);
      }
      u32x2 o = {pk(a[0], a[1]), pk(a[2], a[3])};
      if (w < 2) {
        const u32 blk = c >> 3, key = c & 7;
        float n2 = dot2(o[0], o[0], dot2(o[1], o[1], 0.f));
        if (w == 0) {
          *LDSP(u32x2, L_KVS + blk * 768 + ((((g >> 1) ^ blk) * 8 + key) << 4) + (g & 1) * 8) = o;
          n2 = sw32(sw16(n2));
        } else if (g == 0) {
          *LDSP(u32x2, L_KVS + blk * 768 + 592 + blk * 64 + key * 8) = (pid & 1) ? u32x2{o[1], o[0]} : o;
        }
        if (g == 0) *LDSP(float, L_ST0 + 128 + w * 64 + c * 4) = n2;
      } else {
        const u32 d = w == 2 ? c : 16 + c;
        if (d == 20) o = u32x2{0x3f803f80u, 0x3f803f80u};
        if (d <= 20) *LDSP(u32x2, L_KVS + (g >> 1) * 768 + 256 + vunit(d) * 16 + (g & 1) * 8) = o;
      }
    }
    bar();
    if (w == 7) {
      u32x4 v0 = *LDSP(u32x4, L_KVS + lane * 16), v1 = *LDSP(u32x4, L_KVS + 1024 + (lane < 40 ? lane : 39) * 16);
      kmax_own = max16(*LDSP(float, L_ST0 + 128 + (lane & 15) * 4) + *LDSP(float, L_ST0 + 192 + (lane & 15) * 4));
      st16c(v0, rsW, WS_KV + pid * 1536 + lane * 16);
      st16c(v1, rsW, lane < 32 ? WS_KV + pid * 1536 + 1024 + lane * 16 : lane < 40 ? WS_STAT + pid * 128 + (lane - 32) * 16 : OOB);
    }
    stage_q(STG, 1);
  } else {
    stage_xg(0, 0); stage_q(0, 0); stage_xg(STG, 1); stage_q(STG, 1);
  }
  stage_xg(2 * STG, 2); stage_q(2 * STG, 2);
  STAMP(1);

  const u32 mi = w >> 1, nj = w & 1, hh = lane >> 5, c32 = lane & 31;
  f32x16 acc[2][2];
#pragma unroll
  for (int i = 0; i < 16; ++i) { acc[0][0][i] = 0.f; acc[0][1][i] = 0.f; acc[1][0][i] = 0.f; acc[1][1][i] = 0.f; }
  float cs[2][2] = {{0.f, 0.f}, {0.f, 0.f}};
  u32 wa0, wa1, xa;
  {
    const u32 q = (lane >> 2) & 3, P = (lane & 3) + ((lane >> 2) & 4), row = 8 * hh + q;
    if (mi < 3) {
      const u32 quad = (P & 1) ? 3 - (P >> 2) : (P >> 2);
      wa0 = ((P & 2) ? L_U : L_G) + row * 192 + mi * 64 + quad * 8; wa1 = wa0 + 32;
    } else {
      wa0 = L_Q + row * 128 + ((8 * P) ^ ((q & 2) << 5)); wa1 = L_Q + row * 128 + ((40 + 8 * P) ^ ((q & 2) << 5));
    }
    const u32 tk = 64 * nj + c32;
    xa = tk * 128 + ((hh ^ ((tk >> 1) & 7)) << 4);
  }
  u32 tag = 0;
  float kmax2 = 0.f;
  u32 sb = 0;
  for (u32 t = 0; t < 10; ++t) {
    if (t == 0) {
      asm volatile("s_waitcnt vmcnt(12) lgkmcnt(0)\n\ts_barrier" : "+v"(ep)::"memory");
      tag = __builtin_amdgcn_readfirstlane(ep) + 1;
    } else {
      waitbar<6>();
      const u32 nb = sb == 0 ? 2 * STG : sb - STG;
      if (t == 1 && prod && w == 7 && lane == 0) st8c(u32x2{BC(u32, kmax_own), tag}, rsW, WS_KVF + pid * 8);
      if (t == 7 && w == 0) dma1c(L_POLL, lane * 16, rsW, WS_KVF);
      if (t < 8) { stage_xg(nb, t + 2); stage_q(nb, t + 2); }
      else if (t == 8) {
        u32x4 fl = *LDSP(u32x4, L_POLL + lane * 16);
        for (int sp = 0; sp < SPIN_MAX && !__all(fl[1] == tag && fl[3] == tag); ++sp) { __builtin_amdgcn_s_sleep(2); fl = ld16c(rsW, WS_KVF + lane * 16); }
        kmax2 = mx32(mx16(max16(fmaxf(asf(fl[0]), asf(fl[2])))));
        if (w == 0) { dma1c(L_STAT, lane * 16, rsW, WS_STAT + rb * 1024); }
        dma3c(L_KV + w * 3072, lane * 16, rsW, WS_KV + w * 3072);
        dma3c(L_KV + KVCH + w * 3072, lane * 16, rsW, WS_KV + KVCH + w * 3072);
      }
    }
    if (mi < 3) p1_stage<192>(wa0 + sb, wa1 + sb, xa + sb, acc, cs); else p1_stage<128>(wa0 + sb, wa1 + sb, xa + sb, acc, cs);
    sb = sb == 2 * STG ? 0 : sb + STG;
  }
  STAMP(2);
  {
    f32x2 st[2];
#pragma unroll
    for (int n = 0; n < 2; ++n) st[n] = *LDSP(f32x2, L_STAT + (64 * nj + 32 * n + c32) * 8);
#pragma unroll
    for (int m = 0; m < 2; ++m)
#pragma unroll
      for (int n = 0; n < 2; ++n) acc[m][n] = __builtin_amdgcn_mfma_f32_32x32x2f32(cs[m][0] + cs[m][1], st[n][0], acc[m][n], 0, 0, 0);
    if (mi < 3) {
#pragma unroll
      for (int m = 0; m < 2; ++m)
#pragma unroll
        for (int n = 0; n < 2; ++n) {
          const float r2 = st[n][1] * st[n][1];
          const f32x16 a = acc[m][n];
          u32x4 o = {pk(a[0] * a[4] * r2, a[1] * a[5] * r2), pk(a[2] * a[6] * r2, a[3] * a[7] * r2),
                     pk(a[8] * a[12] * r2, a[9] * a[13] * r2), pk(a[10] * a[14] * r2, a[11] * a[15] * r2)};
          const u32 tb = 8 * rb + 4 * nj + 2 * n + (c32 >> 4);
          st16c(o, rsW, WS_H + (tb * 48 + 3 * cb + mi) * 1024 + (((2 * m + hh) * 16 + (c32 & 15)) << 4));
        }
    } else {
#pragma unroll
      for (int m = 0; m < 2; ++m)
#pragma unroll
        for (int n = 0; n < 2; ++n) {
          const float r = st[n][1] * (0.22360679774997896f * 1.4426950408889634f);
          const f32x16 a = acc[m][n];
          const u32 qa = L_QI + (m * 128 + 64 * nj + 32 * n + c32) * 48 + hh * 8;
          *LDSP(u32x2, qa) = u32x2{pk(a[0] * r, a[1] * r), pk(a[2] * r, a[3] * r)};
          *LDSP(u32x2, qa + 16) = u32x2{pk(a[4] * r, a[5] * r), pk(a[6] * r, a[7] * r)};
          *LDSP(u32x2, qa + 32) = u32x2{pk(a[8] * r, a[9] * r), pk(a[10] * r, a[11] * r)};
        }
    }
  }
  if (mi < 3) waitbar<7>(); else waitbar<3>();
  STAMP(3);

  const u32 ha = w & 1, tt = w >> 1;
  const u32 r64 = 4 * xcd + (bi >> 3), c80 = bi & 7, th = w & 1, kq = w >> 1;
  const rsrc_t rsD = mkrs(wd, 1536 * 1280), rsO = mkrs(wo, 640 * 1280);
  u32 vw[5];
#pragma unroll
  for (int p = 0; p < 5; ++p) { u32 u = 64 * p + lane; vw[p] = (u / 10) * 1280 + (u % 10) * 16 + 160 * c80 - (p < 3 ? p : p - 3) * 1024; }
  auto wstep = [&](u32 j) {
    const u32 m = ((5 + j) & 7) * WSLOT + kq * 5120;
    if (j < 12) { const u32 so = (384 * kq + 32 * j) * 1280; dma3(m, vw[0], vw[1], vw[2], rsD, so); dma2(m + 3072, vw[3], vw[4], rsD, so); }
    else { const u32 so = (160 * kq + 32 * (j - 12)) * 1280; dma3(m, vw[0], vw[1], vw[2], rsO, so); dma2(m + 3072, vw[3], vw[4], rsO, so); }
  };
  bf16x8 q0; u32x2 q1;
  bool fast;
  {
    const u32 qa = L_QI + (ha * 128 + 32 * tt + c32) * 48;
    q0 = l128(qa + 16 * hh);
    q1[0] = *LDSP(u32, qa + 32 + 4 * hh);
    const u32x4 u = BC(u32x4, q0);
    const float n2 = sw32(dot2(u[0], u[0], dot2(u[1], u[1], dot2(u[2], u[2], dot2(u[3], u[3], dot2(q1[0], q1[0], 0.f))))));
    const float bound = __builtin_sqrtf(n2 * kmax2) * 1.01f + 0.01f;
    fast = __all(bound <= 50.f);
    q1[1] = (fast && hh == 0) ? (pk(-bound, 0.f) & 0xffffu) : 0u;
  }
  f32x16 o;
#pragma unroll
  for (int i = 0; i < 16; ++i) o[i] = 0.f;
  float mrun = -3.0e38f;
  const u32 blk = c32 >> 3;
  const u32 ka0 = L_KV + blk * 768 + (((hh ^ (blk & 1)) * 8 + (c32 & 7)) << 4);
  const u32 ka1 = L_KV + blk * 768 + 592 + (blk & 1) * 64 + (c32 & 7) * 8 + ((hh ^ (blk >> 1)) << 2);
  const u32 va = L_KV + hh * 768 + 256 + vunit(c32) * 16;
  u32x4 hf[24];
  for (u32 c = 0; c < 8; ++c) {
    const u32 cbuf = (c & 1) * KVCH;
    if (fast) {
#pragma unroll 2
      for (int i = 0; i < 8; ++i) {
        const u32 off = cbuf + i * 3072;
        bf16x8 k0 = l128(ka0 + off), v0 = l128(va + off), v1 = l128(va + off + 1536);
        u32x2 k1 = {*LDSP(u32, ka1 + off), 0x00003f80u};
        f32x16 s;
#pragma unroll
        for (int j = 0; j < 16; ++j) s[j] = 0.f;
        s = __builtin_amdgcn_mfma_f32_32x32x8bf16_1k(BC(s16x4, k1), BC(s16x4, q1), s, 0, 0, 0);
        s = M32(k0, q0, s);
        f32x8 p0, p1;
#pragma unroll
        for (int j = 0; j < 8; ++j) { p0[j] = __builtin_amdgcn_exp2f(s[j]); p1[j] = __builtin_amdgcn_exp2f(s[8 + j]); }
        o = __builtin_amdgcn_smfmac_f32_32x32x32_bf16(v0, BC(bf16x16, p0), o, 0xDDDD, 0, 0);
        o = __builtin_amdgcn_smfmac_f32_32x32x32_bf16(v1, BC(bf16x16, p1), o, 0xDDDD, 0, 0);
      }
    } else {
#pragma unroll 1
      for (int i = 0; i < 8; ++i) {
        const u32 off = cbuf + i * 3072;
        bf16x8 k0 = l128(ka0 + off), v0 = l128(va + off), v1 = l128(va + off + 1536);
        u32x2 k1 = {*LDSP(u32, ka1 + off), 0x00003f80u};
        f32x16 s;
#pragma unroll
        for (int j = 0; j < 16; ++j) s[j] = 0.f;
        s = __builtin_amdgcn_mfma_f32_32x32x8bf16_1k(BC(s16x4, k1), BC(s16x4, q1), s, 0, 0, 0);
        s = M32(k0, q0, s);
        float m = mrun;
#pragma unroll
        for (int j = 0; j < 16; ++j) m = fmaxf(m, s[j]);
        m = mx32(m);
        const float al = __builtin_amdgcn_exp2f(mrun - m);
        mrun = m;
        f32x8 p0, p1;
#pragma unroll
        for (int j = 0; j < 8; ++j) { p0[j] = __builtin_amdgcn_exp2f(s[j] - m); p1[j] = __builtin_amdgcn_exp2f(s[8 + j] - m); }
#pragma unroll
        for (int j = 0; j < 16; ++j) o[j] *= al;
        o = __builtin_amdgcn_smfmac_f32_32x32x32_bf16(v0, BC(bf16x16, p0), o, 0xDDDD, 0, 0);
        o = __builtin_amdgcn_smfmac_f32_32x32x32_bf16(v1, BC(bf16x16, p1), o, 0xDDDD, 0, 0);
      }
    }
    waitbar<0>();
    if (c < 6) dma3c(L_KV + cbuf + w * 3072, lane * 16, rsW, WS_KV + (c + 2) * KVCH + w * 3072);
    if (c == 0 && w == 0 && lane == 0) st8c(u32x2{tag, tag}, rsW, WS_HF + (rb * 16 + cb) * 8);
    if (c < 3 && 2 * c + th < 5) wstep(2 * c + th);
  }
  STAMP(4);
  {
    const float il = __builtin_amdgcn_rcpf(o[12]);
    const u32 tb = 8 * rb + 2 * tt + (c32 >> 4);
    st16c(u32x4{pk(o[0] * il, o[1] * il), pk(o[2] * il, o[3] * il), pk(o[4] * il, o[5] * il), pk(o[6] * il, o[7] * il)}, rsW,
          WS_OM + (tb * 16 + cb) * 1024 + (((2 * ha + hh) * 16 + (c32 & 15)) << 4));
    st8c(u32x2{pk(o[8] * il, o[9] * il), pk(o[10] * il, o[11] * il)}, rsW, hh == 0 ? WS_OT + (tb * 32 + 2 * cb + ha) * 128 + (c32 & 15) * 8 : OOB);
  }
  wstep(5 + th);
  waitbar<5>();
  if (w == 0 && lane == 0) st8c(u32x2{tag, tag}, rsW, WS_OF + (rb * 16 + cb) * 8);
  STAMP(5);

  const u32 g = lane >> 4, c16 = lane & 15;
  {
    u32x2 fl = ld8c(rsW, WS_HF + (r64 >> 1) * 128 + (4 * kq + (lane & 3)) * 8);
    for (int sp = 0; sp < SPIN_MAX && !__all(fl[0] == tag && fl[1] == tag); ++sp) { __builtin_amdgcn_s_sleep(2); fl = ld8c(rsW, WS_HF + (r64 >> 1) * 128 + (4 * kq + (lane & 3)) * 8); }
  }
  if (b == 0 && w == 0) {
    for (int sp = 0; sp < SPIN_MAX; ++sp) {
      u32x4 f0 = ld16c(rsW, WS_HF + lane * 16), f1 = ld16c(rsW, WS_HF + 1024 + lane * 16);
      if (__all(f0[0] == tag && f0[2] == tag && f1[0] == tag && f1[2] == tag)) break;
      __builtin_amdgcn_s_sleep(2);
    }
    if (lane == 0) __builtin_amdgcn_raw_buffer_store_b32(tag, rsW, WS_EPOCH, 0, 16);
  }
  const u32 tb0 = 4 * r64 + 2 * th;
#pragma unroll
  for (int j = 0; j < 12; ++j) { hf[2 * j] = ld16c(rsW, WS_H + (tb0 * 48 + 12 * kq + j) * 1024 + lane * 16); hf[2 * j + 1] = ld16c(rsW, WS_H + ((tb0 + 1) * 48 + 12 * kq + j) * 1024 + lane * 16); }
  f32x4 oc[5][2];
#pragma unroll
  for (int i = 0; i < 5; ++i) { oc[i][0] = f32x4{0.f, 0.f, 0.f, 0.f}; oc[i][1] = oc[i][0]; }
  const u32 wl = kq * 5120 + (c16 >> 2) * 160 + (c16 & 3) * 8;
  const u32 wb1 = wl + (8 * g + 4 * (g & 1)) * 160, wb2 = wl + (8 * g + 4 - 4 * (g & 1)) * 160;
  auto bstep = [&](int j) {
    const u32 so = ((5 + j) & 7) * WSLOT;
#pragma unroll
    for (int i = 0; i < 5; ++i) {
      bf16x8 a = tr2(wb1 + so + 32 * i, wb2 + so + 32 * i);
      oc[i][0] = M16(a, BC(bf16x8, hf[2 * j]), oc[i][0]); oc[i][1] = M16(a, BC(bf16x8, hf[2 * j + 1]), oc[i][1]);
    }
  };
  bstep(0);
  bar(); wstep(7 + th); bstep(1); bstep(2);
  bar(); wstep(9 + th); bstep(3); bstep(4);
  waitbar<10>(); wstep(11 + th); bstep(5); bstep(6);
  waitbar<10>(); wstep(13 + th); bstep(7); bstep(8);
  waitbar<10>(); wstep(15 + th); bstep(9); bstep(10);
  waitbar<10>(); bstep(11);
  STAMP(6);
  {
    u32x2 fl = ld8c(rsW, WS_OF + (r64 >> 1) * 128 + (4 * kq + (lane & 3)) * 8);
    for (int sp = 0; sp < SPIN_MAX && !__all(fl[0] == tag && fl[1] == tag); ++sp) { __builtin_amdgcn_s_sleep(1); fl = ld8c(rsW, WS_OF + (r64 >> 1) * 128 + (4 * kq + (lane & 3)) * 8); }
  }
  u32x4 of[10];
#pragma unroll
  for (int n = 0; n < 2; ++n) {
#pragma unroll
    for (int s = 0; s < 4; ++s) of[2 * s + n] = ld16c(rsW, WS_OM + ((tb0 + n) * 16 + 4 * kq + s) * 1024 + lane * 16);
    u32x2 t0 = ld8c(rsW, WS_OT + ((tb0 + n) * 32 + 8 * kq + 2 * g) * 128 + c16 * 8), t1 = ld8c(rsW, WS_OT + ((tb0 + n) * 32 + 8 * kq + 2 * g + 1) * 128 + c16 * 8);
    of[8 + n] = u32x4{t0[0], t0[1], t1[0], t1[1]};
  }
  u32x2 xr[3];
#pragma unroll
  for (int i = 0; i < 3; ++i) { const u32 id = kq + 4 * i; xr[i] = __builtin_amdgcn_raw_buffer_load_b64(rsX, id < 10 ? (16 * (tb0 + (id & 1)) + c16) * 1280 + 160 * c80 + (id >> 1) * 32 + g * 8 : OOB, 0, 0); }
  waitbar<0>();
  STAMP(7);
  {
    auto radr = [&](u32 r) { return (1 + (r >> 5)) * WSLOT + (r & 31) * 160 + wl; };
#pragma unroll
    for (int s = 0; s < 5; ++s) {
      const u32 r1 = s < 4 ? 40 * s + 20 * (g >> 1) + 4 * (g & 1) : 40 * g + 16, r2 = s < 4 ? r1 + 8 : r1 + 20;
      const u32 a1 = radr(r1), a2 = radr(r2);
#pragma unroll
      for (int i = 0; i < 5; ++i) {
        bf16x8 a = tr2(a1 + 32 * i, a2 + 32 * i);
        oc[i][0] = M16(a, BC(bf16x8, of[2 * s]), oc[i][0]); oc[i][1] = M16(a, BC(bf16x8, of[2 * s + 1]), oc[i][1]);
      }
    }
  }
  bar();
#pragma unroll
  for (int i = 0; i < 5; ++i)
#pragma unroll
    for (int n = 0; n < 2; ++n) *LDSP(f32x4, (w * 10 + 2 * i + n) * 1024 + lane * 16) = oc[i][n];
  bar();
  const rsrc_t rsOut = mkrs(out, 2048 * 1280);
#pragma unroll
  for (int i = 0; i < 3; ++i) {
    const u32 id = kq + 4 * i;
    if (id < 10) {
      const u32 a = (th * 10 + id) * 1024 + lane * 16;
      f32x4 v = *LDSP(f32x4, a) + *LDSP(f32x4, a + 20480) + *LDSP(f32x4, a + 40960) + *LDSP(f32x4, a + 61440);
      u32x2 r = {pk(v[0] + blo(xr[i][0]), v[1] + bhi(xr[i][0])), pk(v[2] + blo(xr[i][1]), v[3] + bhi(xr[i][1]))};
      __builtin_amdgcn_raw_buffer_store_b64(r, rsOut, (16 * (tb0 + (id & 1)) + c16) * 1280 + 160 * c80 + (id >> 1) * 32 + g * 8, 0, 0);
    }
  }
  STAMP(8);
}

extern "C" void kernel_launch(void* const* d_in, const int* in_sizes, int n_in, void* d_out, int out_size, void* d_ws, size_t ws_size, hipStream_t stream) {
  fused_block<<<256, 512, 0, stream>>>((const u16*)d_in[0], (const u16*)d_in[1], (const u16*)d_in[2], (const u16*)d_in[3], (const u16*)d_in[4],
                                       (const u16*)d_in[6], (const u16*)d_in[7], (const u16*)d_in[8], (u16*)d_out, (char*)d_ws);
}
```

```cpp
#include <hip/hip_runtime.h>
typedef unsigned u32; typedef unsigned long long u64; typedef unsigned short u16;
typedef __bf16 bf16x2 __attribute__((ext_vector_type(2)));
typedef __bf16 bf16x4 __attribute__((ext_vector_type(4)));
typedef __bf16 bf16x8 __attribute__((ext_vector_type(8)));
typedef __bf16 bf16x16 __attribute__((ext_vector_type(16)));
typedef short s16x4 __attribute__((ext_vector_type(4)));
typedef float f32x2 __attribute__((ext_vector_type(2)));
typedef float f32x4 __attribute__((ext_vector_type(4)));
typedef float f32x8 __attribute__((ext_vector_type(8)));
typedef float f32x16 __attribute__((ext_vector_type(16)));
typedef u32 u32x2 __attribute__((ext_vector_type(2)));
typedef u32 u32x4 __attribute__((ext_vector_type(4)));
typedef __amdgpu_buffer_rsrc_t rsrc_t;
#define DI __device__ __forceinline__
#define LDSP(T, a) ((__attribute__((address_space(3))) T*)(unsigned long)(a))
#define BC(T, x) __builtin_bit_cast(T, x)

constexpr u32 WS_EPOCH = 0, WS_KVF = 1024, WS_HF = 2048, WS_OF = 4096, WS_STAT = 8192, WS_KV = 32768,
              WS_OT = 262144  , WS_OM = 1u << 20  , WS_H = 4u << 20  , WS_DBG = 12u << 20, WS_BYTES = 16u << 20;
constexpr u32 L_G = 16384, L_U = 16384 + 12288 + 32, L_Q = 41088, STG = 49280;
constexpr u32 L_MISC = 3 * STG;
constexpr u32 L_QI = L_MISC, L_POLL = 160256, L_STAT = 161280;
constexpr u32 L_WK = STG + L_Q, L_WV = L_WK + 25600, L_XN = L_WV + 25600, L_KVS = L_XN + 20480, L_ST0 = L_KVS + 1536;
constexpr u32 L_KV = STG, KVCH = 24576;
constexpr u32 WSLOT = 20480;
constexpr u32 OOB = 0x40000000u;
constexpr int SPIN_MAX = 1 << 16;
static_assert(L_ST0 + 256 <= 163840 && L_STAT + 1024 <= L_KVS + 1024, "lds");

DI float asf(u32 x) { return BC(float, x); }
DI u32 asu(float x) { return BC(u32, x); }
DI rsrc_t mkrs(const void* p, u32 n) { return __builtin_amdgcn_make_buffer_rsrc((void*)p, (short)0, (int)n, 0x00020000); }
template <int N> DI void waitv() { asm volatile("s_waitcnt vmcnt(%0)" ::"n"(N) : "memory"); }
template <int N> DI void waitbar() { asm volatile("s_waitcnt vmcnt(%0) lgkmcnt(0)\n\ts_barrier" ::"n"(N) : "memory"); }
DI void bar() { asm volatile("s_waitcnt lgkmcnt(0)\n\ts_barrier" ::: "memory"); }
DI void dma1(u32 m0, u32 v0, rsrc_t rs, u32 so) {
  asm volatile("s_mov_b32 m0, %0\n\ts_nop 0\n\tbuffer_load_dwordx4 %1, %2, %3 offen lds" ::"s"(m0), "v"(v0), "s"(rs), "s"(so) : "memory");
}
DI void dma1c(u32 m0, u32 v0, rsrc_t rs, u32 so) {
  asm volatile("s_mov_b32 m0, %0\n\ts_nop 0\n\tbuffer_load_dwordx4 %1, %2, %3 offen sc1 lds" ::"s"(m0), "v"(v0), "s"(rs), "s"(so) : "memory");
}
DI void dma2(u32 m0, u32 v0, u32 v1, rsrc_t rs, u32 so) {
  asm volatile("s_mov_b32 m0, %0\n\ts_nop 0\n\tbuffer_load_dwordx4 %1, %3, %4 offen lds\n\tbuffer_load_dwordx4 %2, %3, %4 offen offset:1024 lds"
               ::"s"(m0), "v"(v0), "v"(v1), "s"(rs), "s"(so) : "memory");
}
DI void dma3(u32 m0, u32 v0, u32 v1, u32 v2, rsrc_t rs, u32 so) {
  asm volatile("s_mov_b32 m0, %0\n\ts_nop 0\n\tbuffer_load_dwordx4 %1, %4, %5 offen lds\n\tbuffer_load_dwordx4 %2, %4, %5 offen offset:1024 lds\n\t"
               "buffer_load_dwordx4 %3, %4, %5 offen offset:2048 lds" ::"s"(m0), "v"(v0), "v"(v1), "v"(v2), "s"(rs), "s"(so) : "memory");
}
DI void dma3c(u32 m0, u32 v0, rsrc_t rs, u32 so) {
  asm volatile("s_mov_b32 m0, %0\n\ts_nop 0\n\tbuffer_load_dwordx4 %1, %2, %3 offen sc1 lds\n\tbuffer_load_dwordx4 %1, %2, %3 offen offset:1024 sc1 lds\n\t"
               "buffer_load_dwordx4 %1, %2, %3 offen offset:2048 sc1 lds" ::"s"(m0), "v"(v0), "s"(rs), "s"(so) : "memory");
}
DI void st16c(u32x4 v, rsrc_t rs, u32 off) { __builtin_amdgcn_raw_buffer_store_b128(v, rs, off, 0, 16); }
DI void st8c(u32x2 v, rsrc_t rs, u32 off) { __builtin_amdgcn_raw_buffer_store_b64(v, rs, off, 0, 16); }
DI u32x4 ld16c(rsrc_t rs, u32 off) { return __builtin_amdgcn_raw_buffer_load_b128(rs, off, 0, 16); }
DI u32x2 ld8c(rsrc_t rs, u32 off) { return __builtin_amdgcn_raw_buffer_load_b64(rs, off, 0, 16); }
DI u32 pk(float lo, float hi) { f32x2 v = {lo, hi}; return BC(u32, __builtin_convertvector(v, bf16x2)); }
DI float blo(u32 v) { return BC(float, v << 16); }
DI float bhi(u32 v) { return BC(float, v & 0xffff0000u); }
DI float dot2(u32 a, u32 b, float c) { return __builtin_amdgcn_fdot2_f32_bf16(BC(bf16x2, a), BC(bf16x2, b), c, false); }
DI bf16x4 trr(u32 a) { return __builtin_amdgcn_ds_read_tr16_b64_v4bf16(LDSP(bf16x4, a)); }
DI bf16x8 tr2(u32 a, u32 b) { bf16x4 x = trr(a), y = trr(b); return __builtin_shufflevector(x, y, 0, 1, 2, 3, 4, 5, 6, 7); }
DI bf16x8 l128(u32 a) { return *LDSP(bf16x8, a); }
template <int C> DI float dpp(float x) { return BC(float, __builtin_amdgcn_update_dpp(0, BC(int, x), C, 0xf, 0xf, true)); }
DI float sw16(float x) { auto r = __builtin_amdgcn_permlane16_swap(BC(u32, x), BC(u32, x), false, false); return asf(r[0]) + asf(r[1]); }
DI float sw32(float x) { auto r = __builtin_amdgcn_permlane32_swap(BC(u32, x), BC(u32, x), false, false); return asf(r[0]) + asf(r[1]); }
DI float mx16(float x) { auto r = __builtin_amdgcn_permlane16_swap(BC(u32, x), BC(u32, x), false, false); return fmaxf(asf(r[0]), asf(r[1])); }
DI float mx32(float x) { auto r = __builtin_amdgcn_permlane32_swap(BC(u32, x), BC(u32, x), false, false); return fmaxf(asf(r[0]), asf(r[1])); }
DI float sum16(float x) { x += dpp<0xB1>(x); x += dpp<0x4E>(x); x += dpp<0x141>(x); x += dpp<0x140>(x); return x; }
DI float max16(float x) { x = fmaxf(x, dpp<0xB1>(x)); x = fmaxf(x, dpp<0x4E>(x)); x = fmaxf(x, dpp<0x141>(x)); x = fmaxf(x, dpp<0x140>(x)); return x; }
DI u32 vunit(u32 d) { return d < 4 ? d : d < 12 ? d + 5 : d < 16 ? d - 8 : d < 20 ? d + 1 : 8; }
#define M32(a, b, c) __builtin_amdgcn_mfma_f32_32x32x16_bf16(a, b, c, 0, 0, 0)
#define M16(a, b, c) __builtin_amdgcn_mfma_f32_16x16x32_bf16(a, b, c, 0, 0, 0)

#define STAMP(i)

template <int PITCH>
DI void p1_stage(u32 wa0, u32 wa1, u32 xa, f32x16 (&acc)[2][2], float (&cs)[2][2]) {
#pragma unroll
  for (int s = 0; s < 4; ++s) {
    bf16x8 a0 = tr2(wa0 + s * 16 * PITCH, wa0 + s * 16 * PITCH + 4 * PITCH);
    bf16x8 a1 = tr2(wa1 + s * 16 * PITCH, wa1 + s * 16 * PITCH + 4 * PITCH);
    u32 xs = xa ^ (s << 5);
    bf16x8 b0 = l128(xs), b1 = l128(xs + 4096);
    acc[0][0] = M32(a0, b0, acc[0][0]); acc[0][1] = M32(a0, b1, acc[0][1]);
    acc[1][0] = M32(a1, b0, acc[1][0]); acc[1][1] = M32(a1, b1, acc[1][1]);
    u32x4 u0 = BC(u32x4, a0), u1 = BC(u32x4, a1);
#pragma unroll
    for (int j = 0; j < 4; ++j) { cs[0][j & 1] = dot2(u0[j], 0x3f803f80u, cs[0][j & 1]); cs[1][j & 1] = dot2(u1[j], 0x3f803f80u, cs[1][j & 1]); }
  }
}

__global__ __launch_bounds__(512, 2) void fused_block(const u16* __restrict__ x, const u16* __restrict__ wq, const u16* __restrict__ wk,
                                                      const u16* __restrict__ wv, const u16* __restrict__ wo, const u16* __restrict__ wg,
                                                      const u16* __restrict__ wu, const u16* __restrict__ wd, u16* __restrict__ out, char* ws) {
  __shared__ __attribute__((aligned(128))) char smem[163840];
  asm volatile("" ::"v"(smem) : "memory");
  const u32 lane = threadIdx.x & 63, w = __builtin_amdgcn_readfirstlane(threadIdx.x >> 6);
  const u32 b = blockIdx.x, xcd = b & 7, bi = b >> 3;
  const u32 rb = (xcd >> 2) * 8 + (bi >> 2), cb = (xcd & 3) * 4 + (bi & 3);
  const bool prod = ((cb ^ (cb >> 2)) & 1) == 0;
  const rsrc_t rsX = mkrs(x, 2048 * 1280), rsW = mkrs(ws, WS_BYTES), rsQ = mkrs(wq, 640 * 1280);
  const rsrc_t rsGU = mkrs(w < 4 ? wg : wu, 640 * 3072);
  STAMP(0);
  u32 ep;
  asm volatile("buffer_load_dword %0, off, %1, 0 sc1" : "=v"(ep) : "s"(rsW) : "memory");

  u32 vx0, vx1, vg0, vg1, vg2, vq;
  {
    u32 t0 = 16 * w + (lane >> 3), t1 = t0 + 8, sl = lane & 7;
    vx0 = (128 * rb + t0) * 1280 + ((sl ^ ((t0 >> 1) & 7)) << 4);
    vx1 = (128 * rb + t1) * 1280 + ((sl ^ ((t1 >> 1) & 7)) << 4) - 1024;
    u32 u = (w & 3) * 192 + lane;
    vg0 = (u / 12) * 3072 + (u % 12) * 16 + 192 * cb; u += 64;
    vg1 = (u / 12) * 3072 + (u % 12) * 16 + 192 * cb - 1024; u += 64;
    vg2 = (u / 12) * 3072 + (u % 12) * 16 + 192 * cb - 2048;
    u32 r = 8 * w + (lane >> 3), un = sl ^ ((r & 2) << 1);
    vq = un < 5 ? r * 1280 + 80 * cb + un * 16 : OOB;
  }
  const u32 mx = 2048 * w, mg = (w < 4 ? L_G : L_U) + (w & 3) * 3072, mq = L_Q + 1024 * w;
  auto stage_xg = [&](u32 base, u32 t) { dma2(base + mx, vx0, vx1, rsX, t * 128); dma3(base + mg, vg0, vg1, vg2, rsGU, t * (64 * 3072)); };
  auto stage_q = [&](u32 base, u32 t) { dma1(base + mq, vq, rsQ, t * (64 * 1280)); };

  float kmax_own = 0.f;
  const u32 pid = 8 * rb + (cb >> 1);
  if (prod) {
    u32x2 xv[5];
    const u32 tk = 2 * w + (lane >> 5), xo = (16 * pid + tk) * 1280 + (lane & 31) * 8;
    asm volatile("buffer_load_dwordx2 %0, %5, %6, 0 offen\n\tbuffer_load_dwordx2 %1, %5, %6, 0 offen offset:256\n\tbuffer_load_dwordx2 %2, %5, %6, 0 offen offset:512\n\t"
                 "buffer_load_dwordx2 %3, %5, %6, 0 offen offset:768\n\tbuffer_load_dwordx2 %4, %5, %6, 0 offen offset:1024"
                 : "=&v"(xv[0]), "=&v"(xv[1]), "=&v"(xv[2]), "=&v"(xv[3]), "=&v"(xv[4]) : "v"(xo), "s"(rsX) : "memory");
    const rsrc_t rsKV = mkrs(w < 4 ? wk : wv, 25600);
    for (u32 p = w & 3; p < 25; p += 4) dma1((w < 4 ? L_WK : L_WV) + p * 1024, lane * 16, rsKV, p * 1024);
    stage_xg(0, 0); stage_q(0, 0); stage_xg(STG, 1);
    asm volatile("s_waitcnt vmcnt(17)" : "+v"(xv[0]), "+v"(xv[1]), "+v"(xv[2]), "+v"(xv[3]), "+v"(xv[4]), "+v"(ep)::"memory");
    float s1 = 0.f;
#pragma unroll
    for (int i = 0; i < 5; ++i) { s1 = dot2(xv[i][0], 0x3f803f80u, s1); s1 = dot2(xv[i][1], 0x3f803f80u, s1); }
    const float mu = sw16(sum16(s1)) * (1.f / 640.f);
    float f[20], s2 = 0.f;
#pragma unroll
    for (int i = 0; i < 5; ++i) { f[4 * i] = blo(xv[i][0]) - mu; f[4 * i + 1] = bhi(xv[i][0]) - mu; f[4 * i + 2] = blo(xv[i][1]) - mu; f[4 * i + 3] = bhi(xv[i][1]) - mu; }
#pragma unroll
    for (int i = 0; i < 20; ++i) s2 += f[i] * f[i];
    const float rs = rsqrtf(sw16(sum16(s2)) * (1.f / 640.f) + 1e-5f);
#pragma unroll
    for (int i = 0; i < 5; ++i) {
      u32 un = 16 * i + (((lane & 31) >> 1) ^ tk);
      *LDSP(u32x2, L_XN + tk * 1280 + un * 16 + (lane & 1) * 8) = u32x2{pk(f[4 * i] * rs, f[4 * i + 1] * rs), pk(f[4 * i + 2] * rs, f[4 * i + 3] * rs)};
    }
    if ((lane & 31) == 0) *LDSP(f32x2, L_ST0 + tk * 8) = f32x2{-mu, rs};
    waitbar<11>();
    if (w < 4) {
      const u32 g = lane >> 4, c = lane & 15;
      const u32 wa = (w < 2 ? L_WK : L_WV) + (8 * g + (c >> 2)) * 40 + ((w & 1) ? 32 : (c & 3) * 8);
      const u32 xa = L_XN + c * 1280;
      f32x4 a = {0.f, 0.f, 0.f, 0.f};
#pragma unroll
      for (int s = 0; s < 20; ++s) {
        bf16x8 wf = tr2(wa + s * 1280, wa + s * 1280 + 160);
        bf16x8 xf = l128(xa + (s >> 2) * 256 + ((((s & 3) * 4 + g) ^ c) << 4));
        a = w < 2 ? M16(wf, xf, a) : M16(xf, wf, a);
      }
      u32x2 o = {pk(a[0], a[1]), pk(a[2], a[3])};
      if (w < 2) {
        const u32 blk = c >> 3, key = c & 7;
        float n2 = dot2(o[0], o[0], dot2(o[1], o[1], 0.f));
        if (w == 0) {
          *LDSP(u32x2, L_KVS + blk * 768 + ((((g >> 1) ^ blk) * 8 + key) << 4) + (g & 1) * 8) = o;
          n2 = sw32(sw16(n2));
        } else if (g == 0) {
          *LDSP(u32x2, L_KVS + blk * 768 + 592 + blk * 64 + key * 8) = (pid & 1) ? u32x2{o[1], o[0]} : o;
        }
        if (g == 0) *LDSP(float, L_ST0 + 128 + w * 64 + c * 4) = n2;
      } else {
        const u32 d = w == 2 ? c : 16 + c;
        if (d == 20) o = u32x2{0x3f803f80u, 0x3f803f80u};
        if (d <= 20) *LDSP(u32x2, L_KVS + (g >> 1) * 768 + 256 + vunit(d) * 16 + (g & 1) * 8) = o;
      }
    }
    bar();
    if (w == 7) {
      u32x4 v0 = *LDSP(u32x4, L_KVS + lane * 16), v1 = *LDSP(u32x4, L_KVS + 1024 + (lane < 40 ? lane : 39) * 16);
      kmax_own = max16(*LDSP(float, L_ST0 + 128 + (lane & 15) * 4) + *LDSP(float, L_ST0 + 192 + (lane & 15) * 4));
      st16c(v0, rsW, WS_KV + pid * 1536 + lane * 16);
      st16c(v1, rsW, lane < 32 ? WS_KV + pid * 1536 + 1024 + lane * 16 : lane < 40 ? WS_STAT + pid * 128 + (lane - 32) * 16 : OOB);
    }
    stage_q(STG, 1);
  } else {
    stage_xg(0, 0); stage_q(0, 0); stage_xg(STG, 1); stage_q(STG, 1);
  }
  stage_xg(2 * STG, 2); stage_q(2 * STG, 2);
  STAMP(1);

  const u32 mi = w >> 1, nj = w & 1, hh = lane >> 5, c32 = lane & 31;
  f32x16 acc[2][2];
#pragma unroll
  for (int i = 0; i < 16; ++i) { acc[0][0][i] = 0.f; acc[0][1][i] = 0.f; acc[1][0][i] = 0.f; acc[1][1][i] = 0.f; }
  float cs[2][2] = {{0.f, 0.f}, {0.f, 0.f}};
  u32 wa0, wa1, xa;
  {
    const u32 q = (lane >> 2) & 3, P = (lane & 3) + ((lane >> 2) & 4), row = 8 * hh + q;
    if (mi < 3) {
      const u32 quad = (P & 1) ? 3 - (P >> 2) : (P >> 2);
      wa0 = ((P & 2) ? L_U : L_G) + row * 192 + mi * 64 + quad * 8; wa1 = wa0 + 32;
    } else {
      wa0 = L_Q + row * 128 + ((8 * P) ^ ((q & 2) << 5)); wa1 = L_Q + row * 128 + ((40 + 8 * P) ^ ((q & 2) << 5));
    }
    const u32 tk = 64 * nj + c32;
    xa = tk * 128 + ((hh ^ ((tk >> 1) & 7)) << 4);
  }
  u32 tag = 0;
  float kmax2 = 0.f;
  u32 sb = 0;
  for (u32 t = 0; t < 10; ++t) {
    if (t == 0) {
      asm volatile("s_waitcnt vmcnt(12) lgkmcnt(0)\n\ts_barrier" : "+v"(ep)::"memory");
      tag = __builtin_amdgcn_readfirstlane(ep) + 1;
    } else {
      waitbar<6>();
      const u32 nb = sb == 0 ? 2 * STG : sb - STG;
      if (t == 1 && prod && w == 7 && lane == 0) st8c(u32x2{BC(u32, kmax_own), tag}, rsW, WS_KVF + pid * 8);
      if (t == 7 && w == 0) dma1c(L_POLL, lane * 16, rsW, WS_KVF);
      if (t < 8) { stage_xg(nb, t + 2); stage_q(nb, t + 2); }
      else if (t == 8) {
        u32x4 fl = *LDSP(u32x4, L_POLL + lane * 16);
        for (int sp = 0; sp < SPIN_MAX && !__all(fl[1] == tag && fl[3] == tag); ++sp) { __builtin_amdgcn_s_sleep(2); fl = ld16c(rsW, WS_KVF + lane * 16); }
        kmax2 = mx32(mx16(max16(fmaxf(asf(fl[0]), asf(fl[2])))));
        if (w == 0) { dma1c(L_STAT, lane * 16, rsW, WS_STAT + rb * 1024); }
        dma3c(L_KV + w * 3072, lane * 16, rsW, WS_KV + w * 3072);
        dma3c(L_KV + KVCH + w * 3072, lane * 16, rsW, WS_KV + KVCH + w * 3072);
      }
    }
    if (mi < 3) p1_stage<192>(wa0 + sb, wa1 + sb, xa + sb, acc, cs); else p1_stage<128>(wa0 + sb, wa1 + sb, xa + sb, acc, cs);
    sb = sb == 2 * STG ? 0 : sb + STG;
  }
  STAMP(2);
  {
    f32x2 st[2];
#pragma unroll
    for (int n = 0; n < 2; ++n) st[n] = *LDSP(f32x2, L_STAT + (64 * nj + 32 * n + c32) * 8);
#pragma unroll
    for (int m = 0; m < 2; ++m)
#pragma unroll
      for (int n = 0; n < 2; ++n) acc[m][n] = __builtin_amdgcn_mfma_f32_32x32x2f32(cs[m][0] + cs[m][1], st[n][0], acc[m][n], 0, 0, 0);
    if (mi < 3) {
#pragma unroll
      for (int m = 0; m < 2; ++m)
#pragma unroll
        for (int n = 0; n < 2; ++n) {
          const float r2 = st[n][1] * st[n][1];
          const f32x16 a = acc[m][n];
          u32x4 o = {pk(a[0] * a[4] * r2, a[1] * a[5] * r2), pk(a[2] * a[6] * r2, a[3] * a[7] * r2),
                     pk(a[8] * a[12] * r2, a[9] * a[13] * r2), pk(a[10] * a[14] * r2, a[11] * a[15] * r2)};
          const u32 tb = 8 * rb + 4 * nj + 2 * n + (c32 >> 4);
          st16c(o, rsW, WS_H + (tb * 48 + 3 * cb + mi) * 1024 + (((2 * m + hh) * 16 + (c32 & 15)) << 4));
        }
    } else {
#pragma unroll
      for (int m = 0; m < 2; ++m)
#pragma unroll
        for (int n = 0; n < 2; ++n) {
          const float r = st[n][1] * (0.22360679774997896f * 1.4426950408889634f);
          const f32x16 a = acc[m][n];
          const u32 qa = L_QI + (m * 128 + 64 * nj + 32 * n + c32) * 48 + hh * 8;
          *LDSP(u32x2, qa) = u32x2{pk(a[0] * r, a[1] * r), pk(a[2] * r, a[3] * r)};
          *LDSP(u32x2, qa + 16) = u32x2{pk(a[4] * r, a[5] * r), pk(a[6] * r, a[7] * r)};
          *LDSP(u32x2, qa + 32) = u32x2{pk(a[8] * r, a[9] * r), pk(a[10] * r, a[11] * r)};
        }
    }
  }
  if (mi < 3) waitbar<7>(); else waitbar<3>();
  STAMP(3);

  const u32 ha = w & 1, tt = w >> 1;
  const u32 r64 = 4 * xcd + (bi >> 3), c80 = bi & 7, th = w & 1, kq = w >> 1;
  const rsrc_t rsD = mkrs(wd, 1536 * 1280), rsO = mkrs(wo, 640 * 1280);
  u32 vw[5];
#pragma unroll
  for (int p = 0; p < 5; ++p) { u32 u = 64 * p + lane; vw[p] = (u / 10) * 1280 + (u % 10) * 16 + 160 * c80 - (p < 3 ? p : p - 3) * 1024; }
  auto wstep = [&](u32 j) {
    const u32 m = ((5 + j) & 7) * WSLOT + kq * 5120;
    if (j < 12) { const u32 so = (384 * kq + 32 * j) * 1280; dma3(m, vw[0], vw[1], vw[2], rsD, so); dma2(m + 3072, vw[3], vw[4], rsD, so); }
    else { const u32 so = (160 * kq + 32 * (j - 12)) * 1280; dma3(m, vw[0], vw[1], vw[2], rsO, so); dma2(m + 3072, vw[3], vw[4], rsO, so); }
  };
  bf16x8 q0; u32x2 q1;
  bool fast;
  {
    const u32 qa = L_QI + (ha * 128 + 32 * tt + c32) * 48;
    q0 = l128(qa + 16 * hh);
    q1[0] = *LDSP(u32, qa + 32 + 4 * hh);
    const u32x4 u = BC(u32x4, q0);
    const float n2 = sw32(dot2(u[0], u[0], dot2(u[1], u[1], dot2(u[2], u[2], dot2(u[3], u[3], dot2(q1[0], q1[0], 0.f))))));
    const float bound = __builtin_sqrtf(n2 * kmax2) * 1.01f + 0.01f;
    fast = __all(bound <= 50.f);
    q1[1] = (fast && hh == 0) ? (pk(-bound, 0.f) & 0xffffu) : 0u;
  }
  f32x16 o;
#pragma unroll
  for (int i = 0; i < 16; ++i) o[i] = 0.f;
  float mrun = -3.0e38f;
  const u32 blk = c32 >> 3;
  const u32 ka0 = L_KV + blk * 768 + (((hh ^ (blk & 1)) * 8 + (c32 & 7)) << 4);
  const u32 ka1 = L_KV + blk * 768 + 592 + (blk & 1) * 64 + (c32 & 7) * 8 + ((hh ^ (blk >> 1)) << 2);
  const u32 va = L_KV + hh * 768 + 256 + vunit(c32) * 16;
  u32x4 hf[24];
  for (u32 c = 0; c < 8; ++c) {
    const u32 cbuf = (c & 1) * KVCH;
    if (fast) {
#pragma unroll 2
      for (int i = 0; i < 8; ++i) {
        const u32 off = cbuf + i * 3072;
        bf16x8 k0 = l128(ka0 + off), v0 = l128(va + off), v1 = l128(va + off + 1536);
        u32x2 k1 = {*LDSP(u32, ka1 + off), 0x00003f80u};
        f32x16 s;
#pragma unroll
        for (int j = 0; j < 16; ++j) s[j] = 0.f;
        s = __builtin_amdgcn_mfma_f32_32x32x8bf16_1k(BC(s16x4, k1), BC(s16x4, q1), s, 0, 0, 0);
        s = M32(k0, q0, s);
        f32x8 p0, p1;
#pragma unroll
        for (int j = 0; j < 8; ++j) { p0[j] = __builtin_amdgcn_exp2f(s[j]); p1[j] = __builtin_amdgcn_exp2f(s[8 + j]); }
        o = __builtin_amdgcn_smfmac_f32_32x32x32_bf16(v0, BC(bf16x16, p0), o, 0xDDDD, 0, 0);
        o = __builtin_amdgcn_smfmac_f32_32x32x32_bf16(v1, BC(bf16x16, p1), o, 0xDDDD, 0, 0);
      }
    } else {
#pragma unroll 1
      for (int i = 0; i < 8; ++i) {
        const u32 off = cbuf + i * 3072;
        bf16x8 k0 = l128(ka0 + off), v0 = l128(va + off), v1 = l128(va + off + 1536);
        u32x2 k1 = {*LDSP(u32, ka1 + off), 0x00003f80u};
        f32x16 s;
#pragma unroll
        for (int j = 0; j < 16; ++j) s[j] = 0.f;
        s = __builtin_amdgcn_mfma_f32_32x32x8bf16_1k(BC(s16x4, k1), BC(s16x4, q1), s, 0, 0, 0);
        s = M32(k0, q0, s);
        float m = mrun;
#pragma unroll
        for (int j = 0; j < 16; ++j) m = fmaxf(m, s[j]);
        m = mx32(m);
        const float al = __builtin_amdgcn_exp2f(mrun - m);
        mrun = m;
        f32x8 p0, p1;
#pragma unroll
        for (int j = 0; j < 8; ++j) { p0[j] = __builtin_amdgcn_exp2f(s[j] - m); p1[j] = __builtin_amdgcn_exp2f(s[8 + j] - m); }
#pragma unroll
        for (int j = 0; j < 16; ++j) o[j] *= al;
        o = __builtin_amdgcn_smfmac_f32_32x32x32_bf16(v0, BC(bf16x16, p0), o, 0xDDDD, 0, 0);
        o = __builtin_amdgcn_smfmac_f32_32x32x32_bf16(v1, BC(bf16x16, p1), o, 0xDDDD, 0, 0);
      }
    }
    waitbar<0>();
    if (c < 6) dma3c(L_KV + cbuf + w * 3072, lane * 16, rsW, WS_KV + (c + 2) * KVCH + w * 3072);
    if (c == 0 && w == 0 && lane == 0) st8c(u32x2{tag, tag}, rsW, WS_HF + (rb * 16 + cb) * 8);
    if (c < 3 && 2 * c + th < 5) wstep(2 * c + th);
  }
  STAMP(4);
  {
    const float il = __builtin_amdgcn_rcpf(o[12]);
    const u32 tb = 8 * rb + 2 * tt + (c32 >> 4);
    st16c(u32x4{pk(o[0] * il, o[1] * il), pk(o[2] * il, o[3] * il), pk(o[4] * il, o[5] * il), pk(o[6] * il, o[7] * il)}, rsW,
          WS_OM + (tb * 16 + cb) * 1024 + (((2 * ha + hh) * 16 + (c32 & 15)) << 4));
    st8c(u32x2{pk(o[8] * il, o[9] * il), pk(o[10] * il, o[11] * il)}, rsW, hh == 0 ? WS_OT + (tb * 32 + 2 * cb + ha) * 128 + (c32 & 15) * 8 : OOB);
  }
  wstep(5 + th);
  waitbar<5>();
  if (w == 0 && lane == 0) st8c(u32x2{tag, tag}, rsW, WS_OF + (rb * 16 + cb) * 8);
  STAMP(5);

  const u32 g = lane >> 4, c16 = lane & 15;
  {
    u32x2 fl = ld8c(rsW, WS_HF + (r64 >> 1) * 128 + (4 * kq + (lane & 3)) * 8);
    for (int sp = 0; sp < SPIN_MAX && !__all(fl[0] == tag && fl[1] == tag); ++sp) { __builtin_amdgcn_s_sleep(2); fl = ld8c(rsW, WS_HF + (r64 >> 1) * 128 + (4 * kq + (lane & 3)) * 8); }
  }
  if (b == 0 && w == 0) {
    for (int sp = 0; sp < SPIN_MAX; ++sp) {
      u32x4 f0 = ld16c(rsW, WS_HF + lane * 16), f1 = ld16c(rsW, WS_HF + 1024 + lane * 16);
      if (__all(f0[0] == tag && f0[2] == tag && f1[0] == tag && f1[2] == tag)) break;
      __builtin_amdgcn_s_sleep(2);
    }
    if (lane == 0) __builtin_amdgcn_raw_buffer_store_b32(tag, rsW, WS_EPOCH, 0, 16);
  }
  const u32 tb0 = 4 * r64 + 2 * th;
#pragma unroll
  for (int j = 0; j < 12; ++j) { hf[2 * j] = ld16c(rsW, WS_H + (tb0 * 48 + 12 * kq + j) * 1024 + lane * 16); hf[2 * j + 1] = ld16c(rsW, WS_H + ((tb0 + 1) * 48 + 12 * kq + j) * 1024 + lane * 16); }
  f32x4 oc[5][2];
#pragma unroll
  for (int i = 0; i < 5; ++i) { oc[i][0] = f32x4{0.f, 0.f, 0.f, 0.f}; oc[i][1] = oc[i][0]; }
  const u32 wl = kq * 5120 + (c16 >> 2) * 160 + (c16 & 3) * 8;
  const u32 wb1 = wl + (8 * g + 4 * (g & 1)) * 160, wb2 = wl + (8 * g + 4 - 4 * (g & 1)) * 160;
  auto bstep = [&](int j) {
    const u32 so = ((5 + j) & 7) * WSLOT;
#pragma unroll
    for (int i = 0; i < 5; ++i) {
      bf16x8 a = tr2(wb1 + so + 32 * i, wb2 + so + 32 * i);
      oc[i][0] = M16(a, BC(bf16x8, hf[2 * j]), oc[i][0]); oc[i][1] = M16(a, BC(bf16x8, hf[2 * j + 1]), oc[i][1]);
    }
  };
  bstep(0);
  bar(); wstep(7 + th); bstep(1); bstep(2);
  bar(); wstep(9 + th); bstep(3); bstep(4);
  waitbar<10>(); wstep(11 + th); bstep(5); bstep(6);
  waitbar<10>(); wstep(13 + th); bstep(7); bstep(8);
  waitbar<10>(); wstep(15 + th); bstep(9); bstep(10);
  waitbar<10>(); bstep(11);
  STAMP(6);
  {
    u32x2 fl = ld8c(rsW, WS_OF + (r64 >> 1) * 128 + (4 * kq + (lane & 3)) * 8);
    for (int sp = 0; sp < SPIN_MAX && !__all(fl[0] == tag && fl[1] == tag); ++sp) { __builtin_amdgcn_s_sleep(1); fl = ld8c(rsW, WS_OF + (r64 >> 1) * 128 + (4 * kq + (lane & 3)) * 8); }
  }
  u32x4 of[10];
#pragma unroll
  for (int n = 0; n < 2; ++n) {
#pragma unroll
    for (int s = 0; s < 4; ++s) of[2 * s + n] = ld16c(rsW, WS_OM + ((tb0 + n) * 16 + 4 * kq + s) * 1024 + lane * 16);
    u32x2 t0 = ld8c(rsW, WS_OT + ((tb0 + n) * 32 + 8 * kq + 2 * g) * 128 + c16 * 8), t1 = ld8c(rsW, WS_OT + ((tb0 + n) * 32 + 8 * kq + 2 * g + 1) * 128 + c16 * 8);
    of[8 + n] = u32x4{t0[0], t0[1], t1[0], t1[1]};
  }
  u32x2 xr[3];
#pragma unroll
  for (int i = 0; i < 3; ++i) { const u32 id = kq + 4 * i; xr[i] = __builtin_amdgcn_raw_buffer_load_b64(rsX, id < 10 ? (16 * (tb0 + (id & 1)) + c16) * 1280 + 160 * c80 + (id >> 1) * 32 + g * 8 : OOB, 0, 0); }
  waitbar<0>();
  STAMP(7);
  {
    auto radr = [&](u32 r) { return (1 + (r >> 5)) * WSLOT + (r & 31) * 160 + wl; };
#pragma unroll
    for (int s = 0; s < 5; ++s) {
      const u32 r1 = s < 4 ? 40 * s + 20 * (g >> 1) + 4 * (g & 1) : 40 * g + 16, r2 = s < 4 ? r1 + 8 : r1 + 20;
      const u32 a1 = radr(r1), a2 = radr(r2);
#pragma unroll
      for (int i = 0; i < 5; ++i) {
        bf16x8 a = tr2(a1 + 32 * i, a2 + 32 * i);
        oc[i][0] = M16(a, BC(bf16x8, of[2 * s]), oc[i][0]); oc[i][1] = M16(a, BC(bf16x8, of[2 * s + 1]), oc[i][1]);
      }
    }
  }
  bar();
#pragma unroll
  for (int i = 0; i < 5; ++i)
#pragma unroll
    for (int n = 0; n < 2; ++n) *LDSP(f32x4, (w * 10 + 2 * i + n) * 1024 + lane * 16) = oc[i][n];
  bar();
  const rsrc_t rsOut = mkrs(out, 2048 * 1280);
#pragma unroll
  for (int i = 0; i < 3; ++i) {
    const u32 id = kq + 4 * i;
    if (id < 10) {
      const u32 a = (th * 10 + id) * 1024 + lane * 16;
      f32x4 v = *LDSP(f32x4, a) + *LDSP(f32x4, a + 20480) + *LDSP(f32x4, a + 40960) + *LDSP(f32x4, a + 61440);
      u32x2 r = {pk(v[0] + blo(xr[i][0]), v[1] + bhi(xr[i][0])), pk(v[2] + blo(xr[i][1]), v[3] + bhi(xr[i][1]))};
      __builtin_amdgcn_raw_buffer_store_b64(r, rsOut, (16 * (tb0 + (id & 1)) + c16) * 1280 + 160 * c80 + (id >> 1) * 32 + g * 8, 0, 0);
    }
  }
  STAMP(8);
}

__global__ void tstamp(char* ws) { u64 t = __builtin_amdgcn_s_memrealtime(); if (threadIdx.x == 0) __hip_atomic_store((u64*)(ws + WS_DBG) + blockIdx.x, t, __ATOMIC_RELAXED, __HIP_MEMORY_SCOPE_AGENT); }
__global__ void tspin(char* ws) { u64 t = __hip_atomic_load((u64*)(ws + WS_DBG) + blockIdx.x, __ATOMIC_RELAXED, __HIP_MEMORY_SCOPE_AGENT); for (int i = 0; i < 100000 && __builtin_amdgcn_s_memrealtime() - t < 8000ull; ++i) __builtin_amdgcn_s_sleep(1); }
extern "C" void kernel_launch(void* const* d_in, const int* in_sizes, int n_in, void* d_out, int out_size, void* d_ws, size_t ws_size, hipStream_t stream) {
  tstamp<<<256, 64, 0, stream>>>((char*)d_ws);
  fused_block<<<256, 512, 0, stream>>>((const u16*)d_in[0], (const u16*)d_in[1], (const u16*)d_in[2], (const u16*)d_in[3], (const u16*)d_in[4],
                                       (const u16*)d_in[6], (const u16*)d_in[7], (const u16*)d_in[8], (u16*)d_out, (char*)d_ws);
  tspin<<<256, 64, 0, stream>>>((char*)d_ws);
}
```

```cpp
#include <hip/hip_runtime.h>
typedef unsigned u32; typedef unsigned long long u64; typedef unsigned short u16;
typedef __bf16 bf16x2 __attribute__((ext_vector_type(2)));
typedef __bf16 bf16x4 __attribute__((ext_vector_type(4)));
typedef __bf16 bf16x8 __attribute__((ext_vector_type(8)));
typedef __bf16 bf16x16 __attribute__((ext_vector_type(16)));
typedef short s16x4 __attribute__((ext_vector_type(4)));
typedef float f32x2 __attribute__((ext_vector_type(2)));
typedef float f32x4 __attribute__((ext_vector_type(4)));
typedef float f32x8 __attribute__((ext_vector_type(8)));
typedef float f32x16 __attribute__((ext_vector_type(16)));
typedef u32 u32x2 __attribute__((ext_vector_type(2)));
typedef u32 u32x4 __attribute__((ext_vector_type(4)));
typedef __amdgpu_buffer_rsrc_t rsrc_t;
#define DI __device__ __forceinline__
#define LDSP(T, a) ((__attribute__((address_space(3))) T*)(unsigned long)(a))
#define BC(T, x) __builtin_bit_cast(T, x)

constexpr u32 WS_EPOCH = 0, WS_KVF = 1024, WS_HF = 2048, WS_OF = 4096, WS_STAT = 8192, WS_KV = 32768,
              WS_OT = 262144  , WS_OM = 1u << 20  , WS_H = 4u << 20  , WS_DBG = 12u << 20, WS_BYTES = 16u << 20;
constexpr u32 L_G = 16384, L_U = 16384 + 12288 + 32, L_Q = 41088, STG = 49280;
constexpr u32 L_MISC = 3 * STG;
constexpr u32 L_QI = L_MISC, L_POLL = 160256, L_STAT = 161280;
constexpr u32 L_WK = STG + L_Q, L_WV = L_WK + 25600, L_XN = L_WV + 25600, L_KVS = L_XN + 20480, L_ST0 = L_KVS + 1536;
constexpr u32 L_KV = STG, KVCH = 24576;
constexpr u32 WSLOT = 20480;
constexpr u32 OOB = 0x40000000u;
constexpr int SPIN_MAX = 1 << 16;
static_assert(L_ST0 + 256 <= 163840 && L_STAT + 1024 <= L_KVS + 1024, "lds");

DI float asf(u32 x) { return BC(float, x); }
DI u32 asu(float x) { return BC(u32, x); }
DI rsrc_t mkrs(const void* p, u32 n) { return __builtin_amdgcn_make_buffer_rsrc((void*)p, (short)0, (int)n, 0x00020000); }
template <int N> DI void waitv() { asm volatile("s_waitcnt vmcnt(%0)" ::"n"(N) : "memory"); }
template <int N> DI void waitbar() { asm volatile("s_waitcnt vmcnt(%0) lgkmcnt(0)\n\ts_barrier" ::"n"(N) : "memory"); }
DI void bar() { asm volatile("s_waitcnt lgkmcnt(0)\n\ts_barrier" ::: "memory"); }
DI void dma1(u32 m0, u32 v0, rsrc_t rs, u32 so) {
  asm volatile("s_mov_b32 m0, %0\n\ts_nop 0\n\tbuffer_load_dwordx4 %1, %2, %3 offen lds" ::"s"(m0), "v"(v0), "s"(rs), "s"(so) : "memory");
}
DI void dma1c(u32 m0, u32 v0, rsrc_t rs, u32 so) {
  asm volatile("s_mov_b32 m0, %0\n\ts_nop 0\n\tbuffer_load_dwordx4 %1, %2, %3 offen sc1 lds" ::"s"(m0), "v"(v0), "s"(rs), "s"(so) : "memory");
}
DI void dma2(u32 m0, u32 v0, u32 v1, rsrc_t rs, u32 so) {
  asm volatile("s_mov_b32 m0, %0\n\ts_nop 0\n\tbuffer_load_dwordx4 %1, %3, %4 offen lds\n\tbuffer_load_dwordx4 %2, %3, %4 offen offset:1024 lds"
               ::"s"(m0), "v"(v0), "v"(v1), "s"(rs), "s"(so) : "memory");
}
DI void dma3(u32 m0, u32 v0, u32 v1, u32 v2, rsrc_t rs, u32 so) {
  asm volatile("s_mov_b32 m0, %0\n\ts_nop 0\n\tbuffer_load_dwordx4 %1, %4, %5 offen lds\n\tbuffer_load_dwordx4 %2, %4, %5 offen offset:1024 lds\n\t"
               "buffer_load_dwordx4 %3, %4, %5 offen offset:2048 lds" ::"s"(m0), "v"(v0), "v"(v1), "v"(v2), "s"(rs), "s"(so) : "memory");
}
DI void dma3c(u32 m0, u32 v0, rsrc_t rs, u32 so) {
  asm volatile("s_mov_b32 m0, %0\n\ts_nop 0\n\tbuffer_load_dwordx4 %1, %2, %3 offen sc1 lds\n\tbuffer_load_dwordx4 %1, %2, %3 offen offset:1024 sc1 lds\n\t"
               "buffer_load_dwordx4 %1, %2, %3 offen offset:2048 sc1 lds" ::"s"(m0), "v"(v0), "s"(rs), "s"(so) : "memory");
}
DI void st16c(u32x4 v, rsrc_t rs, u32 off) { __builtin_amdgcn_raw_buffer_store_b128(v, rs, off, 0, 16); }
DI void st8c(u32x2 v, rsrc_t rs, u32 off) { __builtin_amdgcn_raw_buffer_store_b64(v, rs, off, 0, 16); }
DI u32x4 ld16c(rsrc_t rs, u32 off) { return __builtin_amdgcn_raw_buffer_load_b128(rs, off, 0, 16); }
DI u32x2 ld8c(rsrc_t rs, u32 off) { return __builtin_amdgcn_raw_buffer_load_b64(rs, off, 0, 16); }
DI u32 pk(float lo, float hi) { f32x2 v = {lo, hi}; return BC(u32, __builtin_convertvector(v, bf16x2)); }
DI float blo(u32 v) { return BC(float, v << 16); }
DI float bhi(u32 v) { return BC(float, v & 0xffff0000u); }
DI float dot2(u32 a, u32 b, float c) { return __builtin_amdgcn_fdot2_f32_bf16(BC(bf16x2, a), BC(bf16x2, b), c, false); }
DI bf16x4 trr(u32 a) { return __builtin_amdgcn_ds_read_tr16_b64_v4bf16(LDSP(bf16x4, a)); }
DI bf16x8 tr2(u32 a, u32 b) { bf16x4 x = trr(a), y = trr(b); return __builtin_shufflevector(x, y, 0, 1, 2, 3, 4, 5, 6, 7); }
DI bf16x8 l128(u32 a) { return *LDSP(bf16x8, a); }
template <int C> DI float dpp(float x) { return BC(float, __builtin_amdgcn_update_dpp(0, BC(int, x), C, 0xf, 0xf, true)); }
DI float sw16(float x) { auto r = __builtin_amdgcn_permlane16_swap(BC(u32, x), BC(u32, x), false, false); return asf(r[0]) + asf(r[1]); }
DI float sw32(float x) { auto r = __builtin_amdgcn_permlane32_swap(BC(u32, x), BC(u32, x), false, false); return asf(r[0]) + asf(r[1]); }
DI float mx16(float x) { auto r = __builtin_amdgcn_permlane16_swap(BC(u32, x), BC(u32, x), false, false); return fmaxf(asf(r[0]), asf(r[1])); }
DI float mx32(float x) { auto r = __builtin_amdgcn_permlane32_swap(BC(u32, x), BC(u32, x), false, false); return fmaxf(asf(r[0]), asf(r[1])); }
DI float sum16(float x) { x += dpp<0xB1>(x); x += dpp<0x4E>(x); x += dpp<0x141>(x); x += dpp<0x140>(x); return x; }
DI float max16(float x) { x = fmaxf(x, dpp<0xB1>(x)); x = fmaxf(x, dpp<0x4E>(x)); x = fmaxf(x, dpp<0x141>(x)); x = fmaxf(x, dpp<0x140>(x)); return x; }
DI u32 vunit(u32 d) { return d < 4 ? d : d < 12 ? d + 5 : d < 16 ? d - 8 : d < 20 ? d + 1 : 8; }
#define M32(a, b, c) __builtin_amdgcn_mfma_f32_32x32x16_bf16(a, b, c, 0, 0, 0)
#define M16(a, b, c) __builtin_amdgcn_mfma_f32_16x16x32_bf16(a, b, c, 0, 0, 0)

#define STAMP(i)

template <int PITCH>
DI void p1_stage(u32 wa0, u32 wa1, u32 xa, f32x16 (&acc)[2][2], float (&cs)[2][2]) {
#pragma unroll
  for (int s = 0; s < 4; ++s) {
    bf16x8 a0 = tr2(wa0 + s * 16 * PITCH, wa0 + s * 16 * PITCH + 4 * PITCH);
    bf16x8 a1 = tr2(wa1 + s * 16 * PITCH, wa1 + s * 16 * PITCH + 4 * PITCH);
    u32 xs = xa ^ (s << 5);
    bf16x8 b0 = l128(xs), b1 = l128(xs + 4096);
    acc[0][0] = M32(a0, b0, acc[0][0]); acc[0][1] = M32(a0, b1, acc[0][1]);
    acc[1][0] = M32(a1, b0, acc[1][0]); acc[1][1] = M32(a1, b1, acc[1][1]);
    u32x4 u0 = BC(u32x4, a0), u1 = BC(u32x4, a1);
#pragma unroll
    for (int j = 0; j < 4; ++j) { cs[0][j & 1] = dot2(u0[j], 0x3f803f80u, cs[0][j & 1]); cs[1][j & 1] = dot2(u1[j], 0x3f803f80u, cs[1][j & 1]); }
  }
}

__global__ __launch_bounds__(512, 2) void fused_block(const u16* __restrict__ x, const u16* __restrict__ wq, const u16* __restrict__ wk,
                                                      const u16* __restrict__ wv, const u16* __restrict__ wo, const u16* __restrict__ wg,
                                                      const u16* __restrict__ wu, const u16* __restrict__ wd, u16* __restrict__ out, char* ws) {
  __shared__ __attribute__((aligned(128))) char smem[163840];
  asm volatile("" ::"v"(smem) : "memory");
  const u32 lane = threadIdx.x & 63, w = __builtin_amdgcn_readfirstlane(threadIdx.x >> 6);
  const u32 b = blockIdx.x, xcd = b & 7, bi = b >> 3;
  const u32 rb = (xcd >> 2) * 8 + (bi >> 2), cb = (xcd & 3) * 4 + (bi & 3);
  const bool prod = ((cb ^ (cb >> 2)) & 1) == 0;
  const rsrc_t rsX = mkrs(x, 2048 * 1280), rsW = mkrs(ws, WS_BYTES), rsQ = mkrs(wq, 640 * 1280);
  const rsrc_t rsGU = mkrs(w < 4 ? wg : wu, 640 * 3072);
  { u64 t = __builtin_amdgcn_s_memrealtime(); if (threadIdx.x == 0) __hip_atomic_store((u64*)(ws + WS_DBG) + blockIdx.x, t, __ATOMIC_RELAXED, __HIP_MEMORY_SCOPE_AGENT); }
  u32 ep;
  asm volatile("buffer_load_dword %0, off, %1, 0 sc1" : "=v"(ep) : "s"(rsW) : "memory");

  u32 vx0, vx1, vg0, vg1, vg2, vq;
  {
    u32 t0 = 16 * w + (lane >> 3), t1 = t0 + 8, sl = lane & 7;
    vx0 = (128 * rb + t0) * 1280 + ((sl ^ ((t0 >> 1) & 7)) << 4);
    vx1 = (128 * rb + t1) * 1280 + ((sl ^ ((t1 >> 1) & 7)) << 4) - 1024;
    u32 u = (w & 3) * 192 + lane;
    vg0 = (u / 12) * 3072 + (u % 12) * 16 + 192 * cb; u += 64;
    vg1 = (u / 12) * 3072 + (u % 12) * 16 + 192 * cb - 1024; u += 64;
    vg2 = (u / 12) * 3072 + (u % 12) * 16 + 192 * cb - 2048;
    u32 r = 8 * w + (lane >> 3), un = sl ^ ((r & 2) << 1);
    vq = un < 5 ? r * 1280 + 80 * cb + un * 16 : OOB;
  }
  const u32 mx = 2048 * w, mg = (w < 4 ? L_G : L_U) + (w & 3) * 3072, mq = L_Q + 1024 * w;
  auto stage_xg = [&](u32 base, u32 t) { dma2(base + mx, vx0, vx1, rsX, t * 128); dma3(base + mg, vg0, vg1, vg2, rsGU, t * (64 * 3072)); };
  auto stage_q = [&](u32 base, u32 t) { dma1(base + mq, vq, rsQ, t * (64 * 1280)); };

  float kmax_own = 0.f;
  const u32 pid = 8 * rb + (cb >> 1);
  if (prod) {
    u32x2 xv[5];
    const u32 tk = 2 * w + (lane >> 5), xo = (16 * pid + tk) * 1280 + (lane & 31) * 8;
    asm volatile("buffer_load_dwordx2 %0, %5, %6, 0 offen\n\tbuffer_load_dwordx2 %1, %5, %6, 0 offen offset:256\n\tbuffer_load_dwordx2 %2, %5, %6, 0 offen offset:512\n\t"
                 "buffer_load_dwordx2 %3, %5, %6, 0 offen offset:768\n\tbuffer_load_dwordx2 %4, %5, %6, 0 offen offset:1024"
                 : "=&v"(xv[0]), "=&v"(xv[1]), "=&v"(xv[2]), "=&v"(xv[3]), "=&v"(xv[4]) : "v"(xo), "s"(rsX) : "memory");
    const rsrc_t rsKV = mkrs(w < 4 ? wk : wv, 25600);
    for (u32 p = w & 3; p < 25; p += 4) dma1((w < 4 ? L_WK : L_WV) + p * 1024, lane * 16, rsKV, p * 1024);
    stage_xg(0, 0); stage_q(0, 0); stage_xg(STG, 1);
    asm volatile("s_waitcnt vmcnt(17)" : "+v"(xv[0]), "+v"(xv[1]), "+v"(xv[2]), "+v"(xv[3]), "+v"(xv[4]), "+v"(ep)::"memory");
    float s1 = 0.f;
#pragma unroll
    for (int i = 0; i < 5; ++i) { s1 = dot2(xv[i][0], 0x3f803f80u, s1); s1 = dot2(xv[i][1], 0x3f803f80u, s1); }
    const float mu = sw16(sum16(s1)) * (1.f / 640.f);
    float f[20], s2 = 0.f;
#pragma unroll
    for (int i = 0; i < 5; ++i) { f[4 * i] = blo(xv[i][0]) - mu; f[4 * i + 1] = bhi(xv[i][0]) - mu; f[4 * i + 2] = blo(xv[i][1]) - mu; f[4 * i + 3] = bhi(xv[i][1]) - mu; }
#pragma unroll
    for (int i = 0; i < 20; ++i) s2 += f[i] * f[i];
    const float rs = rsqrtf(sw16(sum16(s2)) * (1.f / 640.f) + 1e-5f);
#pragma unroll
    for (int i = 0; i < 5; ++i) {
      u32 un = 16 * i + (((lane & 31) >> 1) ^ tk);
      *LDSP(u32x2, L_XN + tk * 1280 + un * 16 + (lane & 1) * 8) = u32x2{pk(f[4 * i] * rs, f[4 * i + 1] * rs), pk(f[4 * i + 2] * rs, f[4 * i + 3] * rs)};
    }
    if ((lane & 31) == 0) *LDSP(f32x2, L_ST0 + tk * 8) = f32x2{-mu, rs};
    waitbar<11>();
    if (w < 4) {
      const u32 g = lane >> 4, c = lane & 15;
      const u32 wa = (w < 2 ? L_WK : L_WV) + (8 * g + (c >> 2)) * 40 + ((w & 1) ? 32 : (c & 3) * 8);
      const u32 xa = L_XN + c * 1280;
      f32x4 a = {0.f, 0.f, 0.f, 0.f};
#pragma unroll
      for (int s = 0; s < 20; ++s) {
        bf16x8 wf = tr2(wa + s * 1280, wa + s * 1280 + 160);
        bf16x8 xf = l128(xa + (s >> 2) * 256 + ((((s & 3) * 4 + g) ^ c) << 4));
        a = w < 2 ? M16(wf, xf, a) : M16(xf, wf, a);
      }
      u32x2 o = {pk(a[0], a[1]), pk(a[2], a[3])};
      if (w < 2) {
        const u32 blk = c >> 3, key = c & 7;
        float n2 = dot2(o[0], o[0], dot2(o[1], o[1], 0.f));
        if (w == 0) {
          *LDSP(u32x2, L_KVS + blk * 768 + ((((g >> 1) ^ blk) * 8 + key) << 4) + (g & 1) * 8) = o;
          n2 = sw32(sw16(n2));
        } else if (g == 0) {
          *LDSP(u32x2, L_KVS + blk * 768 + 592 + blk * 64 + key * 8) = (pid & 1) ? u32x2{o[1], o[0]} : o;
        }
        if (g == 0) *LDSP(float, L_ST0 + 128 + w * 64 + c * 4) = n2;
      } else {
        const u32 d = w == 2 ? c : 16 + c;
        if (d == 20) o = u32x2{0x3f803f80u, 0x3f803f80u};
        if (d <= 20) *LDSP(u32x2, L_KVS + (g >> 1) * 768 + 256 + vunit(d) * 16 + (g & 1) * 8) = o;
      }
    }
    bar();
    if (w == 7) {
      u32x4 v0 = *LDSP(u32x4, L_KVS + lane * 16), v1 = *LDSP(u32x4, L_KVS + 1024 + (lane < 40 ? lane : 39) * 16);
      kmax_own = max16(*LDSP(float, L_ST0 + 128 + (lane & 15) * 4) + *LDSP(float, L_ST0 + 192 + (lane & 15) * 4));
      st16c(v0, rsW, WS_KV + pid * 1536 + lane * 16);
      st16c(v1, rsW, lane < 32 ? WS_KV + pid * 1536 + 1024 + lane * 16 : lane < 40 ? WS_STAT + pid * 128 + (lane - 32) * 16 : OOB);
    }
    stage_q(STG, 1);
  } else {
    stage_xg(0, 0); stage_q(0, 0); stage_xg(STG, 1); stage_q(STG, 1);
  }
  stage_xg(2 * STG, 2); stage_q(2 * STG, 2);
  STAMP(1);

  const u32 mi = w >> 1, nj = w & 1, hh = lane >> 5, c32 = lane & 31;
  f32x16 acc[2][2];
#pragma unroll
  for (int i = 0; i < 16; ++i) { acc[0][0][i] = 0.f; acc[0][1][i] = 0.f; acc[1][0][i] = 0.f; acc[1][1][i] = 0.f; }
  float cs[2][2] = {{0.f, 0.f}, {0.f, 0.f}};
  u32 wa0, wa1, xa;
  {
    const u32 q = (lane >> 2) & 3, P = (lane & 3) + ((lane >> 2) & 4), row = 8 * hh + q;
    if (mi < 3) {
      const u32 quad = (P & 1) ? 3 - (P >> 2) : (P >> 2);
      wa0 = ((P & 2) ? L_U : L_G) + row * 192 + mi * 64 + quad * 8; wa1 = wa0 + 32;
    } else {
      wa0 = L_Q + row * 128 + ((8 * P) ^ ((q & 2) << 5)); wa1 = L_Q + row * 128 + ((40 + 8 * P) ^ ((q & 2) << 5));
    }
    const u32 tk = 64 * nj + c32;
    xa = tk * 128 + ((hh ^ ((tk >> 1) & 7)) << 4);
  }
  u32 tag = 0;
  float kmax2 = 0.f;
  u32 sb = 0;
  for (u32 t = 0; t < 10; ++t) {
    if (t == 0) {
      asm volatile("s_waitcnt vmcnt(12) lgkmcnt(0)\n\ts_barrier" : "+v"(ep)::"memory");
      tag = __builtin_amdgcn_readfirstlane(ep) + 1;
    } else {
      waitbar<6>();
      const u32 nb = sb == 0 ? 2 * STG : sb - STG;
      if (t == 1 && prod && w == 7 && lane == 0) st8c(u32x2{BC(u32, kmax_own), tag}, rsW, WS_KVF + pid * 8);
      if (t == 7 && w == 0) dma1c(L_POLL, lane * 16, rsW, WS_KVF);
      if (t < 8) { stage_xg(nb, t + 2); stage_q(nb, t + 2); }
      else if (t == 8) {
        u32x4 fl = *LDSP(u32x4, L_POLL + lane * 16);
        for (int sp = 0; sp < SPIN_MAX && !__all(fl[1] == tag && fl[3] == tag); ++sp) { __builtin_amdgcn_s_sleep(2); fl = ld16c(rsW, WS_KVF + lane * 16); }
        kmax2 = mx32(mx16(max16(fmaxf(asf(fl[0]), asf(fl[2])))));
        if (w == 0) { dma1c(L_STAT, lane * 16, rsW, WS_STAT + rb * 1024); }
        dma3c(L_KV + w * 3072, lane * 16, rsW, WS_KV + w * 3072);
        dma3c(L_KV + KVCH + w * 3072, lane * 16, rsW, WS_KV + KVCH + w * 3072);
      }
    }
    if (mi < 3) p1_stage<192>(wa0 + sb, wa1 + sb, xa + sb, acc, cs); else p1_stage<128>(wa0 + sb, wa1 + sb, xa + sb, acc, cs);
    sb = sb == 2 * STG ? 0 : sb + STG;
  }
  STAMP(2);
  {
    f32x2 st[2];
#pragma unroll
    for (int n = 0; n < 2; ++n) st[n] = *LDSP(f32x2, L_STAT + (64 * nj + 32 * n + c32) * 8);
#pragma unroll
    for (int m = 0; m < 2; ++m)
#pragma unroll
      for (int n = 0; n < 2; ++n) acc[m][n] = __builtin_amdgcn_mfma_f32_32x32x2f32(cs[m][0] + cs[m][1], st[n][0], acc[m][n], 0, 0, 0);
    if (mi < 3) {
#pragma unroll
      for (int m = 0; m < 2; ++m)
#pragma unroll
        for (int n = 0; n < 2; ++n) {
          const float r2 = st[n][1] * st[n][1];
          const f32x16 a = acc[m][n];
          u32x4 o = {pk(a[0] * a[4] * r2, a[1] * a[5] * r2), pk(a[2] * a[6] * r2, a[3] * a[7] * r2),
                     pk(a[8] * a[12] * r2, a[9] * a[13] * r2), pk(a[10] * a[14] * r2, a[11] * a[15] * r2)};
          const u32 tb = 8 * rb + 4 * nj + 2 * n + (c32 >> 4);
          st16c(o, rsW, WS_H + (tb * 48 + 3 * cb + mi) * 1024 + (((2 * m + hh) * 16 + (c32 & 15)) << 4));
        }
    } else {
#pragma unroll
      for (int m = 0; m < 2; ++m)
#pragma unroll
        for (int n = 0; n < 2; ++n) {
          const float r = st[n][1] * (0.22360679774997896f * 1.4426950408889634f);
          const f32x16 a = acc[m][n];
          const u32 qa = L_QI + (m * 128 + 64 * nj + 32 * n + c32) * 48 + hh * 8;
          *LDSP(u32x2, qa) = u32x2{pk(a[0] * r, a[1] * r), pk(a[2] * r, a[3] * r)};
          *LDSP(u32x2, qa + 16) = u32x2{pk(a[4] * r, a[5] * r), pk(a[6] * r, a[7] * r)};
          *LDSP(u32x2, qa + 32) = u32x2{pk(a[8] * r, a[9] * r), pk(a[10] * r, a[11] * r)};
        }
    }
  }
  if (mi < 3) waitbar<7>(); else waitbar<3>();
  STAMP(3);

  const u32 ha = w & 1, tt = w >> 1;
  const u32 r64 = 4 * xcd + (bi >> 3), c80 = bi & 7, th = w & 1, kq = w >> 1;
  const rsrc_t rsD = mkrs(wd, 1536 * 1280), rsO = mkrs(wo, 640 * 1280);
  u32 vw[5];
#pragma unroll
  for (int p = 0; p < 5; ++p) { u32 u = 64 * p + lane; vw[p] = (u / 10) * 1280 + (u % 10) * 16 + 160 * c80 - (p < 3 ? p : p - 3) * 1024; }
  auto wstep = [&](u32 j) {
    const u32 m = ((5 + j) & 7) * WSLOT + kq * 5120;
    if (j < 12) { const u32 so = (384 * kq + 32 * j) * 1280; dma3(m, vw[0], vw[1], vw[2], rsD, so); dma2(m + 3072, vw[3], vw[4], rsD, so); }
    else { const u32 so = (160 * kq + 32 * (j - 12)) * 1280; dma3(m, vw[0], vw[1], vw[2], rsO, so); dma2(m + 3072, vw[3], vw[4], rsO, so); }
  };
  bf16x8 q0; u32x2 q1;
  bool fast;
  {
    const u32 qa = L_QI + (ha * 128 + 32 * tt + c32) * 48;
    q0 = l128(qa + 16 * hh);
    q1[0] = *LDSP(u32, qa + 32 + 4 * hh);
    const u32x4 u = BC(u32x4, q0);
    const float n2 = sw32(dot2(u[0], u[0], dot2(u[1], u[1], dot2(u[2], u[2], dot2(u[3], u[3], dot2(q1[0], q1[0], 0.f))))));
    const float bound = __builtin_sqrtf(n2 * kmax2) * 1.01f + 0.01f;
    fast = __all(bound <= 50.f);
    q1[1] = (fast && hh == 0) ? (pk(-bound, 0.f) & 0xffffu) : 0u;
  }
  f32x16 o;
#pragma unroll
  for (int i = 0; i < 16; ++i) o[i] = 0.f;
  float mrun = -3.0e38f;
  const u32 blk = c32 >> 3;
  const u32 ka0 = L_KV + blk * 768 + (((hh ^ (blk & 1)) * 8 + (c32 & 7)) << 4);
  const u32 ka1 = L_KV + blk * 768 + 592 + (blk & 1) * 64 + (c32 & 7) * 8 + ((hh ^ (blk >> 1)) << 2);
  const u32 va = L_KV + hh * 768 + 256 + vunit(c32) * 16;
  u32x4 hf[24];
  for (u32 c = 0; c < 8; ++c) {
    const u32 cbuf = (c & 1) * KVCH;
    if (fast) {
#pragma unroll 2
      for (int i = 0; i < 8; ++i) {
        const u32 off = cbuf + i * 3072;
        bf16x8 k0 = l128(ka0 + off), v0 = l128(va + off), v1 = l128(va + off + 1536);
        u32x2 k1 = {*LDSP(u32, ka1 + off), 0x00003f80u};
        f32x16 s;
#pragma unroll
        for (int j = 0; j < 16; ++j) s[j] = 0.f;
        s = __builtin_amdgcn_mfma_f32_32x32x8bf16_1k(BC(s16x4, k1), BC(s16x4, q1), s, 0, 0, 0);
        s = M32(k0, q0, s);
        f32x8 p0, p1;
#pragma unroll
        for (int j = 0; j < 8; ++j) { p0[j] = __builtin_amdgcn_exp2f(s[j]); p1[j] = __builtin_amdgcn_exp2f(s[8 + j]); }
        o = __builtin_amdgcn_smfmac_f32_32x32x32_bf16(v0, BC(bf16x16, p0), o, 0xDDDD, 0, 0);
        o = __builtin_amdgcn_smfmac_f32_32x32x32_bf16(v1, BC(bf16x16, p1), o, 0xDDDD, 0, 0);
      }
    } else {
#pragma unroll 1
      for (int i = 0; i < 8; ++i) {
        const u32 off = cbuf + i * 3072;
        bf16x8 k0 = l128(ka0 + off), v0 = l128(va + off), v1 = l128(va + off + 1536);
        u32x2 k1 = {*LDSP(u32, ka1 + off), 0x00003f80u};
        f32x16 s;
#pragma unroll
        for (int j = 0; j < 16; ++j) s[j] = 0.f;
        s = __builtin_amdgcn_mfma_f32_32x32x8bf16_1k(BC(s16x4, k1), BC(s16x4, q1), s, 0, 0, 0);
        s = M32(k0, q0, s);
        float m = mrun;
#pragma unroll
        for (int j = 0; j < 16; ++j) m = fmaxf(m, s[j]);
        m = mx32(m);
        const float al = __builtin_amdgcn_exp2f(mrun - m);
        mrun = m;
        f32x8 p0, p1;
#pragma unroll
        for (int j = 0; j < 8; ++j) { p0[j] = __builtin_amdgcn_exp2f(s[j] - m); p1[j] = __builtin_amdgcn_exp2f(s[8 + j] - m); }
#pragma unroll
        for (int j = 0; j < 16; ++j) o[j] *= al;
        o = __builtin_amdgcn_smfmac_f32_32x32x32_bf16(v0, BC(bf16x16, p0), o, 0xDDDD, 0, 0);
        o = __builtin_amdgcn_smfmac_f32_32x32x32_bf16(v1, BC(bf16x16, p1), o, 0xDDDD, 0, 0);
      }
    }
    waitbar<0>();
    if (c < 6) dma3c(L_KV + cbuf + w * 3072, lane * 16, rsW, WS_KV + (c + 2) * KVCH + w * 3072);
    if (c == 0 && w == 0 && lane == 0) st8c(u32x2{tag, tag}, rsW, WS_HF + (rb * 16 + cb) * 8);
    if (c < 3 && 2 * c + th < 5) wstep(2 * c + th);
  }
  STAMP(4);
  {
    const float il = __builtin_amdgcn_rcpf(o[12]);
    const u32 tb = 8 * rb + 2 * tt + (c32 >> 4);
    st16c(u32x4{pk(o[0] * il, o[1] * il), pk(o[2] * il, o[3] * il), pk(o[4] * il, o[5] * il), pk(o[6] * il, o[7] * il)}, rsW,
          WS_OM + (tb * 16 + cb) * 1024 + (((2 * ha + hh) * 16 + (c32 & 15)) << 4));
    st8c(u32x2{pk(o[8] * il, o[9] * il), pk(o[10] * il, o[11] * il)}, rsW, hh == 0 ? WS_OT + (tb * 32 + 2 * cb + ha) * 128 + (c32 & 15) * 8 : OOB);
  }
  wstep(5 + th);
  waitbar<5>();
  if (w == 0 && lane == 0) st8c(u32x2{tag, tag}, rsW, WS_OF + (rb * 16 + cb) * 8);
  STAMP(5);

  const u32 g = lane >> 4, c16 = lane & 15;
  {
    u32x2 fl = ld8c(rsW, WS_HF + (r64 >> 1) * 128 + (4 * kq + (lane & 3)) * 8);
    for (int sp = 0; sp < SPIN_MAX && !__all(fl[0] == tag && fl[1] == tag); ++sp) { __builtin_amdgcn_s_sleep(2); fl = ld8c(rsW, WS_HF + (r64 >> 1) * 128 + (4 * kq + (lane & 3)) * 8); }
  }
  if (b == 0 && w == 0) {
    for (int sp = 0; sp < SPIN_MAX; ++sp) {
      u32x4 f0 = ld16c(rsW, WS_HF + lane * 16), f1 = ld16c(rsW, WS_HF + 1024 + lane * 16);
      if (__all(f0[0] == tag && f0[2] == tag && f1[0] == tag && f1[2] == tag)) break;
      __builtin_amdgcn_s_sleep(2);
    }
    if (lane == 0) __builtin_amdgcn_raw_buffer_store_b32(tag, rsW, WS_EPOCH, 0, 16);
  }
  const u32 tb0 = 4 * r64 + 2 * th;
#pragma unroll
  for (int j = 0; j < 12; ++j) { hf[2 * j] = ld16c(rsW, WS_H + (tb0 * 48 + 12 * kq + j) * 1024 + lane * 16); hf[2 * j + 1] = ld16c(rsW, WS_H + ((tb0 + 1) * 48 + 12 * kq + j) * 1024 + lane * 16); }
  f32x4 oc[5][2];
#pragma unroll
  for (int i = 0; i < 5; ++i) { oc[i][0] = f32x4{0.f, 0.f, 0.f, 0.f}; oc[i][1] = oc[i][0]; }
  const u32 wl = kq * 5120 + (c16 >> 2) * 160 + (c16 & 3) * 8;
  const u32 wb1 = wl + (8 * g + 4 * (g & 1)) * 160, wb2 = wl + (8 * g + 4 - 4 * (g & 1)) * 160;
  auto bstep = [&](int j) {
    const u32 so = ((5 + j) & 7) * WSLOT;
#pragma unroll
    for (int i = 0; i < 5; ++i) {
      bf16x8 a = tr2(wb1 + so + 32 * i, wb2 + so + 32 * i);
      oc[i][0] = M16(a, BC(bf16x8, hf[2 * j]), oc[i][0]); oc[i][1] = M16(a, BC(bf16x8, hf[2 * j + 1]), oc[i][1]);
    }
  };
  bstep(0);
  bar(); wstep(7 + th); bstep(1); bstep(2);
  bar(); wstep(9 + th); bstep(3); bstep(4);
  waitbar<10>(); wstep(11 + th); bstep(5); bstep(6);
  waitbar<10>(); wstep(13 + th); bstep(7); bstep(8);
  waitbar<10>(); wstep(15 + th); bstep(9); bstep(10);
  waitbar<10>(); bstep(11);
  STAMP(6);
  {
    u32x2 fl = ld8c(rsW, WS_OF + (r64 >> 1) * 128 + (4 * kq + (lane & 3)) * 8);
    for (int sp = 0; sp < SPIN_MAX && !__all(fl[0] == tag && fl[1] == tag); ++sp) { __builtin_amdgcn_s_sleep(1); fl = ld8c(rsW, WS_OF + (r64 >> 1) * 128 + (4 * kq + (lane & 3)) * 8); }
  }
  u32x4 of[10];
#pragma unroll
  for (int n = 0; n < 2; ++n) {
#pragma unroll
    for (int s = 0; s < 4; ++s) of[2 * s + n] = ld16c(rsW, WS_OM + ((tb0 + n) * 16 + 4 * kq + s) * 1024 + lane * 16);
    u32x2 t0 = ld8c(rsW, WS_OT + ((tb0 + n) * 32 + 8 * kq + 2 * g) * 128 + c16 * 8), t1 = ld8c(rsW, WS_OT + ((tb0 + n) * 32 + 8 * kq + 2 * g + 1) * 128 + c16 * 8);
    of[8 + n] = u32x4{t0[0], t0[1], t1[0], t1[1]};
  }
  u32x2 xr[3];
#pragma unroll
  for (int i = 0; i < 3; ++i) { const u32 id = kq + 4 * i; xr[i] = __builtin_amdgcn_raw_buffer_load_b64(rsX, id < 10 ? (16 * (tb0 + (id & 1)) + c16) * 1280 + 160 * c80 + (id >> 1) * 32 + g * 8 : OOB, 0, 0); }
  waitbar<0>();
  STAMP(7);
  {
    auto radr = [&](u32 r) { return (1 + (r >> 5)) * WSLOT + (r & 31) * 160 + wl; };
#pragma unroll
    for (int s = 0; s < 5; ++s) {
      const u32 r1 = s < 4 ? 40 * s + 20 * (g >> 1) + 4 * (g & 1) : 40 * g + 16, r2 = s < 4 ? r1 + 8 : r1 + 20;
      const u32 a1 = radr(r1), a2 = radr(r2);
#pragma unroll
      for (int i = 0; i < 5; ++i) {
        bf16x8 a = tr2(a1 + 32 * i, a2 + 32 * i);
        oc[i][0] = M16(a, BC(bf16x8, of[2 * s]), oc[i][0]); oc[i][1] = M16(a, BC(bf16x8, of[2 * s + 1]), oc[i][1]);
      }
    }
  }
  bar();
#pragma unroll
  for (int i = 0; i < 5; ++i)
#pragma unroll
    for (int n = 0; n < 2; ++n) *LDSP(f32x4, (w * 10 + 2 * i + n) * 1024 + lane * 16) = oc[i][n];
  bar();
  const rsrc_t rsOut = mkrs(out, 2048 * 1280);
#pragma unroll
  for (int i = 0; i < 3; ++i) {
    const u32 id = kq + 4 * i;
    if (id < 10) {
      const u32 a = (th * 10 + id) * 1024 + lane * 16;
      f32x4 v = *LDSP(f32x4, a) + *LDSP(f32x4, a + 20480) + *LDSP(f32x4, a + 40960) + *LDSP(f32x4, a + 61440);
      u32x2 r = {pk(v[0] + blo(xr[i][0]), v[1] + bhi(xr[i][0])), pk(v[2] + blo(xr[i][1]), v[3] + bhi(xr[i][1]))};
      __builtin_amdgcn_raw_buffer_store_b64(r, rsOut, (16 * (tb0 + (id & 1)) + c16) * 1280 + 160 * c80 + (id >> 1) * 32 + g * 8, 0, 0);
    }
  }
  { u64 t = ~0ull, tm = 0; for (int i = 0; i < 256; ++i) { u64 v = __hip_atomic_load((u64*)(ws + WS_DBG) + i, __ATOMIC_RELAXED, __HIP_MEMORY_SCOPE_AGENT); t = v < t ? v : t; }
    for (int i = 0; i < 100000 && __builtin_amdgcn_s_memrealtime() - t < 30000ull; ++i) __builtin_amdgcn_s_sleep(1); }
}

extern "C" void kernel_launch(void* const* d_in, const int* in_sizes, int n_in, void* d_out, int out_size, void* d_ws, size_t ws_size, hipStream_t stream) {
  fused_block<<<256, 512, 0, stream>>>((const u16*)d_in[0], (const u16*)d_in[1], (const u16*)d_in[2], (const u16*)d_in[3], (const u16*)d_in[4],
                                       (const u16*)d_in[6], (const u16*)d_in[7], (const u16*)d_in[8], (u16*)d_out, (char*)d_ws);
}
```

```cpp
#include <hip/hip_runtime.h>
typedef unsigned u32; typedef unsigned long long u64;
typedef __bf16 bf16x2 __attribute__((ext_vector_type(2)));
typedef __bf16 bf16x4 __attribute__((ext_vector_type(4)));
typedef __bf16 bf16x8 __attribute__((ext_vector_type(8)));
typedef __bf16 bf16x16 __attribute__((ext_vector_type(16)));
typedef short s16x4 __attribute__((ext_vector_type(4)));
typedef float f32x2 __attribute__((ext_vector_type(2)));
typedef float f32x4 __attribute__((ext_vector_type(4)));
typedef float f32x8 __attribute__((ext_vector_type(8)));
typedef float f32x16 __attribute__((ext_vector_type(16)));
typedef u32 u32x2 __attribute__((ext_vector_type(2)));
typedef u32 u32x4 __attribute__((ext_vector_type(4)));
typedef __amdgpu_buffer_rsrc_t rsrc_t;
#define DI __device__ __forceinline__
#define LDSP(T, a) ((__attribute__((address_space(3))) T*)(unsigned long)(a))
#define BC(T, x) __builtin_bit_cast(T, x)

constexpr u32 WS_EPOCH = 0, WS_HF = 2048, WS_OF = 24576, WS_KV = 32768, WS_OT = 262144, WS_OM = 1u << 20, WS_H = 4u << 20, WS_BYTES = 16u << 20;
constexpr u32 OOB = 0x40000000u;
constexpr int SPIN_MAX = 1 << 16;

DI float asf(u32 x) { return BC(float, x); }
DI rsrc_t mkrs(u64 p, u32 n) { return __builtin_amdgcn_make_buffer_rsrc((void*)p, (short)0, (int)n, 0x00020000); }
template <int N> DI void waitbar() { asm volatile("s_waitcnt vmcnt(%0) lgkmcnt(0)\n\ts_barrier" ::"n"(N) : "memory"); }
DI void bar() { asm volatile("s_waitcnt lgkmcnt(0)\n\ts_barrier" ::: "memory"); }
DI void dma3(u32 m0, u32 v0, u32 v1, u32 v2, rsrc_t rs, u32 so) {
  asm volatile("s_nop 4\n\ts_mov_b32 m0, %0\n\ts_nop 0\n\tbuffer_load_dwordx4 %1, %4, %5 offen lds\n\tbuffer_load_dwordx4 %2, %4, %5 offen offset:1024 lds\n\t"
               "buffer_load_dwordx4 %3, %4, %5 offen offset:2048 lds" ::"s"(m0), "v"(v0), "v"(v1), "v"(v2), "s"(rs), "s"(so) : "memory");
}
DI void st16c(u32x4 v, rsrc_t rs, u32 off) { __builtin_amdgcn_raw_buffer_store_b128(v, rs, off, 0, 16); }
DI void st8c(u32x2 v, rsrc_t rs, u32 off) { __builtin_amdgcn_raw_buffer_store_b64(v, rs, off, 0, 16); }
DI u32x4 ld16c(rsrc_t rs, u32 off) { return __builtin_amdgcn_raw_buffer_load_b128(rs, off, 0, 16); }
DI u32x2 ld8c(rsrc_t rs, u32 off) { return __builtin_amdgcn_raw_buffer_load_b64(rs, off, 0, 16); }
DI u32 pk(float lo, float hi) { f32x2 v = {lo, hi}; return BC(u32, __builtin_convertvector(v, bf16x2)); }
DI float blo(u32 v) { return BC(float, v << 16); }
DI float bhi(u32 v) { return BC(float, v & 0xffff0000u); }
DI bf16x4 trr(u32 a) { return __builtin_amdgcn_ds_read_tr16_b64_v4bf16(LDSP(bf16x4, a)); }
DI bf16x8 tr2(u32 a, u32 b) { bf16x4 x = trr(a), y = trr(b); return __builtin_shufflevector(x, y, 0, 1, 2, 3, 4, 5, 6, 7); }
DI float mx32(float x) { auto r = __builtin_amdgcn_permlane32_swap(BC(u32, x), BC(u32, x), false, false); return fmaxf(asf(r[0]), asf(r[1])); }
DI u32 vunit(u32 d) { return d < 20 ? d + ((0x90DD8u >> (d & 28)) & 15) - 8 : 8; }
#define M32(a, b, c) __builtin_amdgcn_mfma_f32_32x32x16_bf16(a, b, c, 0, 0, 0)
#define M16(a, b, c) __builtin_amdgcn_mfma_f32_16x16x32_bf16(a, b, c, 0, 0, 0)

DI void nap() { asm volatile("s_sleep 2" ::: "memory"); }
DI void poll2(rsrc_t rsW, u32 off, u32 tag) {
  u32x2 fl = ld8c(rsW, off);
  for (int sp = 0; sp < SPIN_MAX && !__all(fl[0] == tag && fl[1] == tag); ++sp) { nap(); fl = ld8c(rsW, off); }
}

struct Buf { const void* p; u32 n, f; };

__global__ __launch_bounds__(512, 2) void fused_block(Buf ax, Buf aw, Buf aq, Buf ad, Buf ao, Buf ay, Buf ag, Buf au, Buf ak, Buf av) {
  __shared__ __attribute__((aligned(128))) char smem[163840];
  u32 slow, tag, tid = threadIdx.x;
  bf16x8 q0; u32x2 q1;
  u64 wsp;
  asm volatile(
    "v_readfirstlane_b32 s64, v0\n"
    "s_load_dwordx16 s[4:19], s[0:1], 0\n"
    "s_load_dwordx8 s[20:27], s[0:1], 64\n"
    "v_and_b32 v104, 63, v0\n"
    "s_lshr_b32 s64, s64, 6\n"
    "s_lshr_b32 s38, s64, 2\n"
    "s_lshl_b32 s38, s38, 4\n"
    "s_add_u32 s38, s38, 96\n"
    "s_load_dwordx4 s[28:31], s[0:1], s38\n"
    "s_and_b32 s38, s64, 2\n"
    "s_lshl_b32 s38, s38, 3\n"
    "s_add_u32 s38, s38, 128\n"
    "s_load_dwordx4 s[84:87], s[0:1], s38\n"
    "s_lshr_b32 s65, s2, 3\n"
    "s_getpc_b64 s[90:91]\n"
    "PC_%=:\n"
    "s_lshl3_add_u32 s38, s65, s64\n"
    "v_and_b32 v105, 1, v0\n"
    "v_lshl_add_u32 v105, s38, 1, v105\n"
    "v_lshlrev_b32 v105, 7, v105\n"
    "v_min_u32 v105, END_%=-PC_%=, v105\n"
    "global_load_dword v255, v105, s[90:91]\n"
    "v_lshlrev_b32 v21, 4, v104\n"
    "s_and_b32 s66, s2, 7\n"
    "s_lshr_b32 s67, s66, 2\n"
    "s_lshr_b32 s38, s65, 2\n"
    "s_lshl3_add_u32 s67, s67, s38\n"
    "s_and_b32 s68, s66, 3\n"
    "s_and_b32 s38, s65, 3\n"
    "s_lshl2_add_u32 s68, s68, s38\n"
    "s_add_u32 s71, s2, s64\n"
    "s_xor_b32 s70, s2, s65\n"
    "s_andn2_b32 s70, 1, s70\n"
    "s_and_b32 s38, s68, 2\n"
    "s_or_b32 s70, s70, s38\n"
    "s_lshr_b32 s72, s68, 1\n"
    "s_lshl3_add_u32 s72, s67, s72\n"
    "s_and_b32 s73, s64, 3\n"
    "s_and_b32 s41, s64, 1\n"
    "v_lshrrev_b32 v107, 4, v104\n"
    "v_and_b32 v108, 15, v104\n"
    "s_bitcmp1_b32 s70, 0\n"
    "s_cbranch_scc1 FP_%=\n"
    "s_lshl_b32 s74, s64, 11\n"
    "s_lshl_b32 s76, s64, 10\n"
    "s_add_u32 s76, s76, 41088\n"
    "s_mul_i32 s75, s73, 3072\n"
    "s_cmp_lt_u32 s64, 4\n"
    "s_mov_b32 s38, 28704\n"
    "s_cselect_b32 s38, 16384, s38\n"
    "s_add_u32 s75, s75, s38\n"
    "s_lshl3_add_u32 s77, s67, s64\n"
    "s_mul_i32 s77, s77, 20480\n"
    "s_mul_i32 s78, s73, 49152\n"
    "s_mul_i32 s38, s68, 192\n"
    "s_add_u32 s78, s78, s38\n"
    "s_mul_i32 s79, s64, 10240\n"
    "s_mul_i32 s38, s68, 80\n"
    "s_add_u32 s79, s79, s38\n"
    "v_lshrrev_b32 v105, 3, v104\n"
    "v_and_b32 v106, 7, v104\n"
    "v_mul_u32_u24 v113, 1280, v105\n"
    "v_xor_b32 v114, v106, v107\n"
    "v_lshl_add_u32 v49, v114, 4, v113\n"
    "v_xor_b32 v114, 64, v49\n"
    "v_add_u32 v50, 9216, v114\n"
    "v_and_b32 v114, 2, v105\n"
    "v_or_b32 v114, s41, v114\n"
    "v_lshlrev_b32 v114, 1, v114\n"
    "v_xor_b32 v114, v114, v106\n"
    "v_cmp_gt_u32 vcc, 5, v114\n"
    "v_lshl_add_u32 v114, v114, 4, v113\n"
    "v_mov_b32 v116, 2880\n"
    "v_cndmask_b32 v54, 2.0, v114, vcc\n"
    "v_mul_u32_u24 v113, 171, v104\n"
    "v_lshrrev_b32 v114, 11, v113\n"
    "v_mad_u32_u24 v51, v114, v116, v21\n"
    "v_add_u32 v114, 10944, v113\n"
    "v_lshrrev_b32 v114, 11, v114\n"
    "v_mad_u32_u24 v115, v114, v116, v21\n"
    "v_and_b32 v114, 32, v104\n"
    "v_xor_b32 v52, v115, v114\n"
    "v_add_u32 v114, 21888, v113\n"
    "v_lshrrev_b32 v114, 11, v114\n"
    "v_mad_u32_u24 v115, v114, v116, v21\n"
    "v_xor_b32 v53, 32, v115\n"
    "s_waitcnt lgkmcnt(0)\n"
    "s_and_b32 s38, s64, 5\n"
    "s_cbranch_scc1 TK_%=\n"
    "s_lshr_b32 s38, s65, 1\n"
    "s_mul_i32 s38, s38, 1664\n"
    "v_lshl_add_u32 v16, v21, 3, s38\n"
    "s_mov_b64 exec, 0x1fff\n"
    "buffer_load_dword v1, v16, s[84:87], 0 offen\n"
    "s_mov_b64 exec, -1\n"
    "TK_%=:\n"
    "s_cmp_lg_u32 s64, 3\n"
    "s_cbranch_scc1 EP0_%=\n"
    "s_mov_b32 m0, 162304\n"
    "s_mov_b64 exec, 1\n"
    "buffer_load_dword off, s[8:11], 0 sc1 lds\n"
    "s_mov_b64 exec, -1\n"
    "EP0_%=:\n"
    "s_add_u32 m0, s74, 0\n"
    "s_add_u32 s38, s77, 0\n"
    "buffer_load_dwordx4 v49, s[4:7], s38 offen lds\n"
    "buffer_load_dwordx4 v50, s[4:7], s38 offen offset:1024 lds\n"
    "s_add_u32 m0, s75, 0\n"
    "s_add_u32 s38, s78, 0\n"
    "buffer_load_dwordx4 v51, s[28:31], s38 offen lds\n"
    "buffer_load_dwordx4 v52, s[28:31], s38 offen offset:1024 lds\n"
    "buffer_load_dwordx4 v53, s[28:31], s38 offen offset:2048 lds\n"
    "s_mov_b32 m0, s76\n"
    "s_nop 0\n"
    "buffer_load_dwordx4 v54, s[12:15], s79 offen lds\n"
    "s_branch FA_%=\n"
    "FP_%=:\n"
    "s_cmp_lt_u32 s64, 4\n"
    "s_cbranch_scc0 FK_%=\n"
    "v_mul_u32_u24 v113, 1280, v107\n"
    "v_lshl_add_u32 v117, v108, 4, v113\n"
    "s_lshl2_add_u32 s39, s72, s73\n"
    "s_mul_i32 s39, s39, 5120\n"
    "s_lshl_b32 s74, s64, 11\n"
    "s_lshl_b32 s76, s64, 10\n"
    "s_add_u32 s76, s76, 41088\n"
    "s_mul_i32 s75, s73, 3072\n"
    "s_cmp_lt_u32 s64, 4\n"
    "s_mov_b32 s38, 28704\n"
    "s_cselect_b32 s38, 16384, s38\n"
    "s_add_u32 s75, s75, s38\n"
    "s_lshl3_add_u32 s77, s67, s64\n"
    "s_mul_i32 s77, s77, 20480\n"
    "s_mul_i32 s78, s73, 49152\n"
    "s_mul_i32 s38, s68, 192\n"
    "s_add_u32 s78, s78, s38\n"
    "s_mul_i32 s79, s64, 10240\n"
    "s_mul_i32 s38, s68, 80\n"
    "s_add_u32 s79, s79, s38\n"
    "v_lshrrev_b32 v105, 3, v104\n"
    "v_and_b32 v106, 7, v104\n"
    "v_mul_u32_u24 v113, 1280, v105\n"
    "v_xor_b32 v114, v106, v107\n"
    "v_lshl_add_u32 v49, v114, 4, v113\n"
    "v_xor_b32 v114, 64, v49\n"
    "v_add_u32 v50, 9216, v114\n"
    "v_and_b32 v114, 2, v105\n"
    "v_or_b32 v114, s41, v114\n"
    "v_lshlrev_b32 v114, 1, v114\n"
    "v_xor_b32 v114, v114, v106\n"
    "v_cmp_gt_u32 vcc, 5, v114\n"
    "v_lshl_add_u32 v114, v114, 4, v113\n"
    "v_mov_b32 v116, 2880\n"
    "v_cndmask_b32 v54, 2.0, v114, vcc\n"
    "v_mul_u32_u24 v113, 171, v104\n"
    "v_lshrrev_b32 v114, 11, v113\n"
    "v_mad_u32_u24 v51, v114, v116, v21\n"
    "v_add_u32 v114, 10944, v113\n"
    "v_lshrrev_b32 v114, 11, v114\n"
    "v_mad_u32_u24 v115, v114, v116, v21\n"
    "v_and_b32 v114, 32, v104\n"
    "v_xor_b32 v52, v115, v114\n"
    "v_add_u32 v114, 21888, v113\n"
    "v_lshrrev_b32 v114, 11, v114\n"
    "v_mad_u32_u24 v115, v114, v116, v21\n"
    "v_xor_b32 v53, 32, v115\n"
    "s_waitcnt lgkmcnt(0)\n"
    "buffer_load_dwordx4 v[224:227], v117, s[4:7], s39 offen offset:0\n"
    "buffer_load_dwordx4 v[228:231], v117, s[4:7], s39 offen offset:256\n"
    "buffer_load_dwordx4 v[232:235], v117, s[4:7], s39 offen offset:512\n"
    "buffer_load_dwordx4 v[236:239], v117, s[4:7], s39 offen offset:768\n"
    "buffer_load_dwordx4 v[240:243], v117, s[4:7], s39 offen offset:1024\n"
    "s_branch FA_%=\n"
    "FK_%=:\n"
    "s_and_b32 s38, s64, 2\n"
    "s_mul_i32 s38, s38, 12800\n"
    "s_add_u32 s40, s38, 90368\n"
    "s_mul_i32 s36, s41, 12288\n"
    "s_lshr_b32 s39, s65, 1\n"
    "s_waitcnt lgkmcnt(0)\n"
    "s_and_b32 s38, s39, 3\n"
    "s_mul_i32 s38, s38, 3072\n"
    "s_add_u32 s38, s38, s36\n"
    "s_add_u32 m0, s38, s40\n"
    "s_add_u32 s39, s39, 1\n"
    "buffer_load_dwordx4 v21, s[84:87], s38 offen lds\n"
    "buffer_load_dwordx4 v21, s[84:87], s38 offen offset:1024 lds\n"
    "buffer_load_dwordx4 v21, s[84:87], s38 offen offset:2048 lds\n"
    "s_and_b32 s38, s39, 3\n"
    "s_mul_i32 s38, s38, 3072\n"
    "s_add_u32 s38, s38, s36\n"
    "s_add_u32 m0, s38, s40\n"
    "s_add_u32 s39, s39, 1\n"
    "buffer_load_dwordx4 v21, s[84:87], s38 offen lds\n"
    "buffer_load_dwordx4 v21, s[84:87], s38 offen offset:1024 lds\n"
    "buffer_load_dwordx4 v21, s[84:87], s38 offen offset:2048 lds\n"
    "s_and_b32 s38, s39, 3\n"
    "s_mul_i32 s38, s38, 3072\n"
    "s_add_u32 s38, s38, s36\n"
    "s_add_u32 m0, s38, s40\n"
    "s_add_u32 s39, s39, 1\n"
    "buffer_load_dwordx4 v21, s[84:87], s38 offen lds\n"
    "buffer_load_dwordx4 v21, s[84:87], s38 offen offset:1024 lds\n"
    "buffer_load_dwordx4 v21, s[84:87], s38 offen offset:2048 lds\n"
    "s_and_b32 s38, s39, 3\n"
    "s_mul_i32 s38, s38, 3072\n"
    "s_add_u32 s38, s38, s36\n"
    "s_add_u32 m0, s38, s40\n"
    "s_add_u32 s39, s39, 1\n"
    "buffer_load_dwordx4 v21, s[84:87], s38 offen lds\n"
    "buffer_load_dwordx4 v21, s[84:87], s38 offen offset:1024 lds\n"
    "buffer_load_dwordx4 v21, s[84:87], s38 offen offset:2048 lds\n"
    "s_cmp_eq_u32 s41, 0\n"
    "s_cbranch_scc1 FJ_%=\n"
    "s_add_u32 m0, s40, 24576\n"
    "s_movk_i32 s38, 24576\n"
    "buffer_load_dwordx4 v21, s[84:87], s38 offen lds\n"
    "FJ_%=:\n"
    "s_lshl_b32 s74, s64, 11\n"
    "s_lshl_b32 s76, s64, 10\n"
    "s_add_u32 s76, s76, 41088\n"
    "s_mul_i32 s75, s73, 3072\n"
    "s_cmp_lt_u32 s64, 4\n"
    "s_mov_b32 s38, 28704\n"
    "s_cselect_b32 s38, 16384, s38\n"
    "s_add_u32 s75, s75, s38\n"
    "s_lshl3_add_u32 s77, s67, s64\n"
    "s_mul_i32 s77, s77, 20480\n"
    "s_mul_i32 s78, s73, 49152\n"
    "s_mul_i32 s38, s68, 192\n"
    "s_add_u32 s78, s78, s38\n"
    "s_mul_i32 s79, s64, 10240\n"
    "s_mul_i32 s38, s68, 80\n"
    "s_add_u32 s79, s79, s38\n"
    "v_lshrrev_b32 v105, 3, v104\n"
    "v_and_b32 v106, 7, v104\n"
    "v_mul_u32_u24 v113, 1280, v105\n"
    "v_xor_b32 v114, v106, v107\n"
    "v_lshl_add_u32 v49, v114, 4, v113\n"
    "v_xor_b32 v114, 64, v49\n"
    "v_add_u32 v50, 9216, v114\n"
    "v_and_b32 v114, 2, v105\n"
    "v_or_b32 v114, s41, v114\n"
    "v_lshlrev_b32 v114, 1, v114\n"
    "v_xor_b32 v114, v114, v106\n"
    "v_cmp_gt_u32 vcc, 5, v114\n"
    "v_lshl_add_u32 v114, v114, 4, v113\n"
    "v_mov_b32 v116, 2880\n"
    "v_cndmask_b32 v54, 2.0, v114, vcc\n"
    "v_mul_u32_u24 v113, 171, v104\n"
    "v_lshrrev_b32 v114, 11, v113\n"
    "v_mad_u32_u24 v51, v114, v116, v21\n"
    "v_add_u32 v114, 10944, v113\n"
    "v_lshrrev_b32 v114, 11, v114\n"
    "v_mad_u32_u24 v115, v114, v116, v21\n"
    "v_and_b32 v114, 32, v104\n"
    "v_xor_b32 v52, v115, v114\n"
    "v_add_u32 v114, 21888, v113\n"
    "v_lshrrev_b32 v114, 11, v114\n"
    "v_mad_u32_u24 v115, v114, v116, v21\n"
    "v_xor_b32 v53, 32, v115\n"
    "FA_%=:\n"
    "v_lshrrev_b32 v109, 2, v108\n"
    "v_and_b32 v110, 3, v104\n"
    "v_lshl_add_u32 v111, v107, 3, v109\n"
    "v_lshrrev_b32 v112, 5, v104\n"
    "v_and_b32 v113, 1, v107\n"
    "s_cmp_lt_u32 s64, 2\n"
    "s_cbranch_scc1 FQ_%=\n"
    "v_mul_u32_u24 v114, 192, v111\n"
    "v_lshl_add_u32 v115, v113, 2, v110\n"
    "v_lshl_add_u32 v114, v115, 3, v114\n"
    "v_xor_b32 v115, 32, v114\n"
    "s_lshr_b32 s38, s64, 1\n"
    "s_lshl_b32 s38, s38, 6\n"
    "s_mul_i32 s40, s41, 12320\n"
    "s_add_u32 s36, s38, s40\n"
    "s_sub_u32 s38, s38, s40\n"
    "s_add_u32 s36, s36, 16320\n"
    "s_add_u32 s38, s38, 28640\n"
    "v_add_u32 v40, s36, v114\n"
    "v_add_u32 v41, s36, v115\n"
    "v_add_u32 v42, s38, v114\n"
    "v_add_u32 v43, s38, v115\n"
    "s_branch FR_%=\n"
    "FQ_%=:\n"
    "v_and_or_b32 v114, v109, 2, v113\n"
    "v_lshlrev_b32 v114, 5, v114\n"
    "v_lshlrev_b32 v115, 7, v111\n"
    "v_add_u32 v115, 41088, v115\n"
    "v_lshlrev_b32 v116, 3, v110\n"
    "v_xad_u32 v40, v116, v114, v115\n"
    "v_add_u32 v118, 40, v116\n"
    "v_xad_u32 v41, v118, v114, v115\n"
    "v_mov_b32 v118, 0x68604820\n"
    "v_bfe_u32 v118, v118, v116, 8\n"
    "v_xad_u32 v42, v118, v114, v115\n"
    "FR_%=:\n"
    "v_lshrrev_b32 v114, 1, v108\n"
    "v_xor_b32 v114, v114, v107\n"
    "v_lshl_add_u32 v114, v108, 3, v114\n"
    "s_lshl_b32 s38, s41, 13\n"
    "v_lshl_add_u32 v55, v114, 4, s38\n"
    "v_xor_b32 v56, 64, v55\n"
    "v_lshrrev_b32 v114, 1, v109\n"
    "v_lshl_add_u32 v114, v110, 1, v114\n"
    "v_cmp_eq_u32 vcc, v114, v107\n"
    "v_mov_b32 v115, 0x3f803f80\n"
    "v_and_b32 v114, 1, v109\n"
    "v_cndmask_b32 v44, 0, v115, vcc\n"
    "v_mul_u32_u24 v114, 0xaaaa, v114\n"
    "v_add_u32 v48, 0x4444, v114\n"
    "v_mov_b32 v45, v44\n"
    "v_mov_b64 v[46:47], v[44:45]\n"
    "v_mov_b32 v58, 162304\n"
    "s_cmp_eq_u32 s64, 7\n"
    "s_cselect_b32 s69, s70, 0\n"
    "s_lshl3_add_u32 s38, s72, 1024\n"
    "s_bitcmp1_b32 s69, 0\n"
    "s_cselect_b32 s69, s38, 0\n"
    "v_mov_b64 v[60:61], 0\n"
    "v_mov_b64 v[62:63], 0\n"
    "v_mov_b64 v[64:65], 0\n"
    "v_mov_b64 v[66:67], 0\n"
    "v_mov_b64 v[204:205], 0\n"
    "v_mov_b64 v[206:207], 0\n"
    "s_bitcmp1_b32 s70, 0\n"
    "s_cbranch_scc0 FZ_%=\n"
    "s_lshr_b32 s38, s64, 2\n"
    "s_and_b32 s39, s64, 2\n"
    "s_add_u32 s39, s39, s38\n"
    "s_mul_i32 s39, s39, 12800\n"
    "s_lshl_b32 s40, s41, 5\n"
    "s_add_u32 s39, s39, s40\n"
    "s_add_u32 s39, s39, 90368\n"
    "s_xor_b32 s40, s41, 1\n"
    "s_lshl_b32 s40, s40, 3\n"
    "v_mul_u32_u24 v114, 40, v111\n"
    "v_mul_u32_u24 v115, s40, v110\n"
    "v_add3_u32 v57, v114, v115, s39\n"
    "v_mul_u32_u24 v114, 1280, v108\n"
    "v_xor_b32 v115, v107, v108\n"
    "v_lshl_add_u32 v114, v115, 4, v114\n"
    "s_lshl_b32 s39, s38, 7\n"
    "v_xor_b32 v114, s39, v114\n"
    "v_xor_b32 v115, 0x80, v114\n"
    "s_lshl_b32 s39, s38, 9\n"
    "s_add_u32 s39, s39, 141568\n"
    "s_mul_i32 s38, s38, 768\n"
    "s_add_u32 s38, s38, 141568\n"
    "v_add_u32 v118, s39, v114\n"
    "v_add_u32 v119, s38, v115\n"
    "s_nop 0\n"
    "s_cmp_lt_u32 s64, 4\n"
    "s_cbranch_scc0 FS_%=\n"
    "s_lshl_b32 s38, s73, 2\n"
    "v_add_u32 v114, s38, v107\n"
    "v_xor_b32 v115, v114, v108\n"
    "v_mul_u32_u24 v116, 1280, v114\n"
    "v_lshl_add_u32 v115, v115, 4, v116\n"
    "v_add_u32 v115, 141568, v115\n"
    "v_lshlrev_b32 v114, 3, v114\n"
    "v_lshl_add_u32 v114, v108, 26, v114\n"
    "v_mov_b64 v[120:121], 0\n"
    "v_mov_b64 v[122:123], 0\n"
    "v_mov_b32 v124, 0x3f803f80\n"
    "v_mov_b32 v125, 0x3727c5ac\n"
    "s_lshl_b32 s38, s72, 7\n"
    "s_add_u32 s39, s38, 8192\n"
    "s_waitcnt vmcnt(4)\n"
    "v_dot2c_f32_bf16 v120, v224, v124\n"
    "v_dot2c_f32_bf16 v121, v224, v224\n"
    "v_dot2c_f32_bf16 v122, v225, v124\n"
    "v_dot2c_f32_bf16 v123, v225, v225\n"
    "v_dot2c_f32_bf16 v120, v226, v124\n"
    "v_dot2c_f32_bf16 v121, v226, v226\n"
    "v_dot2c_f32_bf16 v122, v227, v124\n"
    "v_dot2c_f32_bf16 v123, v227, v227\n"
    "s_waitcnt vmcnt(3)\n"
    "v_dot2c_f32_bf16 v120, v228, v124\n"
    "v_dot2c_f32_bf16 v121, v228, v228\n"
    "v_dot2c_f32_bf16 v122, v229, v124\n"
    "v_dot2c_f32_bf16 v123, v229, v229\n"
    "v_dot2c_f32_bf16 v120, v230, v124\n"
    "v_dot2c_f32_bf16 v121, v230, v230\n"
    "v_dot2c_f32_bf16 v122, v231, v124\n"
    "v_dot2c_f32_bf16 v123, v231, v231\n"
    "s_waitcnt vmcnt(2)\n"
    "v_dot2c_f32_bf16 v120, v232, v124\n"
    "v_dot2c_f32_bf16 v121, v232, v232\n"
    "v_dot2c_f32_bf16 v122, v233, v124\n"
    "v_dot2c_f32_bf16 v123, v233, v233\n"
    "v_dot2c_f32_bf16 v120, v234, v124\n"
    "v_dot2c_f32_bf16 v121, v234, v234\n"
    "v_dot2c_f32_bf16 v122, v235, v124\n"
    "v_dot2c_f32_bf16 v123, v235, v235\n"
    "s_waitcnt vmcnt(1)\n"
    "v_dot2c_f32_bf16 v120, v236, v124\n"
    "v_dot2c_f32_bf16 v121, v236, v236\n"
    "v_dot2c_f32_bf16 v122, v237, v124\n"
    "v_dot2c_f32_bf16 v123, v237, v237\n"
    "v_dot2c_f32_bf16 v120, v238, v124\n"
    "v_dot2c_f32_bf16 v121, v238, v238\n"
    "v_dot2c_f32_bf16 v122, v239, v124\n"
    "v_dot2c_f32_bf16 v123, v239, v239\n"
    "s_waitcnt vmcnt(0)\n"
    "v_dot2c_f32_bf16 v120, v240, v124\n"
    "v_dot2c_f32_bf16 v121, v240, v240\n"
    "v_dot2c_f32_bf16 v122, v241, v124\n"
    "v_dot2c_f32_bf16 v123, v241, v241\n"
    "v_dot2c_f32_bf16 v120, v242, v124\n"
    "v_dot2c_f32_bf16 v121, v242, v242\n"
    "v_dot2c_f32_bf16 v122, v243, v124\n"
    "v_dot2c_f32_bf16 v123, v243, v243\n"
    "v_lshlrev_b32 v128, 16, v224\n"
    "v_and_b32 v129, 0xffff0000, v224\n"
    "v_lshlrev_b32 v130, 16, v225\n"
    "v_pk_add_f32 v[120:121], v[120:121], v[122:123]\n"
    "v_and_b32 v131, 0xffff0000, v225\n"
    "v_lshlrev_b32 v132, 16, v226\n"
    "v_add_f32_dpp v120, v120, v120 quad_perm:[1,0,3,2] row_mask:0xf bank_mask:0xf\n"
    "v_add_f32_dpp v121, v121, v121 quad_perm:[1,0,3,2] row_mask:0xf bank_mask:0xf\n"
    "v_and_b32 v133, 0xffff0000, v226\n"
    "v_lshlrev_b32 v134, 16, v227\n"
    "v_add_f32_dpp v120, v120, v120 quad_perm:[2,3,0,1] row_mask:0xf bank_mask:0xf\n"
    "v_add_f32_dpp v121, v121, v121 quad_perm:[2,3,0,1] row_mask:0xf bank_mask:0xf\n"
    "v_and_b32 v135, 0xffff0000, v227\n"
    "v_lshlrev_b32 v136, 16, v228\n"
    "v_add_f32_dpp v120, v120, v120 row_half_mirror row_mask:0xf bank_mask:0xf\n"
    "v_add_f32_dpp v121, v121, v121 row_half_mirror row_mask:0xf bank_mask:0xf\n"
    "v_and_b32 v137, 0xffff0000, v228\n"
    "v_lshlrev_b32 v138, 16, v229\n"
    "v_add_f32_dpp v120, v120, v120 row_mirror row_mask:0xf bank_mask:0xf\n"
    "v_add_f32_dpp v121, v121, v121 row_mirror row_mask:0xf bank_mask:0xf\n"
    "v_mul_f32 v120, 0xbacccccd, v120\n"
    "v_mul_f32 v121, 0x3acccccd, v121\n"
    "v_fma_f32 v121, -v120, v120, v121\n"
    "v_max_f32 v121, 0, v121\n"
    "v_add_f32 v121, v121, v125\n"
    "v_rsq_f32 v121, v121\n"
    "v_and_b32 v139, 0xffff0000, v229\n"
    "v_lshlrev_b32 v140, 16, v230\n"
    "v_and_b32 v141, 0xffff0000, v230\n"
    "v_lshlrev_b32 v142, 16, v231\n"
    "v_and_b32 v143, 0xffff0000, v231\n"
    "v_lshlrev_b32 v144, 16, v232\n"
    "v_and_b32 v145, 0xffff0000, v232\n"
    "v_lshlrev_b32 v146, 16, v233\n"
    "v_and_b32 v147, 0xffff0000, v233\n"
    "v_lshlrev_b32 v148, 16, v234\n"
    "v_and_b32 v149, 0xffff0000, v234\n"
    "v_lshlrev_b32 v150, 16, v235\n"
    "v_and_b32 v151, 0xffff0000, v235\n"
    "v_lshlrev_b32 v152, 16, v236\n"
    "v_and_b32 v153, 0xffff0000, v236\n"
    "v_lshlrev_b32 v154, 16, v237\n"
    "v_and_b32 v155, 0xffff0000, v237\n"
    "v_lshlrev_b32 v156, 16, v238\n"
    "v_and_b32 v157, 0xffff0000, v238\n"
    "v_lshlrev_b32 v158, 16, v239\n"
    "v_and_b32 v159, 0xffff0000, v239\n"
    "v_lshlrev_b32 v160, 16, v240\n"
    "v_and_b32 v161, 0xffff0000, v240\n"
    "v_lshlrev_b32 v162, 16, v241\n"
    "v_and_b32 v163, 0xffff0000, v241\n"
    "v_lshlrev_b32 v164, 16, v242\n"
    "v_and_b32 v165, 0xffff0000, v242\n"
    "v_lshlrev_b32 v166, 16, v243\n"
    "v_and_b32 v167, 0xffff0000, v243\n"
    "v_mul_f32 v122, v120, v121\n"
    "v_mov_b32 v124, v121\n"
    "v_pk_fma_f32 v[128:129], v[128:129], v[124:125], v[122:123] op_sel_hi:[1,0,0]\n"
    "v_pk_fma_f32 v[130:131], v[130:131], v[124:125], v[122:123] op_sel_hi:[1,0,0]\n"
    "v_pk_fma_f32 v[132:133], v[132:133], v[124:125], v[122:123] op_sel_hi:[1,0,0]\n"
    "v_pk_fma_f32 v[134:135], v[134:135], v[124:125], v[122:123] op_sel_hi:[1,0,0]\n"
    "v_cvt_pk_bf16_f32 v224, v128, v129\n"
    "v_cvt_pk_bf16_f32 v225, v130, v131\n"
    "v_cvt_pk_bf16_f32 v226, v132, v133\n"
    "v_cvt_pk_bf16_f32 v227, v134, v135\n"
    "ds_write_b128 v115, v[224:227] offset:0\n"
    "v_pk_fma_f32 v[136:137], v[136:137], v[124:125], v[122:123] op_sel_hi:[1,0,0]\n"
    "v_pk_fma_f32 v[138:139], v[138:139], v[124:125], v[122:123] op_sel_hi:[1,0,0]\n"
    "v_pk_fma_f32 v[140:141], v[140:141], v[124:125], v[122:123] op_sel_hi:[1,0,0]\n"
    "v_pk_fma_f32 v[142:143], v[142:143], v[124:125], v[122:123] op_sel_hi:[1,0,0]\n"
    "v_cvt_pk_bf16_f32 v228, v136, v137\n"
    "v_cvt_pk_bf16_f32 v229, v138, v139\n"
    "v_cvt_pk_bf16_f32 v230, v140, v141\n"
    "v_cvt_pk_bf16_f32 v231, v142, v143\n"
    "ds_write_b128 v115, v[228:231] offset:256\n"
    "v_pk_fma_f32 v[144:145], v[144:145], v[124:125], v[122:123] op_sel_hi:[1,0,0]\n"
    "v_pk_fma_f32 v[146:147], v[146:147], v[124:125], v[122:123] op_sel_hi:[1,0,0]\n"
    "v_pk_fma_f32 v[148:149], v[148:149], v[124:125], v[122:123] op_sel_hi:[1,0,0]\n"
    "v_pk_fma_f32 v[150:151], v[150:151], v[124:125], v[122:123] op_sel_hi:[1,0,0]\n"
    "v_cvt_pk_bf16_f32 v232, v144, v145\n"
    "v_cvt_pk_bf16_f32 v233, v146, v147\n"
    "v_cvt_pk_bf16_f32 v234, v148, v149\n"
    "v_cvt_pk_bf16_f32 v235, v150, v151\n"
    "ds_write_b128 v115, v[232:235] offset:512\n"
    "v_pk_fma_f32 v[152:153], v[152:153], v[124:125], v[122:123] op_sel_hi:[1,0,0]\n"
    "v_pk_fma_f32 v[154:155], v[154:155], v[124:125], v[122:123] op_sel_hi:[1,0,0]\n"
    "v_pk_fma_f32 v[156:157], v[156:157], v[124:125], v[122:123] op_sel_hi:[1,0,0]\n"
    "v_pk_fma_f32 v[158:159], v[158:159], v[124:125], v[122:123] op_sel_hi:[1,0,0]\n"
    "v_cvt_pk_bf16_f32 v236, v152, v153\n"
    "v_cvt_pk_bf16_f32 v237, v154, v155\n"
    "v_cvt_pk_bf16_f32 v238, v156, v157\n"
    "v_cvt_pk_bf16_f32 v239, v158, v159\n"
    "ds_write_b128 v115, v[236:239] offset:768\n"
    "v_pk_fma_f32 v[160:161], v[160:161], v[124:125], v[122:123] op_sel_hi:[1,0,0]\n"
    "v_pk_fma_f32 v[162:163], v[162:163], v[124:125], v[122:123] op_sel_hi:[1,0,0]\n"
    "v_pk_fma_f32 v[164:165], v[164:165], v[124:125], v[122:123] op_sel_hi:[1,0,0]\n"
    "v_pk_fma_f32 v[166:167], v[166:167], v[124:125], v[122:123] op_sel_hi:[1,0,0]\n"
    "v_cvt_pk_bf16_f32 v240, v160, v161\n"
    "v_cvt_pk_bf16_f32 v241, v162, v163\n"
    "v_cvt_pk_bf16_f32 v242, v164, v165\n"
    "v_cvt_pk_bf16_f32 v243, v166, v167\n"
    "ds_write_b128 v115, v[240:243] offset:1024\n"
    "v_mov_b64 v[28:29], v[120:121]\n"
    "v_mov_b32 v30, v114\n"
    "s_cmp_lg_u32 s64, 3\n"
    "s_cbranch_scc1 EP1_%=\n"
    "s_mov_b32 m0, 162304\n"
    "s_mov_b64 exec, 1\n"
    "buffer_load_dword off, s[8:11], 0 sc1 lds\n"
    "s_mov_b64 exec, -1\n"
    "EP1_%=:\n"
    "s_add_u32 m0, s74, 0\n"
    "s_add_u32 s38, s77, 0\n"
    "buffer_load_dwordx4 v49, s[4:7], s38 offen lds\n"
    "buffer_load_dwordx4 v50, s[4:7], s38 offen offset:1024 lds\n"
    "s_add_u32 m0, s75, 0\n"
    "s_add_u32 s38, s78, 0\n"
    "buffer_load_dwordx4 v51, s[28:31], s38 offen lds\n"
    "buffer_load_dwordx4 v52, s[28:31], s38 offen offset:1024 lds\n"
    "buffer_load_dwordx4 v53, s[28:31], s38 offen offset:2048 lds\n"
    "s_mov_b32 m0, s76\n"
    "s_nop 0\n"
    "buffer_load_dwordx4 v54, s[12:15], s79 offen lds\n"
    "s_waitcnt lgkmcnt(0)\n"
    "s_branch FB_%=\n"
    "FS_%=:\n"
    "s_add_u32 m0, s74, 0\n"
    "s_add_u32 s38, s77, 0\n"
    "buffer_load_dwordx4 v49, s[4:7], s38 offen lds\n"
    "buffer_load_dwordx4 v50, s[4:7], s38 offen offset:1024 lds\n"
    "s_add_u32 m0, s75, 0\n"
    "s_add_u32 s38, s78, 0\n"
    "buffer_load_dwordx4 v51, s[28:31], s38 offen lds\n"
    "buffer_load_dwordx4 v52, s[28:31], s38 offen offset:1024 lds\n"
    "buffer_load_dwordx4 v53, s[28:31], s38 offen offset:2048 lds\n"
    "s_mov_b32 m0, s76\n"
    "s_nop 0\n"
    "buffer_load_dwordx4 v54, s[12:15], s79 offen lds\n"
    "v_lshrrev_b32 v114, 3, v108\n"
    "v_and_b32 v115, 7, v108\n"
    "s_mul_i32 s39, s72, 1536\n"
    "s_add_u32 s39, s39, 32768\n"
    "s_cmp_lt_u32 s64, 6\n"
    "s_cbranch_scc1 FT_%=\n"
    "s_lshl_b32 s38, s41, 4\n"
    "v_add_u32 v114, s38, v108\n"
    "v_and_b32 v115, 28, v114\n"
    "v_mov_b32 v116, 0x490dd8\n"
    "v_bfe_u32 v115, v116, v115, 4\n"
    "v_add_u32 v115, v115, v114\n"
    "v_cmp_gt_u32 vcc, 20, v114\n"
    "v_mul_u32_u24 v116, 48, v112\n"
    "s_and_b32 s38, s70, 2\n"
    "v_cndmask_b32 v115, 16, v115, vcc\n"
    "v_cmp_ne_u32 vcc, 21, v114\n"
    "s_nop 1\n"
    "v_cndmask_b32 v115, 29, v115, vcc\n"
    "s_mul_i32 s38, s38, 3072\n"
    "v_cmp_gt_u32 vcc, 22, v114\n"
    "v_add_u32 v115, v115, v116\n"
    "v_lshl_add_u32 v115, v115, 1, v113\n"
    "s_add_u32 s39, s39, s38\n"
    "s_add_u32 s39, s39, 256\n"
    "s_branch FU_%=\n"
    "FT_%=:\n"
    "s_cmp_eq_u32 s64, 4\n"
    "s_cbranch_scc0 FV_%=\n"
    "v_xor_b32 v116, v112, v114\n"
    "v_lshl_add_u32 v115, v116, 3, v115\n"
    "v_mad_u32_u24 v115, v114, 48, v115\n"
    "v_lshl_add_u32 v115, v115, 1, v113\n"
    "s_mov_b64 vcc, -1\n"
    "s_branch FU_%=\n"
    "FV_%=:\n"
    "v_mul_u32_u24 v114, 104, v114\n"
    "v_add3_u32 v115, v115, v114, 32\n"
    "s_mov_b64 vcc, 0xffff\n"
    "FU_%=:\n"
    "v_lshl_add_u32 v115, v115, 3, s39\n"
    "v_cndmask_b32 v20, 2.0, v115, vcc\n"
    "s_waitcnt vmcnt(6)\n"
    "FB_%=:\n"
    "s_barrier\n"
    "FC_%=:\n"
    "v_xor_b32 v116, 64, v118\n"
    "v_xor_b32 v117, 64, v119\n"
    "ds_read_b128 v[124:127], v118 offset:0\n"
    "ds_read_b64_tr_b16 v[120:121], v57 offset:0\n"
    "ds_read_b64_tr_b16 v[122:123], v57 offset:160\n"
    "ds_read_b128 v[132:135], v116 offset:0\n"
    "ds_read_b64_tr_b16 v[128:129], v57 offset:1280\n"
    "ds_read_b64_tr_b16 v[130:131], v57 offset:1440\n"
    "ds_read_b128 v[140:143], v119 offset:0\n"
    "ds_read_b64_tr_b16 v[136:137], v57 offset:2560\n"
    "ds_read_b64_tr_b16 v[138:139], v57 offset:2720\n"
    "ds_read_b128 v[148:151], v117 offset:0\n"
    "ds_read_b64_tr_b16 v[144:145], v57 offset:3840\n"
    "ds_read_b64_tr_b16 v[146:147], v57 offset:4000\n"
    "ds_read_b128 v[156:159], v118 offset:256\n"
    "ds_read_b64_tr_b16 v[152:153], v57 offset:5120\n"
    "ds_read_b64_tr_b16 v[154:155], v57 offset:5280\n"
    "s_add_u32 m0, s74, 49280\n"
    "s_add_u32 s38, s77, 128\n"
    "buffer_load_dwordx4 v49, s[4:7], s38 offen lds\n"
    "buffer_load_dwordx4 v50, s[4:7], s38 offen offset:1024 lds\n"
    "s_add_u32 m0, s75, 49280\n"
    "s_add_u32 s38, s78, 196608\n"
    "buffer_load_dwordx4 v51, s[28:31], s38 offen lds\n"
    "buffer_load_dwordx4 v52, s[28:31], s38 offen offset:1024 lds\n"
    "buffer_load_dwordx4 v53, s[28:31], s38 offen offset:2048 lds\n"
    "s_bitcmp1_b32 s64, 1\n"
    "s_cbranch_scc1 FW_%=\n"
    "s_waitcnt lgkmcnt(12)\n"
    "v_mfma_f32_16x16x32_bf16 v[68:71], v[120:123], v[124:127], 0\n"
    "ds_read_b128 v[164:167], v116 offset:256\n"
    "ds_read_b64_tr_b16 v[160:161], v57 offset:6400\n"
    "ds_read_b64_tr_b16 v[162:163], v57 offset:6560\n"
    "s_waitcnt lgkmcnt(12)\n"
    "v_mfma_f32_16x16x32_bf16 v[68:71], v[128:131], v[132:135], v[68:71]\n"
    "ds_read_b128 v[172:175], v119 offset:256\n"
    "ds_read_b64_tr_b16 v[168:169], v57 offset:7680\n"
    "ds_read_b64_tr_b16 v[170:171], v57 offset:7840\n"
    "s_waitcnt lgkmcnt(12)\n"
    "v_mfma_f32_16x16x32_bf16 v[68:71], v[136:139], v[140:143], v[68:71]\n"
    "ds_read_b128 v[180:183], v117 offset:256\n"
    "ds_read_b64_tr_b16 v[176:177], v57 offset:8960\n"
    "ds_read_b64_tr_b16 v[178:179], v57 offset:9120\n"
    "s_waitcnt lgkmcnt(12)\n"
    "v_mfma_f32_16x16x32_bf16 v[68:71], v[144:147], v[148:151], v[68:71]\n"
    "ds_read_b128 v[188:191], v118 offset:512\n"
    "ds_read_b64_tr_b16 v[184:185], v57 offset:10240\n"
    "ds_read_b64_tr_b16 v[186:187], v57 offset:10400\n"
    "s_waitcnt lgkmcnt(12)\n"
    "v_mfma_f32_16x16x32_bf16 v[68:71], v[152:155], v[156:159], v[68:71]\n"
    "ds_read_b128 v[196:199], v116 offset:512\n"
    "ds_read_b64_tr_b16 v[192:193], v57 offset:11520\n"
    "ds_read_b64_tr_b16 v[194:195], v57 offset:11680\n"
    "s_waitcnt lgkmcnt(12)\n"
    "v_mfma_f32_16x16x32_bf16 v[68:71], v[160:163], v[164:167], v[68:71]\n"
    "s_waitcnt lgkmcnt(9)\n"
    "v_mfma_f32_16x16x32_bf16 v[68:71], v[168:171], v[172:175], v[68:71]\n"
    "s_waitcnt lgkmcnt(6)\n"
    "v_mfma_f32_16x16x32_bf16 v[68:71], v[176:179], v[180:183], v[68:71]\n"
    "s_waitcnt lgkmcnt(3)\n"
    "v_mfma_f32_16x16x32_bf16 v[68:71], v[184:187], v[188:191], v[68:71]\n"
    "s_waitcnt lgkmcnt(0)\n"
    "v_mfma_f32_16x16x32_bf16 v[68:71], v[192:195], v[196:199], v[68:71]\n"
    "s_branch FE_%=\n"
    "FW_%=:\n"
    "s_waitcnt lgkmcnt(12)\n"
    "v_mfma_f32_16x16x32_bf16 v[68:71], v[124:127], v[120:123], 0\n"
    "ds_read_b128 v[164:167], v116 offset:256\n"
    "ds_read_b64_tr_b16 v[160:161], v57 offset:6400\n"
    "ds_read_b64_tr_b16 v[162:163], v57 offset:6560\n"
    "s_waitcnt lgkmcnt(12)\n"
    "v_mfma_f32_16x16x32_bf16 v[68:71], v[132:135], v[128:131], v[68:71]\n"
    "ds_read_b128 v[172:175], v119 offset:256\n"
    "ds_read_b64_tr_b16 v[168:169], v57 offset:7680\n"
    "ds_read_b64_tr_b16 v[170:171], v57 offset:7840\n"
    "s_waitcnt lgkmcnt(12)\n"
    "v_mfma_f32_16x16x32_bf16 v[68:71], v[140:143], v[136:139], v[68:71]\n"
    "ds_read_b128 v[180:183], v117 offset:256\n"
    "ds_read_b64_tr_b16 v[176:177], v57 offset:8960\n"
    "ds_read_b64_tr_b16 v[178:179], v57 offset:9120\n"
    "s_waitcnt lgkmcnt(12)\n"
    "v_mfma_f32_16x16x32_bf16 v[68:71], v[148:151], v[144:147], v[68:71]\n"
    "ds_read_b128 v[188:191], v118 offset:512\n"
    "ds_read_b64_tr_b16 v[184:185], v57 offset:10240\n"
    "ds_read_b64_tr_b16 v[186:187], v57 offset:10400\n"
    "s_waitcnt lgkmcnt(12)\n"
    "v_mfma_f32_16x16x32_bf16 v[68:71], v[156:159], v[152:155], v[68:71]\n"
    "ds_read_b128 v[196:199], v116 offset:512\n"
    "ds_read_b64_tr_b16 v[192:193], v57 offset:11520\n"
    "ds_read_b64_tr_b16 v[194:195], v57 offset:11680\n"
    "s_waitcnt lgkmcnt(12)\n"
    "v_mfma_f32_16x16x32_bf16 v[68:71], v[164:167], v[160:163], v[68:71]\n"
    "s_waitcnt lgkmcnt(9)\n"
    "v_mfma_f32_16x16x32_bf16 v[68:71], v[172:175], v[168:171], v[68:71]\n"
    "s_waitcnt lgkmcnt(6)\n"
    "v_mfma_f32_16x16x32_bf16 v[68:71], v[180:183], v[176:179], v[68:71]\n"
    "s_waitcnt lgkmcnt(3)\n"
    "v_mfma_f32_16x16x32_bf16 v[68:71], v[188:191], v[184:187], v[68:71]\n"
    "s_waitcnt lgkmcnt(0)\n"
    "v_mfma_f32_16x16x32_bf16 v[68:71], v[196:199], v[192:195], v[68:71]\n"
    "FE_%=:\n"
    "s_barrier\n"
    "s_lshl_b32 s38, s64, 10\n"
    "s_add_u32 s38, s38, 147840\n"
    "v_add_u32 v114, s38, v21\n"
    "s_cmp_lt_u32 s64, 4\n"
    "s_cbranch_scc0 FY_%=\n"
    "s_nop 2\n"
    "ds_write_b128 v114, v[68:71]\n"
    "buffer_store_dwordx2 v[28:29], v30, s[8:11], s39 offen sc1\n"
    "s_branch FY_%=\n"
    "FZ_%=:\n"
    "s_lshr_b32 s38, s65, 1\n"
    "s_lshl3_add_u32 s38, s38, s66\n"
    "s_cmp_eq_u32 s64, 2\n"
    "s_cbranch_scc1 TQ0_%=\n"
    "s_cmp_lt_u32 s73, 2\n"
    "s_cbranch_scc0 TZ0_%=\n"
    "s_lshl_b32 s40, s73, 7\n"
    "s_add_u32 s38, s38, s40\n"
    "s_add_u32 s38, s38, 24\n"
    "s_lshl_b32 s38, s38, 13\n"
    "v_lshl_add_u32 v17, v21, 3, s38\n"
    "buffer_load_dword v17, v17, s[28:31], 0 offen\n"
    "s_branch TZ0_%=\n"
    "TQ0_%=:\n"
    "s_add_u32 s38, s38, 10\n"
    "s_lshl_b32 s38, s38, 13\n"
    "v_lshl_add_u32 v17, v21, 3, s38\n"
    "buffer_load_dword v17, v17, s[12:15], 0 offen\n"
    "TZ0_%=:\n"
    "s_add_u32 m0, s74, 49280\n"
    "s_add_u32 s38, s77, 128\n"
    "buffer_load_dwordx4 v49, s[4:7], s38 offen lds\n"
    "buffer_load_dwordx4 v50, s[4:7], s38 offen offset:1024 lds\n"
    "s_add_u32 m0, s75, 49280\n"
    "s_add_u32 s38, s78, 196608\n"
    "buffer_load_dwordx4 v51, s[28:31], s38 offen lds\n"
    "buffer_load_dwordx4 v52, s[28:31], s38 offen offset:1024 lds\n"
    "buffer_load_dwordx4 v53, s[28:31], s38 offen offset:2048 lds\n"
    "FY_%=:\n"
    "v_mov_b64 v[236:237], 0\n"
    "v_mov_b64 v[238:239], 0\n"
    "s_cmp_lt_u32 s64, 4\n"
    "s_waitcnt vmcnt(5) lgkmcnt(0)\n"
    "s_barrier\n"
    "ds_read_b32 v58, v58\n"
    "s_cbranch_scc1 VL_%=\n"
    "s_add_u32 m0, s76, 49280\n"
    "s_add_u32 s38, s79, 81920\n"
    "buffer_load_dwordx4 v54, s[12:15], s38 offen lds\n"
    "s_add_u32 m0, s74, 98560\n"
    "s_add_u32 s38, s77, 256\n"
    "buffer_load_dwordx4 v49, s[4:7], s38 offen lds\n"
    "buffer_load_dwordx4 v50, s[4:7], s38 offen offset:1024 lds\n"
    "s_add_u32 m0, s75, 98560\n"
    "s_add_u32 s38, s78, 393216\n"
    "buffer_load_dwordx4 v51, s[28:31], s38 offen lds\n"
    "buffer_load_dwordx4 v52, s[28:31], s38 offen offset:1024 lds\n"
    "buffer_load_dwordx4 v53, s[28:31], s38 offen offset:2048 lds\n"
    "s_add_u32 m0, s76, 98560\n"
    "s_add_u32 s38, s79, 163840\n"
    "buffer_load_dwordx4 v54, s[12:15], s38 offen lds\n"
    "ds_read_b64_tr_b16 v[192:193], v40 offset:0\n"
    "ds_read_b64_tr_b16 v[194:195], v40 offset:768\n"
    "ds_read_b128 v[208:211], v55 offset:0\n"
    "ds_read_b128 v[212:215], v55 offset:2048\n"
    "ds_read_b128 v[216:219], v55 offset:4096\n"
    "ds_read_b128 v[220:223], v55 offset:6144\n"
    "ds_read_b64_tr_b16 v[196:197], v41 offset:0\n"
    "ds_read_b64_tr_b16 v[198:199], v41 offset:768\n"
    "ds_read_b64_tr_b16 v[200:201], v42 offset:0\n"
    "ds_read_b64_tr_b16 v[202:203], v42 offset:768\n"
    "ds_read_b64_tr_b16 v[204:205], v43 offset:0\n"
    "ds_read_b64_tr_b16 v[206:207], v43 offset:768\n"
    "s_waitcnt lgkmcnt(10)\n"
    "v_readfirstlane_b32 s37, v58\n"
    "s_nop 3\n"
    "s_add_u32 s37, s37, 1\n"
    "s_waitcnt lgkmcnt(0)\n"
    "v_mfma_f32_16x16x32_bf16 v[128:131], v[192:195], v[208:211], 0\n"
    "ds_read_b64_tr_b16 v[224:225], v40 offset:6144\n"
    "v_mfma_f32_16x16x32_bf16 v[132:135], v[192:195], v[212:215], 0\n"
    "ds_read_b64_tr_b16 v[226:227], v40 offset:6912\n"
    "v_mfma_f32_16x16x32_bf16 v[136:139], v[192:195], v[216:219], 0\n"
    "ds_read_b128 v[240:243], v56 offset:0\n"
    "v_mfma_f32_16x16x32_bf16 v[140:143], v[192:195], v[220:223], 0\n"
    "ds_read_b128 v[244:247], v56 offset:2048\n"
    "v_mfma_f32_16x16x32_bf16 v[144:147], v[196:199], v[208:211], 0\n"
    "ds_read_b128 v[248:251], v56 offset:4096\n"
    "v_mfma_f32_16x16x32_bf16 v[148:151], v[196:199], v[212:215], 0\n"
    "ds_read_b128 v[252:255], v56 offset:6144\n"
    "v_mfma_f32_16x16x32_bf16 v[152:155], v[196:199], v[216:219], 0\n"
    "ds_read_b64_tr_b16 v[228:229], v41 offset:6144\n"
    "v_mfma_f32_16x16x32_bf16 v[156:159], v[196:199], v[220:223], 0\n"
    "ds_read_b64_tr_b16 v[230:231], v41 offset:6912\n"
    "v_smfmac_f32_16x16x64_bf16 v[60:63], v[44:47], v[192:199], v48\n"
    "ds_read_b64_tr_b16 v[232:233], v42 offset:6144\n"
    "v_mfma_f32_16x16x32_bf16 v[160:163], v[200:203], v[208:211], 0\n"
    "ds_read_b64_tr_b16 v[234:235], v42 offset:6912\n"
    "v_mfma_f32_16x16x32_bf16 v[164:167], v[200:203], v[212:215], 0\n"
    "ds_read_b64_tr_b16 v[236:237], v43 offset:6144\n"
    "v_mfma_f32_16x16x32_bf16 v[168:171], v[200:203], v[216:219], 0\n"
    "ds_read_b64_tr_b16 v[238:239], v43 offset:6912\n"
    "v_mfma_f32_16x16x32_bf16 v[172:175], v[200:203], v[220:223], 0\n"
    "v_add_u32 v40, 49280, v40\n"
    "v_add_u32 v41, 49280, v41\n"
    "v_add_u32 v42, 49280, v42\n"
    "v_mfma_f32_16x16x32_bf16 v[176:179], v[204:207], v[208:211], 0\n"
    "v_add_u32 v43, 49280, v43\n"
    "v_add_u32 v55, 49280, v55\n"
    "v_add_u32 v56, 49280, v56\n"
    "v_mfma_f32_16x16x32_bf16 v[180:183], v[204:207], v[212:215], 0\n"
    "v_mfma_f32_16x16x32_bf16 v[184:187], v[204:207], v[216:219], 0\n"
    "v_mfma_f32_16x16x32_bf16 v[188:191], v[204:207], v[220:223], 0\n"
    "s_waitcnt vmcnt(6) lgkmcnt(0)\n"
    "s_barrier\n"
    "s_add_u32 m0, s74, 0\n"
    "s_add_u32 s38, s77, 384\n"
    "buffer_load_dwordx4 v49, s[4:7], s38 offen lds\n"
    "buffer_load_dwordx4 v50, s[4:7], s38 offen offset:1024 lds\n"
    "s_add_u32 m0, s75, 0\n"
    "s_add_u32 s38, s78, 589824\n"
    "buffer_load_dwordx4 v51, s[28:31], s38 offen lds\n"
    "buffer_load_dwordx4 v52, s[28:31], s38 offen offset:1024 lds\n"
    "buffer_load_dwordx4 v53, s[28:31], s38 offen offset:2048 lds\n"
    "s_add_u32 m0, s76, 0\n"
    "s_add_u32 s38, s79, 245760\n"
    "buffer_load_dwordx4 v54, s[12:15], s38 offen lds\n"
    "s_bitcmp1_b32 s70, 0\n"
    "s_cbranch_scc0 P9a_%=\n"
    "s_lshl_b32 s38, s64, 10\n"
    "s_add_u32 s38, s38, 143744\n"
    "v_add_u32 v104, s38, v21\n"
    "ds_read_b128 v[104:107], v104\n"
    "v_and_b32 v108, 0xf0, v21\n"
    "v_mov_b32 v109, 0\n"
    "s_waitcnt lgkmcnt(0)\n"
    "v_pk_add_f32 v[68:69], v[68:69], v[104:105]\n"
    "v_pk_add_f32 v[70:71], v[70:71], v[106:107]\n"
    "v_cvt_pk_bf16_f32 v104, v68, v69\n"
    "v_cvt_pk_bf16_f32 v105, v70, v71\n"
    "s_cmp_lt_u32 s64, 6\n"
    "s_cbranch_scc1 P1a_%=\n"
    "v_cmp_eq_u32 vcc, 64, v108\n"
    "s_cmp_eq_u32 s64, 7\n"
    "s_cselect_b64 vcc, vcc, 0\n"
    "v_mov_b32 v109, 0x3f803f80\n"
    "v_cndmask_b32 v104, v104, v109, vcc\n"
    "v_cndmask_b32 v105, v105, v109, vcc\n"
    "s_lshl_b64 vcc, vcc, 1\n"
    "s_not_b64 vcc, vcc\n"
    "v_cndmask_b32 v104, 0, v104, vcc\n"
    "v_cndmask_b32 v105, 0, v105, vcc\n"
    "s_branch P8a_%=\n"
    "P1a_%=:\n"
    "v_dot2c_f32_bf16 v109, v104, v104\n"
    "v_dot2c_f32_bf16 v109, v105, v105\n"
    "v_lshrrev_b32 v108, 2, v108\n"
    "v_add_u32 v108, 163712, v108\n"
    "s_cmp_eq_u32 s64, 4\n"
    "s_cbranch_scc0 P2a_%=\n"
    "v_mov_b32 v110, v109\n"
    "s_nop 1\n"
    "v_permlane16_swap_b32 v109, v110\n"
    "v_add_f32 v109, v109, v110\n"
    "v_mov_b32 v110, v109\n"
    "s_nop 1\n"
    "v_permlane32_swap_b32 v109, v110\n"
    "v_add_f32 v109, v109, v110\n"
    "ds_write_b32 v108, v109\n"
    "s_branch P8a_%=\n"
    "P2a_%=:\n"
    "ds_write_b32 v108, v109 offset:64\n"
    "s_bitcmp1_b32 s70, 1\n"
    "s_cbranch_scc0 P8a_%=\n"
    "v_swap_b32 v104, v105\n"
    "P8a_%=:\n"
    "buffer_store_dwordx2 v[104:105], v20, s[8:11], 0 offen sc1\n"
    "P9a_%=:\n"
    "v_mfma_f32_16x16x32_bf16 v[128:131], v[224:227], v[240:243], v[128:131]\n"
    "ds_read_b64_tr_b16 v[192:193], v40 offset:0\n"
    "v_mfma_f32_16x16x32_bf16 v[132:135], v[224:227], v[244:247], v[132:135]\n"
    "ds_read_b64_tr_b16 v[194:195], v40 offset:768\n"
    "v_mfma_f32_16x16x32_bf16 v[136:139], v[224:227], v[248:251], v[136:139]\n"
    "ds_read_b128 v[208:211], v55 offset:0\n"
    "s_lshr_b32 s80, s64, 1\n"
    "v_mfma_f32_16x16x32_bf16 v[140:143], v[224:227], v[252:255], v[140:143]\n"
    "ds_read_b128 v[212:215], v55 offset:2048\n"
    "s_add_u32 s42, s80, s65\n"
    "v_mfma_f32_16x16x32_bf16 v[144:147], v[228:231], v[240:243], v[144:147]\n"
    "ds_read_b128 v[216:219], v55 offset:4096\n"
    "s_and_b32 s42, s42, 3\n"
    "v_mfma_f32_16x16x32_bf16 v[148:151], v[228:231], v[244:247], v[148:151]\n"
    "ds_read_b128 v[220:223], v55 offset:6144\n"
    "s_mul_i32 s43, s42, 5120\n"
    "v_mfma_f32_16x16x32_bf16 v[152:155], v[228:231], v[248:251], v[152:155]\n"
    "ds_read_b64_tr_b16 v[196:197], v41 offset:0\n"
    "s_lshl_b32 s81, s41, 11\n"
    "v_mfma_f32_16x16x32_bf16 v[156:159], v[228:231], v[252:255], v[156:159]\n"
    "ds_read_b64_tr_b16 v[198:199], v41 offset:768\n"
    "s_add_u32 s43, s43, s81\n"
    "v_smfmac_f32_16x16x64_bf16 v[60:63], v[44:47], v[224:231], v48\n"
    "ds_read_b64_tr_b16 v[200:201], v42 offset:0\n"
    "s_and_b32 s81, s65, 7\n"
    "v_mfma_f32_16x16x32_bf16 v[160:163], v[232:235], v[240:243], v[160:163]\n"
    "ds_read_b64_tr_b16 v[202:203], v42 offset:768\n"
    "s_mul_i32 s81, s81, 160\n"
    "v_mfma_f32_16x16x32_bf16 v[164:167], v[232:235], v[244:247], v[164:167]\n"
    "ds_read_b64_tr_b16 v[204:205], v43 offset:0\n"
    "s_mul_i32 s44, s42, 491520\n"
    "v_mfma_f32_16x16x32_bf16 v[168:171], v[232:235], v[248:251], v[168:171]\n"
    "ds_read_b64_tr_b16 v[206:207], v43 offset:768\n"
    "s_add_u32 s44, s44, s81\n"
    "v_mfma_f32_16x16x32_bf16 v[172:175], v[232:235], v[252:255], v[172:175]\n"
    "s_mul_i32 s45, s42, 204800\n"
    "v_mfma_f32_16x16x32_bf16 v[176:179], v[236:239], v[240:243], v[176:179]\n"
    "s_add_u32 s45, s45, s81\n"
    "v_mfma_f32_16x16x32_bf16 v[180:183], v[236:239], v[244:247], v[180:183]\n"
    "s_lshr_b32 s82, s65, 3\n"
    "v_mfma_f32_16x16x32_bf16 v[184:187], v[236:239], v[248:251], v[184:187]\n"
    "s_lshl2_add_u32 s82, s66, s82\n"
    "v_mfma_f32_16x16x32_bf16 v[188:191], v[236:239], v[252:255], v[188:191]\n"
    "s_xor_b32 s82, s82, 16\n"
    "s_waitcnt lgkmcnt(0)\n"
    "v_mfma_f32_16x16x32_bf16 v[128:131], v[192:195], v[208:211], v[128:131]\n"
    "ds_read_b64_tr_b16 v[224:225], v40 offset:6144\n"
    "v_mfma_f32_16x16x32_bf16 v[132:135], v[192:195], v[212:215], v[132:135]\n"
    "ds_read_b64_tr_b16 v[226:227], v40 offset:6912\n"
    "v_mfma_f32_16x16x32_bf16 v[136:139], v[192:195], v[216:219], v[136:139]\n"
    "ds_read_b128 v[240:243], v56 offset:0\n"
    "s_lshr_b32 s46, s82, 1\n"
    "v_mfma_f32_16x16x32_bf16 v[140:143], v[192:195], v[220:223], v[140:143]\n"
    "ds_read_b128 v[244:247], v56 offset:2048\n"
    "s_lshl2_add_u32 s46, s46, s42\n"
    "v_mfma_f32_16x16x32_bf16 v[144:147], v[196:199], v[208:211], v[144:147]\n"
    "ds_read_b128 v[248:251], v56 offset:4096\n"
    "s_lshl_b32 s46, s46, 5\n"
    "v_mfma_f32_16x16x32_bf16 v[148:151], v[196:199], v[212:215], v[148:151]\n"
    "ds_read_b128 v[252:255], v56 offset:6144\n"
    "s_add_u32 s46, s46, 2048\n"
    "v_mfma_f32_16x16x32_bf16 v[152:155], v[196:199], v[216:219], v[152:155]\n"
    "ds_read_b64_tr_b16 v[228:229], v41 offset:6144\n"
    "s_lshl1_add_u32 s82, s82, s41\n"
    "v_mfma_f32_16x16x32_bf16 v[156:159], v[196:199], v[220:223], v[156:159]\n"
    "ds_read_b64_tr_b16 v[230:231], v41 offset:6912\n"
    "s_and_b32 s83, s42, 1\n"
    "v_smfmac_f32_16x16x64_bf16 v[60:63], v[44:47], v[192:199], v48\n"
    "ds_read_b64_tr_b16 v[232:233], v42 offset:6144\n"
    "s_lshl1_add_u32 s82, s82, s83\n"
    "v_mfma_f32_16x16x32_bf16 v[160:163], v[200:203], v[208:211], v[160:163]\n"
    "ds_read_b64_tr_b16 v[234:235], v42 offset:6912\n"
    "s_mul_i32 s48, s82, 20480\n"
    "v_mfma_f32_16x16x32_bf16 v[164:167], v[200:203], v[212:215], v[164:167]\n"
    "ds_read_b64_tr_b16 v[236:237], v43 offset:6144\n"
    "s_add_u32 s48, s48, s81\n"
    "v_mfma_f32_16x16x32_bf16 v[168:171], v[200:203], v[216:219], v[168:171]\n"
    "ds_read_b64_tr_b16 v[238:239], v43 offset:6912\n"
    "s_lshr_b32 s47, s42, 1\n"
    "v_mfma_f32_16x16x32_bf16 v[172:175], v[200:203], v[220:223], v[172:175]\n"
    "v_add_u32 v40, 49280, v40\n"
    "v_add_u32 v41, 49280, v41\n"
    "v_add_u32 v42, 49280, v42\n"
    "v_mfma_f32_16x16x32_bf16 v[176:179], v[204:207], v[208:211], v[176:179]\n"
    "v_add_u32 v43, 49280, v43\n"
    "v_add_u32 v55, 49280, v55\n"
    "v_add_u32 v56, 49280, v56\n"
    "v_mfma_f32_16x16x32_bf16 v[180:183], v[204:207], v[212:215], v[180:183]\n"
    "s_lshl_b32 s47, s47, 6\n"
    "v_mfma_f32_16x16x32_bf16 v[184:187], v[204:207], v[216:219], v[184:187]\n"
    "s_add_u32 s47, s47, s48\n"
    "v_mfma_f32_16x16x32_bf16 v[188:191], v[204:207], v[220:223], v[188:191]\n"
    "s_lshl2_add_u32 s49, s82, s42\n"
    "s_waitcnt vmcnt(6) lgkmcnt(0)\n"
    "s_barrier\n"
    "s_add_u32 m0, s74, 49280\n"
    "s_add_u32 s38, s77, 512\n"
    "buffer_load_dwordx4 v49, s[4:7], s38 offen lds\n"
    "buffer_load_dwordx4 v50, s[4:7], s38 offen offset:1024 lds\n"
    "s_add_u32 m0, s75, 49280\n"
    "s_add_u32 s38, s78, 786432\n"
    "buffer_load_dwordx4 v51, s[28:31], s38 offen lds\n"
    "buffer_load_dwordx4 v52, s[28:31], s38 offen offset:1024 lds\n"
    "buffer_load_dwordx4 v53, s[28:31], s38 offen offset:2048 lds\n"
    "s_add_u32 m0, s76, 49280\n"
    "s_add_u32 s38, s79, 327680\n"
    "buffer_load_dwordx4 v54, s[12:15], s38 offen lds\n"
    "s_xor_b32 s50, s49, 4\n"
    "s_mul_i32 s51, s49, 12288\n"
    "s_mul_i32 s52, s50, 12288\n"
    "s_add_u32 s51, s51, 4194304\n"
    "s_add_u32 s52, s52, 4194304\n"
    "s_lshl_b32 s53, s49, 12\n"
    "s_lshl_b32 s54, s50, 12\n"
    "s_add_u32 s53, s53, 1048576\n"
    "v_mfma_f32_16x16x32_bf16 v[128:131], v[224:227], v[240:243], v[128:131]\n"
    "ds_read_b64_tr_b16 v[192:193], v40 offset:0\n"
    "v_mfma_f32_16x16x32_bf16 v[132:135], v[224:227], v[244:247], v[132:135]\n"
    "ds_read_b64_tr_b16 v[194:195], v40 offset:768\n"
    "v_mfma_f32_16x16x32_bf16 v[136:139], v[224:227], v[248:251], v[136:139]\n"
    "ds_read_b128 v[208:211], v55 offset:0\n"
    "s_add_u32 s54, s54, 1048576\n"
    "v_mfma_f32_16x16x32_bf16 v[140:143], v[224:227], v[252:255], v[140:143]\n"
    "ds_read_b128 v[212:215], v55 offset:2048\n"
    "s_lshl_b32 s49, s49, 10\n"
    "v_mfma_f32_16x16x32_bf16 v[144:147], v[228:231], v[240:243], v[144:147]\n"
    "ds_read_b128 v[216:219], v55 offset:4096\n"
    "s_lshl_b32 s50, s50, 10\n"
    "v_mfma_f32_16x16x32_bf16 v[148:151], v[228:231], v[244:247], v[148:151]\n"
    "ds_read_b128 v[220:223], v55 offset:6144\n"
    "s_add_u32 s49, s49, 262144\n"
    "v_mfma_f32_16x16x32_bf16 v[152:155], v[228:231], v[248:251], v[152:155]\n"
    "ds_read_b64_tr_b16 v[196:197], v41 offset:0\n"
    "s_add_u32 s50, s50, 262144\n"
    "v_mfma_f32_16x16x32_bf16 v[156:159], v[228:231], v[252:255], v[156:159]\n"
    "ds_read_b64_tr_b16 v[198:199], v41 offset:768\n"
    "s_lshl1_add_u32 s56, s41, s83\n"
    "v_smfmac_f32_16x16x64_bf16 v[60:63], v[44:47], v[224:231], v48\n"
    "ds_read_b64_tr_b16 v[200:201], v42 offset:0\n"
    "s_lshl2_add_u32 s56, s56, s42\n"
    "v_mfma_f32_16x16x32_bf16 v[160:163], v[232:235], v[240:243], v[160:163]\n"
    "ds_read_b64_tr_b16 v[202:203], v42 offset:768\n"
    "s_lshl_b32 s56, s56, 10\n"
    "v_mfma_f32_16x16x32_bf16 v[164:167], v[232:235], v[244:247], v[164:167]\n"
    "ds_read_b64_tr_b16 v[204:205], v43 offset:0\n"
    "s_add_u32 s56, s56, 49152\n"
    "v_mfma_f32_16x16x32_bf16 v[168:171], v[232:235], v[248:251], v[168:171]\n"
    "ds_read_b64_tr_b16 v[206:207], v43 offset:768\n"
    "s_lshl2_add_u32 s80, s41, s42\n"
    "v_mfma_f32_16x16x32_bf16 v[172:175], v[232:235], v[252:255], v[172:175]\n"
    "s_mul_i32 s55, s80, 6144\n"
    "v_mfma_f32_16x16x32_bf16 v[176:179], v[236:239], v[240:243], v[176:179]\n"
    "s_xor_b32 s88, s80, 1\n"
    "v_mfma_f32_16x16x32_bf16 v[180:183], v[236:239], v[244:247], v[180:183]\n"
    "s_mul_i32 s88, s88, 6144\n"
    "v_mfma_f32_16x16x32_bf16 v[184:187], v[236:239], v[248:251], v[184:187]\n"
    "s_xor_b32 s89, s80, 2\n"
    "v_mfma_f32_16x16x32_bf16 v[188:191], v[236:239], v[252:255], v[188:191]\n"
    "s_mul_i32 s89, s89, 6144\n"
    "s_waitcnt lgkmcnt(0)\n"
    "v_mfma_f32_16x16x32_bf16 v[128:131], v[192:195], v[208:211], v[128:131]\n"
    "ds_read_b64_tr_b16 v[224:225], v40 offset:6144\n"
    "v_mfma_f32_16x16x32_bf16 v[132:135], v[192:195], v[212:215], v[132:135]\n"
    "ds_read_b64_tr_b16 v[226:227], v40 offset:6912\n"
    "v_mfma_f32_16x16x32_bf16 v[136:139], v[192:195], v[216:219], v[136:139]\n"
    "ds_read_b128 v[240:243], v56 offset:0\n"
    "s_xor_b32 s96, s80, 3\n"
    "v_mfma_f32_16x16x32_bf16 v[140:143], v[192:195], v[220:223], v[140:143]\n"
    "ds_read_b128 v[244:247], v56 offset:2048\n"
    "s_mul_i32 s96, s96, 6144\n"
    "v_mfma_f32_16x16x32_bf16 v[144:147], v[196:199], v[208:211], v[144:147]\n"
    "ds_read_b128 v[248:251], v56 offset:4096\n"
    "s_lshr_b32 s80, s64, 1\n"
    "v_mfma_f32_16x16x32_bf16 v[148:151], v[196:199], v[212:215], v[148:151]\n"
    "ds_read_b128 v[252:255], v56 offset:6144\n"
    "s_lshl2_add_u32 s81, s67, s80\n"
    "v_mfma_f32_16x16x32_bf16 v[152:155], v[196:199], v[216:219], v[152:155]\n"
    "ds_read_b64_tr_b16 v[228:229], v41 offset:6144\n"
    "s_lshl_b32 s81, s81, 5\n"
    "v_mfma_f32_16x16x32_bf16 v[156:159], v[196:199], v[220:223], v[156:159]\n"
    "ds_read_b64_tr_b16 v[230:231], v41 offset:6912\n"
    "s_add_u32 s81, s81, s68\n"
    "v_smfmac_f32_16x16x64_bf16 v[60:63], v[44:47], v[192:199], v48\n"
    "ds_read_b64_tr_b16 v[232:233], v42 offset:6144\n"
    "s_lshl1_add_u32 s81, s81, s41\n"
    "v_mfma_f32_16x16x32_bf16 v[160:163], v[200:203], v[208:211], v[160:163]\n"
    "ds_read_b64_tr_b16 v[234:235], v42 offset:6912\n"
    "s_lshl_b32 s57, s81, 9\n"
    "v_mfma_f32_16x16x32_bf16 v[164:167], v[200:203], v[212:215], v[164:167]\n"
    "ds_read_b64_tr_b16 v[236:237], v43 offset:6144\n"
    "s_add_u32 s57, s57, 1048576\n"
    "v_mfma_f32_16x16x32_bf16 v[168:171], v[200:203], v[216:219], v[168:171]\n"
    "ds_read_b64_tr_b16 v[238:239], v43 offset:6912\n"
    "s_lshl_b32 s58, s81, 7\n"
    "v_mfma_f32_16x16x32_bf16 v[172:175], v[200:203], v[220:223], v[172:175]\n"
    "v_add_u32 v40, -98560, v40\n"
    "v_add_u32 v41, -98560, v41\n"
    "v_add_u32 v42, -98560, v42\n"
    "v_mfma_f32_16x16x32_bf16 v[176:179], v[204:207], v[208:211], v[176:179]\n"
    "v_add_u32 v43, -98560, v43\n"
    "v_add_u32 v55, -98560, v55\n"
    "v_add_u32 v56, -98560, v56\n"
    "v_mfma_f32_16x16x32_bf16 v[180:183], v[204:207], v[212:215], v[180:183]\n"
    "s_add_u32 s58, s58, 262144\n"
    "v_mfma_f32_16x16x32_bf16 v[184:187], v[204:207], v[216:219], v[184:187]\n"
    "s_lshl4_add_u32 s59, s67, s68\n"
    "v_mfma_f32_16x16x32_bf16 v[188:191], v[204:207], v[220:223], v[188:191]\n"
    "s_lshl_b32 s59, s59, 3\n"
    "s_waitcnt vmcnt(6) lgkmcnt(0)\n"
    "s_barrier\n"
    "s_cmp_eq_u32 s69, 0\n"
    "s_cbranch_scc1 J0a_%=\n"
    "v_lshrrev_b32 v58, 2, v21\n"
    "v_and_b32 v58, 63, v58\n"
    "v_add_u32 v58, 163712, v58\n"
    "ds_read2_b32 v[58:59], v58 offset1:16\n"
    "s_waitcnt lgkmcnt(0)\n"
    "v_add_f32 v58, v58, v59\n"
    "s_nop 1\n"
    "v_max_f32_dpp v58, v58, v58 quad_perm:[1,0,3,2] row_mask:0xf bank_mask:0xf\n"
    "s_nop 1\n"
    "v_max_f32_dpp v58, v58, v58 quad_perm:[2,3,0,1] row_mask:0xf bank_mask:0xf\n"
    "s_nop 1\n"
    "v_max_f32_dpp v58, v58, v58 row_half_mirror row_mask:0xf bank_mask:0xf\n"
    "s_nop 1\n"
    "v_max_f32_dpp v58, v58, v58 row_mirror row_mask:0xf bank_mask:0xf\n"
    "v_mov_b32 v59, s37\n"
    "s_mov_b64 exec, 1\n"
    "buffer_store_dwordx2 v[58:59], off, s[8:11], s69 sc1\n"
    "s_mov_b64 exec, -1\n"
    "J0a_%=:\n"
    "s_add_u32 m0, s74, 98560\n"
    "s_add_u32 s38, s77, 640\n"
    "buffer_load_dwordx4 v49, s[4:7], s38 offen lds\n"
    "buffer_load_dwordx4 v50, s[4:7], s38 offen offset:1024 lds\n"
    "s_add_u32 m0, s75, 98560\n"
    "s_add_u32 s38, s78, 983040\n"
    "buffer_load_dwordx4 v51, s[28:31], s38 offen lds\n"
    "buffer_load_dwordx4 v52, s[28:31], s38 offen offset:1024 lds\n"
    "buffer_load_dwordx4 v53, s[28:31], s38 offen offset:2048 lds\n"
    "s_add_u32 m0, s76, 98560\n"
    "s_add_u32 s38, s79, 409600\n"
    "buffer_load_dwordx4 v54, s[12:15], s38 offen lds\n"
    "s_add_u32 s59, s59, 2048\n"
    "s_lshl1_add_u32 s60, s67, s41\n"
    "s_mul_i32 s60, s60, 192\n"
    "s_mul_i32 s81, s68, 3\n"
    "s_add_u32 s60, s60, s81\n"
    "s_add_u32 s60, s60, s80\n"
    "s_lshl_b32 s60, s60, 10\n"
    "s_add_u32 s60, s60, 4193280\n"
    "v_mfma_f32_16x16x32_bf16 v[128:131], v[224:227], v[240:243], v[128:131]\n"
    "ds_read_b64_tr_b16 v[192:193], v40 offset:0\n"
    "v_mfma_f32_16x16x32_bf16 v[132:135], v[224:227], v[244:247], v[132:135]\n"
    "ds_read_b64_tr_b16 v[194:195], v40 offset:768\n"
    "v_mfma_f32_16x16x32_bf16 v[136:139], v[224:227], v[248:251], v[136:139]\n"
    "ds_read_b128 v[208:211], v55 offset:0\n"
    "s_mul_i32 s61, s64, 3072\n"
    "v_mfma_f32_16x16x32_bf16 v[140:143], v[224:227], v[252:255], v[140:143]\n"
    "ds_read_b128 v[212:215], v55 offset:2048\n"
    "s_add_u32 s62, s61, 38912\n"
    "v_mfma_f32_16x16x32_bf16 v[144:147], v[228:231], v[240:243], v[144:147]\n"
    "ds_read_b128 v[216:219], v55 offset:4096\n"
    "s_add_u32 s61, s61, 49280\n"
    "v_mfma_f32_16x16x32_bf16 v[148:151], v[228:231], v[244:247], v[148:151]\n"
    "ds_read_b128 v[220:223], v55 offset:6144\n"
    "v_mov_b64 v[72:73], 0\n"
    "v_mfma_f32_16x16x32_bf16 v[152:155], v[228:231], v[248:251], v[152:155]\n"
    "ds_read_b64_tr_b16 v[196:197], v41 offset:0\n"
    "v_mov_b64 v[74:75], 0\n"
    "v_mfma_f32_16x16x32_bf16 v[156:159], v[228:231], v[252:255], v[156:159]\n"
    "ds_read_b64_tr_b16 v[198:199], v41 offset:768\n"
    "v_mov_b64 v[76:77], 0\n"
    "v_smfmac_f32_16x16x64_bf16 v[60:63], v[44:47], v[224:231], v48\n"
    "ds_read_b64_tr_b16 v[200:201], v42 offset:0\n"
    "v_mov_b64 v[78:79], 0\n"
    "v_mfma_f32_16x16x32_bf16 v[160:163], v[232:235], v[240:243], v[160:163]\n"
    "ds_read_b64_tr_b16 v[202:203], v42 offset:768\n"
    "v_mov_b64 v[80:81], 0\n"
    "v_mfma_f32_16x16x32_bf16 v[164:167], v[232:235], v[244:247], v[164:167]\n"
    "ds_read_b64_tr_b16 v[204:205], v43 offset:0\n"
    "v_mov_b64 v[82:83], 0\n"
    "v_mfma_f32_16x16x32_bf16 v[168:171], v[232:235], v[248:251], v[168:171]\n"
    "ds_read_b64_tr_b16 v[206:207], v43 offset:768\n"
    "v_mov_b64 v[84:85], 0\n"
    "v_mfma_f32_16x16x32_bf16 v[172:175], v[232:235], v[252:255], v[172:175]\n"
    "v_mov_b64 v[86:87], 0\n"
    "v_mfma_f32_16x16x32_bf16 v[176:179], v[236:239], v[240:243], v[176:179]\n"
    "v_mov_b64 v[88:89], 0\n"
    "v_mfma_f32_16x16x32_bf16 v[180:183], v[236:239], v[244:247], v[180:183]\n"
    "v_mov_b64 v[90:91], 0\n"
    "v_mfma_f32_16x16x32_bf16 v[184:187], v[236:239], v[248:251], v[184:187]\n"
    "v_mov_b64 v[92:93], 0\n"
    "v_mfma_f32_16x16x32_bf16 v[188:191], v[236:239], v[252:255], v[188:191]\n"
    "v_mov_b64 v[94:95], 0\n"
    "s_waitcnt lgkmcnt(0)\n"
    "v_mfma_f32_16x16x32_bf16 v[128:131], v[192:195], v[208:211], v[128:131]\n"
    "ds_read_b64_tr_b16 v[224:225], v40 offset:6144\n"
    "v_mfma_f32_16x16x32_bf16 v[132:135], v[192:195], v[212:215], v[132:135]\n"
    "ds_read_b64_tr_b16 v[226:227], v40 offset:6912\n"
    "v_mfma_f32_16x16x32_bf16 v[136:139], v[192:195], v[216:219], v[136:139]\n"
    "ds_read_b128 v[240:243], v56 offset:0\n"
    "v_mov_b64 v[96:97], 0\n"
    "v_mfma_f32_16x16x32_bf16 v[140:143], v[192:195], v[220:223], v[140:143]\n"
    "ds_read_b128 v[244:247], v56 offset:2048\n"
    "v_mov_b64 v[98:99], 0\n"
    "v_mfma_f32_16x16x32_bf16 v[144:147], v[196:199], v[208:211], v[144:147]\n"
    "ds_read_b128 v[248:251], v56 offset:4096\n"
    "v_mov_b64 v[100:101], 0\n"
    "v_mfma_f32_16x16x32_bf16 v[148:151], v[196:199], v[212:215], v[148:151]\n"
    "ds_read_b128 v[252:255], v56 offset:6144\n"
    "v_mov_b64 v[102:103], 0\n"
    "v_mfma_f32_16x16x32_bf16 v[152:155], v[196:199], v[216:219], v[152:155]\n"
    "ds_read_b64_tr_b16 v[228:229], v41 offset:6144\n"
    "v_lshrrev_b32 v104, 4, v21\n"
    "v_mfma_f32_16x16x32_bf16 v[156:159], v[196:199], v[220:223], v[156:159]\n"
    "ds_read_b64_tr_b16 v[230:231], v41 offset:6912\n"
    "s_lshl_b32 s80, s41, 7\n"
    "v_smfmac_f32_16x16x64_bf16 v[60:63], v[44:47], v[192:199], v48\n"
    "ds_read_b64_tr_b16 v[232:233], v42 offset:6144\n"
    "s_and_b32 s81, s42, 2\n"
    "v_mfma_f32_16x16x32_bf16 v[160:163], v[200:203], v[208:211], v[160:163]\n"
    "ds_read_b64_tr_b16 v[234:235], v42 offset:6912\n"
    "s_lshl_b32 s81, s81, 1\n"
    "v_mfma_f32_16x16x32_bf16 v[164:167], v[200:203], v[212:215], v[164:167]\n"
    "ds_read_b64_tr_b16 v[236:237], v43 offset:6144\n"
    "v_mov_b32 v112, 1280\n"
    "v_mfma_f32_16x16x32_bf16 v[168:171], v[200:203], v[216:219], v[168:171]\n"
    "ds_read_b64_tr_b16 v[238:239], v43 offset:6912\n"
    "v_mov_b32 v113, 15360\n"
    "v_mfma_f32_16x16x32_bf16 v[172:175], v[200:203], v[220:223], v[172:175]\n"
    "v_add_u32 v40, 49280, v40\n"
    "v_add_u32 v41, 49280, v41\n"
    "v_add_u32 v42, 49280, v42\n"
    "v_mfma_f32_16x16x32_bf16 v[176:179], v[204:207], v[208:211], v[176:179]\n"
    "v_add_u32 v43, 49280, v43\n"
    "v_add_u32 v55, 49280, v55\n"
    "v_add_u32 v56, 49280, v56\n"
    "v_mfma_f32_16x16x32_bf16 v[180:183], v[204:207], v[212:215], v[180:183]\n"
    "v_mov_b32 v114, -10240\n"
    "v_mfma_f32_16x16x32_bf16 v[184:187], v[204:207], v[216:219], v[184:187]\n"
    "v_mov_b32 v115, 46080\n"
    "v_mfma_f32_16x16x32_bf16 v[188:191], v[204:207], v[220:223], v[188:191]\n"
    "v_mov_b32 v116, 5120\n"
    "s_waitcnt vmcnt(6) lgkmcnt(0)\n"
    "s_barrier\n"
    "s_add_u32 m0, s74, 0\n"
    "s_add_u32 s38, s77, 768\n"
    "buffer_load_dwordx4 v49, s[4:7], s38 offen lds\n"
    "buffer_load_dwordx4 v50, s[4:7], s38 offen offset:1024 lds\n"
    "s_add_u32 m0, s75, 0\n"
    "s_add_u32 s38, s78, 1179648\n"
    "buffer_load_dwordx4 v51, s[28:31], s38 offen lds\n"
    "buffer_load_dwordx4 v52, s[28:31], s38 offen offset:1024 lds\n"
    "buffer_load_dwordx4 v53, s[28:31], s38 offen offset:2048 lds\n"
    "s_add_u32 m0, s76, 0\n"
    "s_add_u32 s38, s79, 491520\n"
    "buffer_load_dwordx4 v54, s[12:15], s38 offen lds\n"
    "v_add_u32 v105, s80, v104\n"
    "v_mul_u32_u24 v106, 205, v105\n"
    "v_lshrrev_b32 v106, 11, v106\n"
    "v_mad_i32_i24 v107, v106, -10, v105\n"
    "v_lshrrev_b32 v108, 1, v107\n"
    "v_bfi_b32 v108, v108, 0, s81\n"
    "v_xor_b32 v107, v107, v108\n"
    "v_subrev_u32 v107, 0, v107\n"
    "v_mfma_f32_16x16x32_bf16 v[128:131], v[224:227], v[240:243], v[128:131]\n"
    "ds_read_b64_tr_b16 v[192:193], v40 offset:0\n"
    "v_mfma_f32_16x16x32_bf16 v[132:135], v[224:227], v[244:247], v[132:135]\n"
    "ds_read_b64_tr_b16 v[194:195], v40 offset:768\n"
    "v_mfma_f32_16x16x32_bf16 v[136:139], v[224:227], v[248:251], v[136:139]\n"
    "ds_read_b128 v[208:211], v55 offset:0\n"
    "v_lshlrev_b32 v107, 4, v107\n"
    "v_mfma_f32_16x16x32_bf16 v[140:143], v[224:227], v[252:255], v[140:143]\n"
    "ds_read_b128 v[212:215], v55 offset:2048\n"
    "v_mad_u32_u24 v2, v106, v112, v107\n"
    "v_mfma_f32_16x16x32_bf16 v[144:147], v[228:231], v[240:243], v[144:147]\n"
    "ds_read_b128 v[216:219], v55 offset:4096\n"
    "v_bfe_u32 v108, v106, 3, 1\n"
    "v_mfma_f32_16x16x32_bf16 v[148:151], v[228:231], v[244:247], v[148:151]\n"
    "ds_read_b128 v[220:223], v55 offset:6144\n"
    "v_lshrrev_b32 v109, 4, v106\n"
    "v_mfma_f32_16x16x32_bf16 v[152:155], v[228:231], v[248:251], v[152:155]\n"
    "ds_read_b64_tr_b16 v[196:197], v41 offset:0\n"
    "v_mad_u32_u24 v5, v108, v113, v2\n"
    "v_mfma_f32_16x16x32_bf16 v[156:159], v[228:231], v[252:255], v[156:159]\n"
    "ds_read_b64_tr_b16 v[198:199], v41 offset:768\n"
    "v_mad_i32_i24 v5, v109, v114, v5\n"
    "v_smfmac_f32_16x16x64_bf16 v[60:63], v[44:47], v[224:231], v48\n"
    "ds_read_b64_tr_b16 v[200:201], v42 offset:0\n"
    "v_bfe_u32 v108, v106, 2, 2\n"
    "v_mfma_f32_16x16x32_bf16 v[160:163], v[232:235], v[240:243], v[160:163]\n"
    "ds_read_b64_tr_b16 v[202:203], v42 offset:768\n"
    "v_mad_u32_u24 v8, v108, v115, v2\n"
    "v_mfma_f32_16x16x32_bf16 v[164:167], v[232:235], v[244:247], v[164:167]\n"
    "ds_read_b64_tr_b16 v[204:205], v43 offset:0\n"
    "v_mad_u32_u24 v8, v109, v116, v8\n"
    "v_mfma_f32_16x16x32_bf16 v[168:171], v[232:235], v[248:251], v[168:171]\n"
    "ds_read_b64_tr_b16 v[206:207], v43 offset:768\n"
    "v_add_u32 v105, 64, v105\n"
    "v_mfma_f32_16x16x32_bf16 v[172:175], v[232:235], v[252:255], v[172:175]\n"
    "v_mul_u32_u24 v106, 205, v105\n"
    "v_mfma_f32_16x16x32_bf16 v[176:179], v[236:239], v[240:243], v[176:179]\n"
    "v_lshrrev_b32 v106, 11, v106\n"
    "v_mfma_f32_16x16x32_bf16 v[180:183], v[236:239], v[244:247], v[180:183]\n"
    "v_mad_i32_i24 v107, v106, -10, v105\n"
    "v_mfma_f32_16x16x32_bf16 v[184:187], v[236:239], v[248:251], v[184:187]\n"
    "v_lshrrev_b32 v108, 1, v107\n"
    "v_mfma_f32_16x16x32_bf16 v[188:191], v[236:239], v[252:255], v[188:191]\n"
    "v_bfi_b32 v108, v108, 0, s81\n"
    "s_waitcnt lgkmcnt(0)\n"
    "v_mfma_f32_16x16x32_bf16 v[128:131], v[192:195], v[208:211], v[128:131]\n"
    "ds_read_b64_tr_b16 v[224:225], v40 offset:6144\n"
    "v_mfma_f32_16x16x32_bf16 v[132:135], v[192:195], v[212:215], v[132:135]\n"
    "ds_read_b64_tr_b16 v[226:227], v40 offset:6912\n"
    "v_mfma_f32_16x16x32_bf16 v[136:139], v[192:195], v[216:219], v[136:139]\n"
    "ds_read_b128 v[240:243], v56 offset:0\n"
    "v_xor_b32 v107, v107, v108\n"
    "v_mfma_f32_16x16x32_bf16 v[140:143], v[192:195], v[220:223], v[140:143]\n"
    "ds_read_b128 v[244:247], v56 offset:2048\n"
    "v_subrev_u32 v107, 64, v107\n"
    "v_mfma_f32_16x16x32_bf16 v[144:147], v[196:199], v[208:211], v[144:147]\n"
    "ds_read_b128 v[248:251], v56 offset:4096\n"
    "v_lshlrev_b32 v107, 4, v107\n"
    "v_mfma_f32_16x16x32_bf16 v[148:151], v[196:199], v[212:215], v[148:151]\n"
    "ds_read_b128 v[252:255], v56 offset:6144\n"
    "v_mad_u32_u24 v3, v106, v112, v107\n"
    "v_mfma_f32_16x16x32_bf16 v[152:155], v[196:199], v[216:219], v[152:155]\n"
    "ds_read_b64_tr_b16 v[228:229], v41 offset:6144\n"
    "v_bfe_u32 v108, v106, 3, 1\n"
    "v_mfma_f32_16x16x32_bf16 v[156:159], v[196:199], v[220:223], v[156:159]\n"
    "ds_read_b64_tr_b16 v[230:231], v41 offset:6912\n"
    "v_lshrrev_b32 v109, 4, v106\n"
    "v_smfmac_f32_16x16x64_bf16 v[60:63], v[44:47], v[192:199], v48\n"
    "ds_read_b64_tr_b16 v[232:233], v42 offset:6144\n"
    "v_mad_u32_u24 v6, v108, v113, v3\n"
    "v_mfma_f32_16x16x32_bf16 v[160:163], v[200:203], v[208:211], v[160:163]\n"
    "ds_read_b64_tr_b16 v[234:235], v42 offset:6912\n"
    "v_mad_i32_i24 v6, v109, v114, v6\n"
    "v_mfma_f32_16x16x32_bf16 v[164:167], v[200:203], v[212:215], v[164:167]\n"
    "ds_read_b64_tr_b16 v[236:237], v43 offset:6144\n"
    "v_bfe_u32 v108, v106, 2, 2\n"
    "v_mfma_f32_16x16x32_bf16 v[168:171], v[200:203], v[216:219], v[168:171]\n"
    "ds_read_b64_tr_b16 v[238:239], v43 offset:6912\n"
    "v_mad_u32_u24 v9, v108, v115, v3\n"
    "v_mfma_f32_16x16x32_bf16 v[172:175], v[200:203], v[220:223], v[172:175]\n"
    "v_add_u32 v40, 49280, v40\n"
    "v_add_u32 v41, 49280, v41\n"
    "v_add_u32 v42, 49280, v42\n"
    "v_mfma_f32_16x16x32_bf16 v[176:179], v[204:207], v[208:211], v[176:179]\n"
    "v_add_u32 v43, 49280, v43\n"
    "v_add_u32 v55, 49280, v55\n"
    "v_add_u32 v56, 49280, v56\n"
    "v_mfma_f32_16x16x32_bf16 v[180:183], v[204:207], v[212:215], v[180:183]\n"
    "v_mad_u32_u24 v9, v109, v116, v9\n"
    "v_mfma_f32_16x16x32_bf16 v[184:187], v[204:207], v[216:219], v[184:187]\n"
    "v_add_u32 v105, 64, v105\n"
    "v_mfma_f32_16x16x32_bf16 v[188:191], v[204:207], v[220:223], v[188:191]\n"
    "v_mul_u32_u24 v106, 205, v105\n"
    "s_waitcnt vmcnt(6) lgkmcnt(0)\n"
    "s_barrier\n"
    "s_add_u32 m0, s74, 49280\n"
    "s_add_u32 s38, s77, 896\n"
    "buffer_load_dwordx4 v49, s[4:7], s38 offen lds\n"
    "buffer_load_dwordx4 v50, s[4:7], s38 offen offset:1024 lds\n"
    "s_add_u32 m0, s75, 49280\n"
    "s_add_u32 s38, s78, 1376256\n"
    "buffer_load_dwordx4 v51, s[28:31], s38 offen lds\n"
    "buffer_load_dwordx4 v52, s[28:31], s38 offen offset:1024 lds\n"
    "buffer_load_dwordx4 v53, s[28:31], s38 offen offset:2048 lds\n"
    "s_add_u32 m0, s76, 49280\n"
    "s_add_u32 s38, s79, 573440\n"
    "buffer_load_dwordx4 v54, s[12:15], s38 offen lds\n"
    "v_lshrrev_b32 v106, 11, v106\n"
    "v_mad_i32_i24 v107, v106, -10, v105\n"
    "v_lshrrev_b32 v108, 1, v107\n"
    "v_bfi_b32 v108, v108, 0, s81\n"
    "v_xor_b32 v107, v107, v108\n"
    "v_subrev_u32 v107, 128, v107\n"
    "v_lshlrev_b32 v107, 4, v107\n"
    "v_mad_u32_u24 v4, v106, v112, v107\n"
    "v_mfma_f32_16x16x32_bf16 v[128:131], v[224:227], v[240:243], v[128:131]\n"
    "ds_read_b64_tr_b16 v[192:193], v40 offset:0\n"
    "v_mfma_f32_16x16x32_bf16 v[132:135], v[224:227], v[244:247], v[132:135]\n"
    "ds_read_b64_tr_b16 v[194:195], v40 offset:768\n"
    "v_mfma_f32_16x16x32_bf16 v[136:139], v[224:227], v[248:251], v[136:139]\n"
    "ds_read_b128 v[208:211], v55 offset:0\n"
    "v_bfe_u32 v108, v106, 3, 1\n"
    "v_mfma_f32_16x16x32_bf16 v[140:143], v[224:227], v[252:255], v[140:143]\n"
    "ds_read_b128 v[212:215], v55 offset:2048\n"
    "v_lshrrev_b32 v109, 4, v106\n"
    "v_mfma_f32_16x16x32_bf16 v[144:147], v[228:231], v[240:243], v[144:147]\n"
    "ds_read_b128 v[216:219], v55 offset:4096\n"
    "v_mad_u32_u24 v7, v108, v113, v4\n"
    "v_mfma_f32_16x16x32_bf16 v[148:151], v[228:231], v[244:247], v[148:151]\n"
    "ds_read_b128 v[220:223], v55 offset:6144\n"
    "v_mad_i32_i24 v7, v109, v114, v7\n"
    "v_mfma_f32_16x16x32_bf16 v[152:155], v[228:231], v[248:251], v[152:155]\n"
    "ds_read_b64_tr_b16 v[196:197], v41 offset:0\n"
    "v_bfe_u32 v108, v106, 2, 2\n"
    "v_mfma_f32_16x16x32_bf16 v[156:159], v[228:231], v[252:255], v[156:159]\n"
    "ds_read_b64_tr_b16 v[198:199], v41 offset:768\n"
    "v_mad_u32_u24 v10, v108, v115, v4\n"
    "v_smfmac_f32_16x16x64_bf16 v[60:63], v[44:47], v[224:231], v48\n"
    "ds_read_b64_tr_b16 v[200:201], v42 offset:0\n"
    "v_mad_u32_u24 v10, v109, v116, v10\n"
    "v_mfma_f32_16x16x32_bf16 v[160:163], v[232:235], v[240:243], v[160:163]\n"
    "ds_read_b64_tr_b16 v[202:203], v42 offset:768\n"
    "v_lshrrev_b32 v104, 4, v21\n"
    "v_mfma_f32_16x16x32_bf16 v[164:167], v[232:235], v[244:247], v[164:167]\n"
    "ds_read_b64_tr_b16 v[204:205], v43 offset:0\n"
    "v_bfe_u32 v105, v104, 3, 2\n"
    "v_mfma_f32_16x16x32_bf16 v[168:171], v[232:235], v[248:251], v[168:171]\n"
    "ds_read_b64_tr_b16 v[206:207], v43 offset:768\n"
    "v_lshrrev_b32 v106, 5, v104\n"
    "v_mfma_f32_16x16x32_bf16 v[172:175], v[232:235], v[252:255], v[172:175]\n"
    "v_and_b32 v107, 7, v104\n"
    "v_mfma_f32_16x16x32_bf16 v[176:179], v[236:239], v[240:243], v[176:179]\n"
    "v_and_b32 v108, 31, v104\n"
    "v_mfma_f32_16x16x32_bf16 v[180:183], v[236:239], v[244:247], v[180:183]\n"
    "v_and_b32 v109, 15, v104\n"
    "v_mfma_f32_16x16x32_bf16 v[184:187], v[236:239], v[248:251], v[184:187]\n"
    "v_lshrrev_b32 v110, 4, v104\n"
    "v_mfma_f32_16x16x32_bf16 v[188:191], v[236:239], v[252:255], v[188:191]\n"
    "s_mul_i32 s80, s41, 3072\n"
    "s_waitcnt lgkmcnt(0)\n"
    "v_mfma_f32_16x16x32_bf16 v[128:131], v[192:195], v[208:211], v[128:131]\n"
    "ds_read_b64_tr_b16 v[224:225], v40 offset:6144\n"
    "v_mfma_f32_16x16x32_bf16 v[132:135], v[192:195], v[212:215], v[132:135]\n"
    "ds_read_b64_tr_b16 v[226:227], v40 offset:6912\n"
    "v_mfma_f32_16x16x32_bf16 v[136:139], v[192:195], v[216:219], v[136:139]\n"
    "ds_read_b128 v[240:243], v56 offset:0\n"
    "s_add_u32 s80, s80, 49280\n"
    "v_mfma_f32_16x16x32_bf16 v[140:143], v[192:195], v[220:223], v[140:143]\n"
    "ds_read_b128 v[244:247], v56 offset:2048\n"
    "v_mul_u32_u24 v111, 768, v105\n"
    "v_mfma_f32_16x16x32_bf16 v[144:147], v[196:199], v[208:211], v[144:147]\n"
    "ds_read_b128 v[248:251], v56 offset:4096\n"
    "v_add_u32 v111, s80, v111\n"
    "v_mfma_f32_16x16x32_bf16 v[148:151], v[196:199], v[212:215], v[148:151]\n"
    "ds_read_b128 v[252:255], v56 offset:6144\n"
    "v_and_b32 v112, 1, v105\n"
    "v_mfma_f32_16x16x32_bf16 v[152:155], v[196:199], v[216:219], v[152:155]\n"
    "ds_read_b64_tr_b16 v[228:229], v41 offset:6144\n"
    "v_xor_b32 v113, v112, v106\n"
    "v_mfma_f32_16x16x32_bf16 v[156:159], v[196:199], v[220:223], v[156:159]\n"
    "ds_read_b64_tr_b16 v[230:231], v41 offset:6912\n"
    "v_lshl_add_u32 v113, v113, 3, v107\n"
    "v_smfmac_f32_16x16x64_bf16 v[60:63], v[44:47], v[192:199], v48\n"
    "ds_read_b64_tr_b16 v[232:233], v42 offset:6144\n"
    "v_lshl_add_u32 v11, v113, 4, v111\n"
    "v_mfma_f32_16x16x32_bf16 v[160:163], v[200:203], v[208:211], v[160:163]\n"
    "ds_read_b64_tr_b16 v[234:235], v42 offset:6912\n"
    "v_lshl_add_u32 v113, v112, 3, v107\n"
    "v_mfma_f32_16x16x32_bf16 v[164:167], v[200:203], v[212:215], v[164:167]\n"
    "ds_read_b64_tr_b16 v[236:237], v43 offset:6144\n"
    "v_lshrrev_b32 v114, 1, v105\n"
    "v_mfma_f32_16x16x32_bf16 v[168:171], v[200:203], v[216:219], v[168:171]\n"
    "ds_read_b64_tr_b16 v[238:239], v43 offset:6912\n"
    "v_xor_b32 v114, v114, v106\n"
    "v_mfma_f32_16x16x32_bf16 v[172:175], v[200:203], v[220:223], v[172:175]\n"
    "v_add_u32 v40, -98560, v40\n"
    "v_add_u32 v41, -98560, v41\n"
    "v_add_u32 v42, -98560, v42\n"
    "v_mfma_f32_16x16x32_bf16 v[176:179], v[204:207], v[208:211], v[176:179]\n"
    "v_add_u32 v43, -98560, v43\n"
    "v_add_u32 v55, -98560, v55\n"
    "v_add_u32 v56, -98560, v56\n"
    "v_mfma_f32_16x16x32_bf16 v[180:183], v[204:207], v[212:215], v[180:183]\n"
    "v_lshl_add_u32 v113, v113, 1, v114\n"
    "v_mfma_f32_16x16x32_bf16 v[184:187], v[204:207], v[216:219], v[184:187]\n"
    "v_lshl_add_u32 v113, v113, 2, v111\n"
    "v_mfma_f32_16x16x32_bf16 v[188:191], v[204:207], v[220:223], v[188:191]\n"
    "v_add_u32 v12, 256, v113\n"
    "s_waitcnt vmcnt(6) lgkmcnt(0)\n"
    "s_barrier\n"
    "s_add_u32 m0, s74, 98560\n"
    "s_add_u32 s38, s77, 1024\n"
    "buffer_load_dwordx4 v49, s[4:7], s38 offen lds\n"
    "buffer_load_dwordx4 v50, s[4:7], s38 offen offset:1024 lds\n"
    "s_add_u32 m0, s75, 98560\n"
    "s_add_u32 s38, s78, 1572864\n"
    "buffer_load_dwordx4 v51, s[28:31], s38 offen lds\n"
    "buffer_load_dwordx4 v52, s[28:31], s38 offen offset:1024 lds\n"
    "buffer_load_dwordx4 v53, s[28:31], s38 offen offset:2048 lds\n"
    "s_add_u32 m0, s76, 98560\n"
    "s_add_u32 s38, s79, 655360\n"
    "buffer_load_dwordx4 v54, s[12:15], s38 offen lds\n"
    "v_and_b32 v113, 28, v108\n"
    "v_mov_b32 v114, 0x90dd8\n"
    "v_bfe_u32 v113, v114, v113, 4\n"
    "v_add_u32 v113, v113, v108\n"
    "v_cmp_gt_u32 vcc, 20, v108\n"
    "s_add_u32 s80, s80, 256\n"
    "v_mul_u32_u24 v114, 768, v106\n"
    "v_cndmask_b32 v113, 29, v113, vcc\n"
    "v_mfma_f32_16x16x32_bf16 v[128:131], v[224:227], v[240:243], v[128:131]\n"
    "ds_read_b64_tr_b16 v[192:193], v40 offset:0\n"
    "v_mfma_f32_16x16x32_bf16 v[132:135], v[224:227], v[244:247], v[132:135]\n"
    "ds_read_b64_tr_b16 v[194:195], v40 offset:768\n"
    "v_mfma_f32_16x16x32_bf16 v[136:139], v[224:227], v[248:251], v[136:139]\n"
    "ds_read_b128 v[208:211], v55 offset:0\n"
    "v_and_b32 v115, 31, v108\n"
    "v_mfma_f32_16x16x32_bf16 v[140:143], v[224:227], v[252:255], v[140:143]\n"
    "ds_read_b128 v[212:215], v55 offset:2048\n"
    "v_cmp_ne_u32 vcc, 24, v115\n"
    "v_mfma_f32_16x16x32_bf16 v[144:147], v[228:231], v[240:243], v[144:147]\n"
    "ds_read_b128 v[216:219], v55 offset:4096\n"
    "s_nop 1\n"
    "v_mfma_f32_16x16x32_bf16 v[148:151], v[228:231], v[244:247], v[148:151]\n"
    "ds_read_b128 v[220:223], v55 offset:6144\n"
    "v_cndmask_b32 v113, 16, v113, vcc\n"
    "v_mfma_f32_16x16x32_bf16 v[152:155], v[228:231], v[248:251], v[152:155]\n"
    "ds_read_b64_tr_b16 v[196:197], v41 offset:0\n"
    "v_lshl_add_u32 v113, v113, 4, v114\n"
    "v_mfma_f32_16x16x32_bf16 v[156:159], v[228:231], v[252:255], v[156:159]\n"
    "ds_read_b64_tr_b16 v[198:199], v41 offset:768\n"
    "v_add_u32 v13, s80, v113\n"
    "v_smfmac_f32_16x16x64_bf16 v[60:63], v[44:47], v[224:231], v48\n"
    "ds_read_b64_tr_b16 v[200:201], v42 offset:0\n"
    "v_and_b32 v113, 3, v104\n"
    "v_mfma_f32_16x16x32_bf16 v[160:163], v[232:235], v[240:243], v[160:163]\n"
    "ds_read_b64_tr_b16 v[202:203], v42 offset:768\n"
    "v_lshlrev_b32 v14, 3, v113\n"
    "v_mfma_f32_16x16x32_bf16 v[164:167], v[232:235], v[244:247], v[164:167]\n"
    "ds_read_b64_tr_b16 v[204:205], v43 offset:0\n"
    "v_lshrrev_b32 v113, 2, v104\n"
    "v_mfma_f32_16x16x32_bf16 v[168:171], v[232:235], v[248:251], v[168:171]\n"
    "ds_read_b64_tr_b16 v[206:207], v43 offset:768\n"
    "v_mul_u32_u24 v113, 160, v113\n"
    "v_mfma_f32_16x16x32_bf16 v[172:175], v[232:235], v[252:255], v[172:175]\n"
    "s_mul_i32 s80, s42, 5120\n"
    "v_mfma_f32_16x16x32_bf16 v[176:179], v[236:239], v[240:243], v[176:179]\n"
    "v_add3_u32 v15, v113, v14, s80\n"
    "v_mfma_f32_16x16x32_bf16 v[180:183], v[236:239], v[244:247], v[180:183]\n"
    "v_mul_u32_u24 v113, 1280, v109\n"
    "v_mfma_f32_16x16x32_bf16 v[184:187], v[236:239], v[248:251], v[184:187]\n"
    "v_lshl_add_u32 v16, v110, 3, v113\n"
    "v_mfma_f32_16x16x32_bf16 v[188:191], v[236:239], v[252:255], v[188:191]\n"
    "v_lshlrev_b32 v113, 3, v109\n"
    "s_waitcnt lgkmcnt(0)\n"
    "v_mfma_f32_16x16x32_bf16 v[128:131], v[192:195], v[208:211], v[128:131]\n"
    "ds_read_b64_tr_b16 v[224:225], v40 offset:6144\n"
    "v_mfma_f32_16x16x32_bf16 v[132:135], v[192:195], v[212:215], v[132:135]\n"
    "ds_read_b64_tr_b16 v[226:227], v40 offset:6912\n"
    "v_mfma_f32_16x16x32_bf16 v[136:139], v[192:195], v[216:219], v[136:139]\n"
    "ds_read_b128 v[240:243], v56 offset:0\n"
    "v_lshl_add_u32 v18, v110, 8, v113\n"
    "v_mfma_f32_16x16x32_bf16 v[140:143], v[192:195], v[220:223], v[140:143]\n"
    "ds_read_b128 v[244:247], v56 offset:2048\n"
    "v_and_b32 v114, 1, v110\n"
    "v_mfma_f32_16x16x32_bf16 v[144:147], v[196:199], v[208:211], v[144:147]\n"
    "ds_read_b128 v[248:251], v56 offset:4096\n"
    "v_lshl_add_u32 v115, v114, 12, v113\n"
    "v_mfma_f32_16x16x32_bf16 v[148:151], v[196:199], v[212:215], v[148:151]\n"
    "ds_read_b128 v[252:255], v56 offset:6144\n"
    "v_lshl_add_u32 v20, v106, 30, v115\n"
    "v_mfma_f32_16x16x32_bf16 v[152:155], v[196:199], v[216:219], v[152:155]\n"
    "ds_read_b64_tr_b16 v[228:229], v41 offset:6144\n"
    "v_lshlrev_b32 v114, 14, v114\n"
    "v_mfma_f32_16x16x32_bf16 v[156:159], v[196:199], v[220:223], v[156:159]\n"
    "ds_read_b64_tr_b16 v[230:231], v41 offset:6912\n"
    "v_lshl_add_u32 v114, v106, 8, v114\n"
    "v_smfmac_f32_16x16x64_bf16 v[60:63], v[44:47], v[192:199], v48\n"
    "ds_read_b64_tr_b16 v[232:233], v42 offset:6144\n"
    "v_lshl_add_u32 v19, v109, 4, v114\n"
    "v_mfma_f32_16x16x32_bf16 v[160:163], v[200:203], v[208:211], v[160:163]\n"
    "ds_read_b64_tr_b16 v[234:235], v42 offset:6912\n"
    "s_lshl_b32 s80, s41, 9\n"
    "v_mfma_f32_16x16x32_bf16 v[164:167], v[200:203], v[212:215], v[164:167]\n"
    "ds_read_b64_tr_b16 v[236:237], v43 offset:6144\n"
    "s_add_u32 s80, s80, 161280\n"
    "v_mfma_f32_16x16x32_bf16 v[168:171], v[200:203], v[216:219], v[168:171]\n"
    "ds_read_b64_tr_b16 v[238:239], v43 offset:6912\n"
    "v_add_u32 v23, s80, v113\n"
    "v_mfma_f32_16x16x32_bf16 v[172:175], v[200:203], v[220:223], v[172:175]\n"
    "v_add_u32 v40, 49280, v40\n"
    "v_add_u32 v41, 49280, v41\n"
    "v_add_u32 v42, 49280, v42\n"
    "v_mfma_f32_16x16x32_bf16 v[176:179], v[204:207], v[208:211], v[176:179]\n"
    "v_add_u32 v43, 49280, v43\n"
    "v_add_u32 v55, 49280, v55\n"
    "v_add_u32 v56, 49280, v56\n"
    "v_mfma_f32_16x16x32_bf16 v[180:183], v[204:207], v[212:215], v[180:183]\n"
    "s_mul_i32 s80, s41, 3072\n"
    "v_mfma_f32_16x16x32_bf16 v[184:187], v[204:207], v[216:219], v[184:187]\n"
    "s_add_u32 s80, s80, 147840\n"
    "v_mfma_f32_16x16x32_bf16 v[188:191], v[204:207], v[220:223], v[188:191]\n"
    "v_mul_u32_u24 v113, 48, v109\n"
    "s_waitcnt vmcnt(6) lgkmcnt(0)\n"
    "s_barrier\n"
    "s_cmp_lg_u32 s64, 4\n"
    "s_cbranch_scc1 J6a_%=\n"
    "s_mov_b32 m0, 160256\n"
    "s_movk_i32 s38, 1024\n"
    "buffer_load_dwordx4 v21, s[8:11], s38 offen sc1 lds\n"
    "J6a_%=:\n"
    "s_add_u32 m0, s74, 0\n"
    "s_add_u32 s38, s77, 1152\n"
    "buffer_load_dwordx4 v49, s[4:7], s38 offen lds\n"
    "buffer_load_dwordx4 v50, s[4:7], s38 offen offset:1024 lds\n"
    "s_add_u32 m0, s75, 0\n"
    "s_add_u32 s38, s78, 1769472\n"
    "buffer_load_dwordx4 v51, s[28:31], s38 offen lds\n"
    "buffer_load_dwordx4 v52, s[28:31], s38 offen offset:1024 lds\n"
    "buffer_load_dwordx4 v53, s[28:31], s38 offen offset:2048 lds\n"
    "s_add_u32 m0, s76, 0\n"
    "s_add_u32 s38, s79, 737280\n"
    "buffer_load_dwordx4 v54, s[12:15], s38 offen lds\n"
    "v_add_u32 v24, s80, v113\n"
    "s_lshr_b32 s80, s64, 1\n"
    "s_mul_i32 s80, s80, 1536\n"
    "s_mul_i32 s81, s41, 6144\n"
    "s_add_u32 s82, s80, s81\n"
    "s_sub_u32 s80, s80, s81\n"
    "s_add_u32 s82, s82, 147840\n"
    "s_add_u32 s80, s80, 153984\n"
    "v_mul_u32_u24 v113, 48, v108\n"
    "v_lshl_add_u32 v113, v106, 2, v113\n"
    "v_add_u32 v25, s80, v113\n"
    "v_add_u32 v26, s82, v113\n"
    "v_mad_u32_u24 v118, v106, 12, v25\n"
    "v_mad_u32_u24 v119, v106, 12, v26\n"
    "v_mov_b32 v27, 1.0\n"
    "s_lshl_b32 s80, s46, 2\n"
    "s_add_u32 s80, s80, 16384\n"
    "s_and_b32 s82, s59, 24\n"
    "s_andn2_b32 s81, s59, 24\n"
    "s_lshl_b32 s81, s81, 2\n"
    "s_add_u32 s81, s81, s82\n"
    "s_add_u32 s81, s81, 16384\n"
    "v_mfma_f32_16x16x32_bf16 v[128:131], v[224:227], v[240:243], v[128:131]\n"
    "ds_read_b64_tr_b16 v[192:193], v40 offset:0\n"
    "v_mfma_f32_16x16x32_bf16 v[132:135], v[224:227], v[244:247], v[132:135]\n"
    "ds_read_b64_tr_b16 v[194:195], v40 offset:768\n"
    "v_mfma_f32_16x16x32_bf16 v[136:139], v[224:227], v[248:251], v[136:139]\n"
    "ds_read_b128 v[208:211], v55 offset:0\n"
    "v_mfma_f32_16x16x32_bf16 v[140:143], v[224:227], v[252:255], v[140:143]\n"
    "ds_read_b128 v[212:215], v55 offset:2048\n"
    "v_mfma_f32_16x16x32_bf16 v[144:147], v[228:231], v[240:243], v[144:147]\n"
    "ds_read_b128 v[216:219], v55 offset:4096\n"
    "v_mfma_f32_16x16x32_bf16 v[148:151], v[228:231], v[244:247], v[148:151]\n"
    "ds_read_b128 v[220:223], v55 offset:6144\n"
    "v_mfma_f32_16x16x32_bf16 v[152:155], v[228:231], v[248:251], v[152:155]\n"
    "ds_read_b64_tr_b16 v[196:197], v41 offset:0\n"
    "v_mfma_f32_16x16x32_bf16 v[156:159], v[228:231], v[252:255], v[156:159]\n"
    "ds_read_b64_tr_b16 v[198:199], v41 offset:768\n"
    "v_smfmac_f32_16x16x64_bf16 v[60:63], v[44:47], v[224:231], v48\n"
    "ds_read_b64_tr_b16 v[200:201], v42 offset:0\n"
    "v_mfma_f32_16x16x32_bf16 v[160:163], v[232:235], v[240:243], v[160:163]\n"
    "ds_read_b64_tr_b16 v[202:203], v42 offset:768\n"
    "v_mfma_f32_16x16x32_bf16 v[164:167], v[232:235], v[244:247], v[164:167]\n"
    "ds_read_b64_tr_b16 v[204:205], v43 offset:0\n"
    "v_mfma_f32_16x16x32_bf16 v[168:171], v[232:235], v[248:251], v[168:171]\n"
    "ds_read_b64_tr_b16 v[206:207], v43 offset:768\n"
    "v_mfma_f32_16x16x32_bf16 v[172:175], v[232:235], v[252:255], v[172:175]\n"
    "v_mfma_f32_16x16x32_bf16 v[176:179], v[236:239], v[240:243], v[176:179]\n"
    "v_mfma_f32_16x16x32_bf16 v[180:183], v[236:239], v[244:247], v[180:183]\n"
    "v_mfma_f32_16x16x32_bf16 v[184:187], v[236:239], v[248:251], v[184:187]\n"
    "v_mfma_f32_16x16x32_bf16 v[188:191], v[236:239], v[252:255], v[188:191]\n"
    "s_waitcnt lgkmcnt(0)\n"
    "v_mfma_f32_16x16x32_bf16 v[128:131], v[192:195], v[208:211], v[128:131]\n"
    "ds_read_b64_tr_b16 v[224:225], v40 offset:6144\n"
    "v_mfma_f32_16x16x32_bf16 v[132:135], v[192:195], v[212:215], v[132:135]\n"
    "ds_read_b64_tr_b16 v[226:227], v40 offset:6912\n"
    "v_mfma_f32_16x16x32_bf16 v[136:139], v[192:195], v[216:219], v[136:139]\n"
    "ds_read_b128 v[240:243], v56 offset:0\n"
    "v_mfma_f32_16x16x32_bf16 v[140:143], v[192:195], v[220:223], v[140:143]\n"
    "ds_read_b128 v[244:247], v56 offset:2048\n"
    "v_mfma_f32_16x16x32_bf16 v[144:147], v[196:199], v[208:211], v[144:147]\n"
    "ds_read_b128 v[248:251], v56 offset:4096\n"
    "v_mfma_f32_16x16x32_bf16 v[148:151], v[196:199], v[212:215], v[148:151]\n"
    "ds_read_b128 v[252:255], v56 offset:6144\n"
    "v_mfma_f32_16x16x32_bf16 v[152:155], v[196:199], v[216:219], v[152:155]\n"
    "ds_read_b64_tr_b16 v[228:229], v41 offset:6144\n"
    "v_mfma_f32_16x16x32_bf16 v[156:159], v[196:199], v[220:223], v[156:159]\n"
    "ds_read_b64_tr_b16 v[230:231], v41 offset:6912\n"
    "v_smfmac_f32_16x16x64_bf16 v[60:63], v[44:47], v[192:199], v48\n"
    "ds_read_b64_tr_b16 v[232:233], v42 offset:6144\n"
    "v_mfma_f32_16x16x32_bf16 v[160:163], v[200:203], v[208:211], v[160:163]\n"
    "ds_read_b64_tr_b16 v[234:235], v42 offset:6912\n"
    "v_mfma_f32_16x16x32_bf16 v[164:167], v[200:203], v[212:215], v[164:167]\n"
    "ds_read_b64_tr_b16 v[236:237], v43 offset:6144\n"
    "v_mfma_f32_16x16x32_bf16 v[168:171], v[200:203], v[216:219], v[168:171]\n"
    "ds_read_b64_tr_b16 v[238:239], v43 offset:6912\n"
    "v_mfma_f32_16x16x32_bf16 v[172:175], v[200:203], v[220:223], v[172:175]\n"
    "v_add_u32 v40, 49280, v40\n"
    "v_add_u32 v41, 49280, v41\n"
    "v_add_u32 v42, 49280, v42\n"
    "v_mfma_f32_16x16x32_bf16 v[176:179], v[204:207], v[208:211], v[176:179]\n"
    "v_add_u32 v43, 49280, v43\n"
    "v_add_u32 v55, 49280, v55\n"
    "v_add_u32 v56, 49280, v56\n"
    "v_mfma_f32_16x16x32_bf16 v[180:183], v[204:207], v[212:215], v[180:183]\n"
    "s_lshl_b32 s38, s64, 9\n"
    "s_add_u32 s38, s38, 148480\n"
    "v_lshrrev_b32 v117, 1, v21\n"
    "v_add_u32 v117, s38, v117\n"
    "v_mfma_f32_16x16x32_bf16 v[184:187], v[204:207], v[216:219], v[184:187]\n"
    "v_mfma_f32_16x16x32_bf16 v[188:191], v[204:207], v[220:223], v[188:191]\n"
    "ds_write_b64 v117, v[60:61]\n"
    "s_waitcnt vmcnt(6) lgkmcnt(0)\n"
    "s_barrier\n"
    "v_add_u32 v58, 160256, v21\n"
    "ds_read_b128 v[68:71], v58\n"
    "s_mov_b32 s36, 0x10000\n"
    "s_waitcnt lgkmcnt(0)\n"
    "Ca_%=:\n"
    "v_cmp_ne_u32 vcc, s37, v69\n"
    "s_nop 1\n"
    "s_cbranch_vccnz Sa_%=\n"
    "v_cmp_ne_u32 vcc, s37, v71\n"
    "s_nop 1\n"
    "s_cbranch_vccz Ka_%=\n"
    "Sa_%=:\n"
    "s_sleep 2\n"
    "s_movk_i32 s38, 1024\n"
    "buffer_load_dwordx4 v[68:71], v21, s[8:11], s38 offen sc1\n"
    "s_sub_u32 s36, s36, 1\n"
    "s_cmp_lg_u32 s36, 0\n"
    "s_waitcnt vmcnt(0)\n"
    "s_cbranch_scc1 Ca_%=\n"
    "Ka_%=:\n"
    "s_cmp_lg_u32 s64, 4\n"
    "s_cbranch_scc1 K2a_%=\n"
    "s_mov_b32 m0, 161280\n"
    "s_lshl_b32 s38, s67, 10\n"
    "s_add_u32 s38, s38, 8192\n"
    "buffer_load_dwordx4 v21, s[8:11], s38 offen sc1 lds\n"
    "K2a_%=:\n"
    "s_mov_b32 m0, s61\n"
    "s_add_u32 s38, s62, 24576\n"
    "buffer_load_dwordx4 v21, s[8:11], s62 offen sc1 lds\n"
    "buffer_load_dwordx4 v21, s[8:11], s62 offen offset:1024 sc1 lds\n"
    "buffer_load_dwordx4 v21, s[8:11], s62 offen offset:2048 sc1 lds\n"
    "s_add_u32 m0, s61, 24576\n"
    "v_max_f32 v68, v68, v70\n"
    "buffer_load_dwordx4 v21, s[8:11], s38 offen sc1 lds\n"
    "buffer_load_dwordx4 v21, s[8:11], s38 offen offset:1024 sc1 lds\n"
    "buffer_load_dwordx4 v21, s[8:11], s38 offen offset:2048 sc1 lds\n"
    "s_nop 1\n"
    "v_max_f32_dpp v68, v68, v68 quad_perm:[1,0,3,2] row_mask:0xf bank_mask:0xf\n"
    "s_nop 1\n"
    "v_max_f32_dpp v68, v68, v68 quad_perm:[2,3,0,1] row_mask:0xf bank_mask:0xf\n"
    "s_nop 1\n"
    "v_max_f32_dpp v68, v68, v68 row_half_mirror row_mask:0xf bank_mask:0xf\n"
    "s_nop 1\n"
    "v_max_f32_dpp v68, v68, v68 row_mirror row_mask:0xf bank_mask:0xf\n"
    "s_nop 1\n"
    "v_max_f32_dpp v68, v68, v68 row_bcast:15 row_mask:0xf bank_mask:0xf\n"
    "s_nop 1\n"
    "v_max_f32_dpp v68, v68, v68 row_bcast:31 row_mask:0xf bank_mask:0xf\n"
    "s_nop 1\n"
    "v_readlane_b32 s63, v68, 63\n"
    "v_mfma_f32_16x16x32_bf16 v[128:131], v[224:227], v[240:243], v[128:131]\n"
    "ds_read_b64_tr_b16 v[192:193], v40 offset:0\n"
    "v_mfma_f32_16x16x32_bf16 v[132:135], v[224:227], v[244:247], v[132:135]\n"
    "ds_read_b64_tr_b16 v[194:195], v40 offset:768\n"
    "v_mfma_f32_16x16x32_bf16 v[136:139], v[224:227], v[248:251], v[136:139]\n"
    "ds_read_b128 v[208:211], v55 offset:0\n"
    "v_mfma_f32_16x16x32_bf16 v[140:143], v[224:227], v[252:255], v[140:143]\n"
    "ds_read_b128 v[212:215], v55 offset:2048\n"
    "v_mfma_f32_16x16x32_bf16 v[144:147], v[228:231], v[240:243], v[144:147]\n"
    "ds_read_b128 v[216:219], v55 offset:4096\n"
    "v_mfma_f32_16x16x32_bf16 v[148:151], v[228:231], v[244:247], v[148:151]\n"
    "ds_read_b128 v[220:223], v55 offset:6144\n"
    "v_mfma_f32_16x16x32_bf16 v[152:155], v[228:231], v[248:251], v[152:155]\n"
    "ds_read_b64_tr_b16 v[196:197], v41 offset:0\n"
    "v_mfma_f32_16x16x32_bf16 v[156:159], v[228:231], v[252:255], v[156:159]\n"
    "ds_read_b64_tr_b16 v[198:199], v41 offset:768\n"
    "v_smfmac_f32_16x16x64_bf16 v[60:63], v[44:47], v[224:231], v48\n"
    "ds_read_b64_tr_b16 v[200:201], v42 offset:0\n"
    "v_mfma_f32_16x16x32_bf16 v[160:163], v[232:235], v[240:243], v[160:163]\n"
    "ds_read_b64_tr_b16 v[202:203], v42 offset:768\n"
    "v_mfma_f32_16x16x32_bf16 v[164:167], v[232:235], v[244:247], v[164:167]\n"
    "ds_read_b64_tr_b16 v[204:205], v43 offset:0\n"
    "v_mfma_f32_16x16x32_bf16 v[168:171], v[232:235], v[248:251], v[168:171]\n"
    "ds_read_b64_tr_b16 v[206:207], v43 offset:768\n"
    "v_mfma_f32_16x16x32_bf16 v[172:175], v[232:235], v[252:255], v[172:175]\n"
    "v_xor_b32 v117, 0x200, v117\n"
    "ds_read_b64 v[110:111], v117\n"
    "v_mfma_f32_16x16x32_bf16 v[176:179], v[236:239], v[240:243], v[176:179]\n"
    "v_mfma_f32_16x16x32_bf16 v[180:183], v[236:239], v[244:247], v[180:183]\n"
    "v_mfma_f32_16x16x32_bf16 v[184:187], v[236:239], v[248:251], v[184:187]\n"
    "v_mfma_f32_16x16x32_bf16 v[188:191], v[236:239], v[252:255], v[188:191]\n"
    "v_smfmac_f32_16x16x64_bf16 v[64:67], v[44:47], v[232:239], v48\n"
    "s_waitcnt lgkmcnt(0)\n"
    "v_mfma_f32_16x16x32_bf16 v[128:131], v[192:195], v[208:211], v[128:131]\n"
    "ds_read_b64_tr_b16 v[224:225], v40 offset:6144\n"
    "v_mfma_f32_16x16x32_bf16 v[132:135], v[192:195], v[212:215], v[132:135]\n"
    "ds_read_b64_tr_b16 v[226:227], v40 offset:6912\n"
    "v_mfma_f32_16x16x32_bf16 v[136:139], v[192:195], v[216:219], v[136:139]\n"
    "ds_read_b128 v[240:243], v56 offset:0\n"
    "v_mfma_f32_16x16x32_bf16 v[140:143], v[192:195], v[220:223], v[140:143]\n"
    "ds_read_b128 v[244:247], v56 offset:2048\n"
    "v_mfma_f32_16x16x32_bf16 v[144:147], v[196:199], v[208:211], v[144:147]\n"
    "ds_read_b128 v[248:251], v56 offset:4096\n"
    "v_mfma_f32_16x16x32_bf16 v[148:151], v[196:199], v[212:215], v[148:151]\n"
    "ds_read_b128 v[252:255], v56 offset:6144\n"
    "v_mfma_f32_16x16x32_bf16 v[152:155], v[196:199], v[216:219], v[152:155]\n"
    "ds_read_b64_tr_b16 v[228:229], v41 offset:6144\n"
    "v_mfma_f32_16x16x32_bf16 v[156:159], v[196:199], v[220:223], v[156:159]\n"
    "ds_read_b64_tr_b16 v[230:231], v41 offset:6912\n"
    "v_smfmac_f32_16x16x64_bf16 v[60:63], v[44:47], v[192:199], v48\n"
    "ds_read_b64_tr_b16 v[232:233], v42 offset:6144\n"
    "v_mfma_f32_16x16x32_bf16 v[160:163], v[200:203], v[208:211], v[160:163]\n"
    "ds_read_b64_tr_b16 v[234:235], v42 offset:6912\n"
    "v_mfma_f32_16x16x32_bf16 v[164:167], v[200:203], v[212:215], v[164:167]\n"
    "ds_read_b64_tr_b16 v[236:237], v43 offset:6144\n"
    "v_mfma_f32_16x16x32_bf16 v[168:171], v[200:203], v[216:219], v[168:171]\n"
    "ds_read_b64_tr_b16 v[238:239], v43 offset:6912\n"
    "v_mfma_f32_16x16x32_bf16 v[172:175], v[200:203], v[220:223], v[172:175]\n"
    "v_add_u32 v40, -98560, v40\n"
    "v_add_u32 v41, -98560, v41\n"
    "v_add_u32 v42, -98560, v42\n"
    "v_mfma_f32_16x16x32_bf16 v[176:179], v[204:207], v[208:211], v[176:179]\n"
    "v_add_u32 v43, -98560, v43\n"
    "v_add_u32 v55, -98560, v55\n"
    "v_add_u32 v56, -98560, v56\n"
    "v_mfma_f32_16x16x32_bf16 v[180:183], v[204:207], v[212:215], v[180:183]\n"
    "v_mfma_f32_16x16x32_bf16 v[184:187], v[204:207], v[216:219], v[184:187]\n"
    "v_mfma_f32_16x16x32_bf16 v[188:191], v[204:207], v[220:223], v[188:191]\n"
    "v_smfmac_f32_16x16x64_bf16 v[64:67], v[44:47], v[200:207], v48\n"
    "s_waitcnt vmcnt(6) lgkmcnt(0)\n"
    "s_barrier\n"
    "s_lshl_b32 s39, s64, 11\n"
    "s_add_u32 m0, s39, 90368\n"
    "s_add_u32 s38, s39, 24576\n"
    "buffer_load_dwordx4 v21, s[8:11], s38 offen sc1 lds\n"
    "buffer_load_dwordx4 v21, s[8:11], s38 offen offset:1024 sc1 lds\n"
    "s_mul_i32 s38, s2, 3\n"
    "s_add_u32 s38, s38, s73\n"
    "s_mul_i32 s38, s38, 2560\n"
    "s_mul_i32 s39, s2, 3200\n"
    "s_cmp_eq_u32 s64, 7\n"
    "s_cselect_b32 s38, s39, s38\n"
    "s_cselect_b64 s[84:85], s[20:21], s[16:17]\n"
    "s_cselect_b64 s[86:87], s[22:23], s[18:19]\n"
    "v_lshl_add_u32 v17, v21, 3, s38\n"
    "s_mov_b64 exec, 0x1ffffff\n"
    "buffer_load_dword v17, v17, s[84:87], 0 offen\n"
    "s_mov_b64 exec, -1\n"
    "v_mov_b64 v[104:105], -1\n"
    "v_mov_b64 v[106:107], -1\n"
    "buffer_store_dwordx4 v[104:107], v19, s[8:11], s57 offen sc1\n"
    "buffer_store_dwordx2 v[104:105], v20, s[8:11], s58 offen sc1\n"
    "v_mfma_f32_16x16x32_bf16 v[128:131], v[224:227], v[240:243], v[128:131]\n"
    "ds_read_b64_tr_b16 v[192:193], v40 offset:0\n"
    "v_mfma_f32_16x16x32_bf16 v[132:135], v[224:227], v[244:247], v[132:135]\n"
    "ds_read_b64_tr_b16 v[194:195], v40 offset:768\n"
    "v_mfma_f32_16x16x32_bf16 v[136:139], v[224:227], v[248:251], v[136:139]\n"
    "ds_read_b128 v[208:211], v55 offset:0\n"
    "v_mfma_f32_16x16x32_bf16 v[140:143], v[224:227], v[252:255], v[140:143]\n"
    "ds_read_b128 v[212:215], v55 offset:2048\n"
    "v_mfma_f32_16x16x32_bf16 v[144:147], v[228:231], v[240:243], v[144:147]\n"
    "ds_read_b128 v[216:219], v55 offset:4096\n"
    "v_mfma_f32_16x16x32_bf16 v[148:151], v[228:231], v[244:247], v[148:151]\n"
    "ds_read_b128 v[220:223], v55 offset:6144\n"
    "v_mfma_f32_16x16x32_bf16 v[152:155], v[228:231], v[248:251], v[152:155]\n"
    "ds_read_b64_tr_b16 v[196:197], v41 offset:0\n"
    "v_mfma_f32_16x16x32_bf16 v[156:159], v[228:231], v[252:255], v[156:159]\n"
    "ds_read_b64_tr_b16 v[198:199], v41 offset:768\n"
    "v_smfmac_f32_16x16x64_bf16 v[60:63], v[44:47], v[224:231], v48\n"
    "ds_read_b64_tr_b16 v[200:201], v42 offset:0\n"
    "v_mfma_f32_16x16x32_bf16 v[160:163], v[232:235], v[240:243], v[160:163]\n"
    "ds_read_b64_tr_b16 v[202:203], v42 offset:768\n"
    "v_mfma_f32_16x16x32_bf16 v[164:167], v[232:235], v[244:247], v[164:167]\n"
    "ds_read_b64_tr_b16 v[204:205], v43 offset:0\n"
    "v_mfma_f32_16x16x32_bf16 v[168:171], v[232:235], v[248:251], v[168:171]\n"
    "ds_read_b64_tr_b16 v[206:207], v43 offset:768\n"
    "v_mfma_f32_16x16x32_bf16 v[172:175], v[232:235], v[252:255], v[172:175]\n"
    "ds_read2_b64 v[120:123], v23 offset1:16\n"
    "ds_read2_b64 v[124:127], v23 offset0:32 offset1:48\n"
    "v_mfma_f32_16x16x32_bf16 v[176:179], v[236:239], v[240:243], v[176:179]\n"
    "v_mfma_f32_16x16x32_bf16 v[180:183], v[236:239], v[244:247], v[180:183]\n"
    "v_mfma_f32_16x16x32_bf16 v[184:187], v[236:239], v[248:251], v[184:187]\n"
    "v_mfma_f32_16x16x32_bf16 v[188:191], v[236:239], v[252:255], v[188:191]\n"
    "v_smfmac_f32_16x16x64_bf16 v[64:67], v[44:47], v[232:239], v48\n"
    "s_waitcnt lgkmcnt(0)\n"
    "v_mfma_f32_16x16x32_bf16 v[128:131], v[192:195], v[208:211], v[128:131]\n"
    "ds_read_b64_tr_b16 v[224:225], v40 offset:6144\n"
    "v_mul_f32 v50, v121, v121\n"
    "v_mul_f32 v52, v123, v123\n"
    "v_mfma_f32_16x16x32_bf16 v[132:135], v[192:195], v[212:215], v[132:135]\n"
    "ds_read_b64_tr_b16 v[226:227], v40 offset:6912\n"
    "v_mul_f32 v54, v125, v125\n"
    "v_mul_f32 v70, v127, v127\n"
    "v_mfma_f32_16x16x32_bf16 v[136:139], v[192:195], v[216:219], v[136:139]\n"
    "ds_read_b128 v[240:243], v56 offset:0\n"
    "v_mov_b64 v[68:69], v[110:111]\n"
    "v_mfma_f32_16x16x32_bf16 v[140:143], v[192:195], v[220:223], v[140:143]\n"
    "ds_read_b128 v[244:247], v56 offset:2048\n"
    "v_mfma_f32_16x16x32_bf16 v[144:147], v[196:199], v[208:211], v[144:147]\n"
    "ds_read_b128 v[248:251], v56 offset:4096\n"
    "v_mfma_f32_16x16x32_bf16 v[148:151], v[196:199], v[212:215], v[148:151]\n"
    "ds_read_b128 v[252:255], v56 offset:6144\n"
    "v_mfma_f32_16x16x32_bf16 v[152:155], v[196:199], v[216:219], v[152:155]\n"
    "ds_read_b64_tr_b16 v[228:229], v41 offset:6144\n"
    "v_mfma_f32_16x16x32_bf16 v[156:159], v[196:199], v[220:223], v[156:159]\n"
    "ds_read_b64_tr_b16 v[230:231], v41 offset:6912\n"
    "v_smfmac_f32_16x16x64_bf16 v[60:63], v[44:47], v[192:199], v48\n"
    "ds_read_b64_tr_b16 v[232:233], v42 offset:6144\n"
    "v_mfma_f32_16x16x32_bf16 v[160:163], v[200:203], v[208:211], v[160:163]\n"
    "ds_read_b64_tr_b16 v[234:235], v42 offset:6912\n"
    "v_mfma_f32_16x16x32_bf16 v[164:167], v[200:203], v[212:215], v[164:167]\n"
    "ds_read_b64_tr_b16 v[236:237], v43 offset:6144\n"
    "v_mfma_f32_16x16x32_bf16 v[168:171], v[200:203], v[216:219], v[168:171]\n"
    "ds_read_b64_tr_b16 v[238:239], v43 offset:6912\n"
    "v_mfma_f32_16x16x32_bf16 v[172:175], v[200:203], v[220:223], v[172:175]\n"
    "v_mfma_f32_16x16x32_bf16 v[176:179], v[204:207], v[208:211], v[176:179]\n"
    "v_mfma_f32_16x16x32_bf16 v[180:183], v[204:207], v[212:215], v[180:183]\n"
    "v_mfma_f32_16x16x32_bf16 v[184:187], v[204:207], v[216:219], v[184:187]\n"
    "v_mfma_f32_16x16x32_bf16 v[188:191], v[204:207], v[220:223], v[188:191]\n"
    "v_smfmac_f32_16x16x64_bf16 v[64:67], v[44:47], v[200:207], v48\n"
    "s_waitcnt vmcnt(3) lgkmcnt(0)\n"
    "s_barrier\n"
    "ds_read_b128 v[28:31], v118\n"
    "ds_read_b32 v32, v25 offset:32\n"
    "ds_read_b128 v[36:39], v119\n"
    "ds_read_b32 v34, v26 offset:32\n"
    "ds_read_b32 v108, v12 offset:49280\n"
    "ds_read_b128 v[104:107], v11 offset:49280\n"
    "ds_read_b128 v[112:115], v13 offset:49280\n"
    "s_setprio 3\n"
    "v_smfmac_f32_16x16x64_bf16 v[60:63], v[44:47], v[224:231], v48\n"
    "v_smfmac_f32_16x16x64_bf16 v[64:67], v[44:47], v[232:239], v48\n"
    "v_mfma_f32_16x16x32_bf16 v[128:131], v[224:227], v[240:243], v[128:131]\n"
    "v_mfma_f32_16x16x32_bf16 v[144:147], v[228:231], v[240:243], v[144:147]\n"
    "v_mfma_f32_16x16x32_bf16 v[160:163], v[232:235], v[240:243], v[160:163]\n"
    "v_mfma_f32_16x16x32_bf16 v[176:179], v[236:239], v[240:243], v[176:179]\n"
    "s_nop 3\n"
    "v_add_f32 v64, v64, v68\n"
    "v_add_f32 v65, v65, v69\n"
    "v_mfma_f32_16x16x4_f32 v[192:195], v60, v27, 0\n"
    "v_mfma_f32_16x16x4_f32 v[196:199], v61, v27, 0\n"
    "v_mfma_f32_16x16x4_f32 v[200:203], v64, v27, 0\n"
    "v_mfma_f32_16x16x4_f32 v[204:207], v65, v27, 0\n"
    "v_mfma_f32_16x16x32_bf16 v[132:135], v[224:227], v[244:247], v[132:135]\n"
    "v_mfma_f32_16x16x32_bf16 v[148:151], v[228:231], v[244:247], v[148:151]\n"
    "v_mfma_f32_16x16x32_bf16 v[164:167], v[232:235], v[244:247], v[164:167]\n"
    "v_mfma_f32_16x16x32_bf16 v[180:183], v[236:239], v[244:247], v[180:183]\n"
    "v_mfma_f32_16x16x32_bf16 v[136:139], v[224:227], v[248:251], v[136:139]\n"
    "v_mfma_f32_16x16x32_bf16 v[152:155], v[228:231], v[248:251], v[152:155]\n"
    "v_mfma_f32_16x16x32_bf16 v[168:171], v[232:235], v[248:251], v[168:171]\n"
    "v_mfma_f32_16x16x32_bf16 v[184:187], v[236:239], v[248:251], v[184:187]\n"
    "v_mfma_f32_16x16x32_bf16 v[140:143], v[224:227], v[252:255], v[140:143]\n"
    "v_mfma_f32_16x16x32_bf16 v[156:159], v[228:231], v[252:255], v[156:159]\n"
    "v_mfma_f32_16x16x32_bf16 v[172:175], v[232:235], v[252:255], v[172:175]\n"
    "v_mfma_f32_16x16x32_bf16 v[188:191], v[236:239], v[252:255], v[188:191]\n"
    "v_pk_fma_f32 v[128:129], v[192:193], v[120:121], v[128:129] op_sel_hi:[1,0,1]\n"
    "v_pk_fma_f32 v[130:131], v[194:195], v[120:121], v[130:131] op_sel_hi:[1,0,1]\n"
    "v_pk_fma_f32 v[144:145], v[196:197], v[120:121], v[144:145] op_sel_hi:[1,0,1]\n"
    "v_pk_fma_f32 v[146:147], v[198:199], v[120:121], v[146:147] op_sel_hi:[1,0,1]\n"
    "v_pk_fma_f32 v[160:161], v[200:201], v[120:121], v[160:161] op_sel_hi:[1,0,1]\n"
    "v_pk_fma_f32 v[162:163], v[202:203], v[120:121], v[162:163] op_sel_hi:[1,0,1]\n"
    "v_pk_fma_f32 v[176:177], v[204:205], v[120:121], v[176:177] op_sel_hi:[1,0,1]\n"
    "v_pk_fma_f32 v[178:179], v[206:207], v[120:121], v[178:179] op_sel_hi:[1,0,1]\n"
    "v_pk_mul_f32 v[128:129], v[128:129], v[50:51] op_sel_hi:[1,0]\n"
    "v_pk_mul_f32 v[130:131], v[130:131], v[50:51] op_sel_hi:[1,0]\n"
    "v_pk_mul_f32 v[144:145], v[144:145], v[50:51] op_sel_hi:[1,0]\n"
    "v_pk_mul_f32 v[146:147], v[146:147], v[50:51] op_sel_hi:[1,0]\n"
    "v_pk_mul_f32 v[128:129], v[128:129], v[160:161]\n"
    "v_pk_mul_f32 v[130:131], v[130:131], v[162:163]\n"
    "v_pk_mul_f32 v[144:145], v[144:145], v[176:177]\n"
    "v_pk_mul_f32 v[146:147], v[146:147], v[178:179]\n"
    "v_cvt_pk_bf16_f32 v208, v128, v129\n"
    "v_cvt_pk_bf16_f32 v209, v130, v131\n"
    "v_cvt_pk_bf16_f32 v210, v144, v145\n"
    "v_cvt_pk_bf16_f32 v211, v146, v147\n"
    "buffer_store_dwordx4 v[208:211], v21, s[8:11], s60 offen sc1\n"
    "s_add_u32 s60, s60, 49152\n"
    "v_pk_fma_f32 v[132:133], v[192:193], v[122:123], v[132:133] op_sel_hi:[1,0,1]\n"
    "v_pk_fma_f32 v[134:135], v[194:195], v[122:123], v[134:135] op_sel_hi:[1,0,1]\n"
    "v_pk_fma_f32 v[148:149], v[196:197], v[122:123], v[148:149] op_sel_hi:[1,0,1]\n"
    "v_pk_fma_f32 v[150:151], v[198:199], v[122:123], v[150:151] op_sel_hi:[1,0,1]\n"
    "v_pk_fma_f32 v[164:165], v[200:201], v[122:123], v[164:165] op_sel_hi:[1,0,1]\n"
    "v_pk_fma_f32 v[166:167], v[202:203], v[122:123], v[166:167] op_sel_hi:[1,0,1]\n"
    "v_pk_fma_f32 v[180:181], v[204:205], v[122:123], v[180:181] op_sel_hi:[1,0,1]\n"
    "v_pk_fma_f32 v[182:183], v[206:207], v[122:123], v[182:183] op_sel_hi:[1,0,1]\n"
    "v_pk_mul_f32 v[132:133], v[132:133], v[52:53] op_sel_hi:[1,0]\n"
    "v_pk_mul_f32 v[134:135], v[134:135], v[52:53] op_sel_hi:[1,0]\n"
    "v_pk_mul_f32 v[148:149], v[148:149], v[52:53] op_sel_hi:[1,0]\n"
    "v_pk_mul_f32 v[150:151], v[150:151], v[52:53] op_sel_hi:[1,0]\n"
    "v_pk_mul_f32 v[132:133], v[132:133], v[164:165]\n"
    "v_pk_mul_f32 v[134:135], v[134:135], v[166:167]\n"
    "v_pk_mul_f32 v[148:149], v[148:149], v[180:181]\n"
    "v_pk_mul_f32 v[150:151], v[150:151], v[182:183]\n"
    "v_cvt_pk_bf16_f32 v216, v132, v133\n"
    "v_cvt_pk_bf16_f32 v217, v134, v135\n"
    "v_cvt_pk_bf16_f32 v218, v148, v149\n"
    "v_cvt_pk_bf16_f32 v219, v150, v151\n"
    "buffer_store_dwordx4 v[216:219], v21, s[8:11], s60 offen sc1\n"
    "s_add_u32 s60, s60, 49152\n"
    "v_pk_fma_f32 v[136:137], v[192:193], v[124:125], v[136:137] op_sel_hi:[1,0,1]\n"
    "v_pk_fma_f32 v[138:139], v[194:195], v[124:125], v[138:139] op_sel_hi:[1,0,1]\n"
    "v_pk_fma_f32 v[152:153], v[196:197], v[124:125], v[152:153] op_sel_hi:[1,0,1]\n"
    "v_pk_fma_f32 v[154:155], v[198:199], v[124:125], v[154:155] op_sel_hi:[1,0,1]\n"
    "v_pk_fma_f32 v[168:169], v[200:201], v[124:125], v[168:169] op_sel_hi:[1,0,1]\n"
    "v_pk_fma_f32 v[170:171], v[202:203], v[124:125], v[170:171] op_sel_hi:[1,0,1]\n"
    "v_pk_fma_f32 v[184:185], v[204:205], v[124:125], v[184:185] op_sel_hi:[1,0,1]\n"
    "v_pk_fma_f32 v[186:187], v[206:207], v[124:125], v[186:187] op_sel_hi:[1,0,1]\n"
    "v_pk_mul_f32 v[136:137], v[136:137], v[54:55] op_sel_hi:[1,0]\n"
    "v_pk_mul_f32 v[138:139], v[138:139], v[54:55] op_sel_hi:[1,0]\n"
    "v_pk_mul_f32 v[152:153], v[152:153], v[54:55] op_sel_hi:[1,0]\n"
    "v_pk_mul_f32 v[154:155], v[154:155], v[54:55] op_sel_hi:[1,0]\n"
    "v_pk_mul_f32 v[136:137], v[136:137], v[168:169]\n"
    "v_pk_mul_f32 v[138:139], v[138:139], v[170:171]\n"
    "v_pk_mul_f32 v[152:153], v[152:153], v[184:185]\n"
    "v_pk_mul_f32 v[154:155], v[154:155], v[186:187]\n"
    "v_cvt_pk_bf16_f32 v208, v136, v137\n"
    "v_cvt_pk_bf16_f32 v209, v138, v139\n"
    "v_cvt_pk_bf16_f32 v210, v152, v153\n"
    "v_cvt_pk_bf16_f32 v211, v154, v155\n"
    "buffer_store_dwordx4 v[208:211], v21, s[8:11], s60 offen sc1\n"
    "s_add_u32 s60, s60, 49152\n"
    "v_pk_fma_f32 v[140:141], v[192:193], v[126:127], v[140:141] op_sel_hi:[1,0,1]\n"
    "v_pk_fma_f32 v[142:143], v[194:195], v[126:127], v[142:143] op_sel_hi:[1,0,1]\n"
    "v_pk_fma_f32 v[156:157], v[196:197], v[126:127], v[156:157] op_sel_hi:[1,0,1]\n"
    "v_pk_fma_f32 v[158:159], v[198:199], v[126:127], v[158:159] op_sel_hi:[1,0,1]\n"
    "v_pk_fma_f32 v[172:173], v[200:201], v[126:127], v[172:173] op_sel_hi:[1,0,1]\n"
    "v_pk_fma_f32 v[174:175], v[202:203], v[126:127], v[174:175] op_sel_hi:[1,0,1]\n"
    "v_pk_fma_f32 v[188:189], v[204:205], v[126:127], v[188:189] op_sel_hi:[1,0,1]\n"
    "v_pk_fma_f32 v[190:191], v[206:207], v[126:127], v[190:191] op_sel_hi:[1,0,1]\n"
    "v_pk_mul_f32 v[140:141], v[140:141], v[70:71] op_sel_hi:[1,0]\n"
    "v_pk_mul_f32 v[142:143], v[142:143], v[70:71] op_sel_hi:[1,0]\n"
    "v_pk_mul_f32 v[156:157], v[156:157], v[70:71] op_sel_hi:[1,0]\n"
    "v_pk_mul_f32 v[158:159], v[158:159], v[70:71] op_sel_hi:[1,0]\n"
    "v_pk_mul_f32 v[140:141], v[140:141], v[172:173]\n"
    "v_pk_mul_f32 v[142:143], v[142:143], v[174:175]\n"
    "v_pk_mul_f32 v[156:157], v[156:157], v[188:189]\n"
    "v_pk_mul_f32 v[158:159], v[158:159], v[190:191]\n"
    "v_cvt_pk_bf16_f32 v216, v140, v141\n"
    "v_cvt_pk_bf16_f32 v217, v142, v143\n"
    "v_cvt_pk_bf16_f32 v218, v156, v157\n"
    "v_cvt_pk_bf16_f32 v219, v158, v159\n"
    "buffer_store_dwordx4 v[216:219], v21, s[8:11], s60 offen sc1\n"
    "s_add_u32 s60, s60, 49152\n"
    "s_branch ATT_%=\n"
    "VL_%=:\n"
    "s_setprio 1\n"
    "s_cmp_lt_u32 s64, 2\n"
    "s_cbranch_scc1 VQ_%=\n"
    "ds_read_b64_tr_b16 v[192:193], v40 offset:0\n"
    "ds_read_b64_tr_b16 v[194:195], v40 offset:768\n"
    "ds_read_b128 v[208:211], v55 offset:0\n"
    "ds_read_b128 v[212:215], v55 offset:2048\n"
    "ds_read_b128 v[216:219], v55 offset:4096\n"
    "ds_read_b128 v[220:223], v55 offset:6144\n"
    "ds_read_b64_tr_b16 v[196:197], v41 offset:0\n"
    "ds_read_b64_tr_b16 v[198:199], v41 offset:768\n"
    "ds_read_b64_tr_b16 v[200:201], v42 offset:0\n"
    "ds_read_b64_tr_b16 v[202:203], v42 offset:768\n"
    "ds_read_b64_tr_b16 v[204:205], v43 offset:0\n"
    "ds_read_b64_tr_b16 v[206:207], v43 offset:768\n"
    "s_add_u32 m0, s76, 49280\n"
    "s_add_u32 s38, s79, 81920\n"
    "buffer_load_dwordx4 v54, s[12:15], s38 offen lds\n"
    "s_waitcnt lgkmcnt(10)\n"
    "v_readfirstlane_b32 s37, v58\n"
    "s_nop 3\n"
    "s_add_u32 s37, s37, 1\n"
    "s_waitcnt lgkmcnt(0)\n"
    "v_mfma_f32_16x16x32_bf16 v[128:131], v[192:195], v[208:211], 0\n"
    "ds_read_b64_tr_b16 v[224:225], v40 offset:6144\n"
    "v_mfma_f32_16x16x32_bf16 v[132:135], v[192:195], v[212:215], 0\n"
    "ds_read_b64_tr_b16 v[226:227], v40 offset:6912\n"
    "v_mfma_f32_16x16x32_bf16 v[136:139], v[192:195], v[216:219], 0\n"
    "ds_read_b128 v[240:243], v56 offset:0\n"
    "v_mfma_f32_16x16x32_bf16 v[140:143], v[192:195], v[220:223], 0\n"
    "ds_read_b128 v[244:247], v56 offset:2048\n"
    "v_mfma_f32_16x16x32_bf16 v[144:147], v[196:199], v[208:211], 0\n"
    "ds_read_b128 v[248:251], v56 offset:4096\n"
    "v_mfma_f32_16x16x32_bf16 v[148:151], v[196:199], v[212:215], 0\n"
    "ds_read_b128 v[252:255], v56 offset:6144\n"
    "v_mfma_f32_16x16x32_bf16 v[152:155], v[196:199], v[216:219], 0\n"
    "ds_read_b64_tr_b16 v[228:229], v41 offset:6144\n"
    "v_mfma_f32_16x16x32_bf16 v[156:159], v[196:199], v[220:223], 0\n"
    "ds_read_b64_tr_b16 v[230:231], v41 offset:6912\n"
    "v_smfmac_f32_16x16x64_bf16 v[60:63], v[44:47], v[192:199], v48\n"
    "ds_read_b64_tr_b16 v[232:233], v42 offset:6144\n"
    "v_mfma_f32_16x16x32_bf16 v[160:163], v[200:203], v[208:211], 0\n"
    "ds_read_b64_tr_b16 v[234:235], v42 offset:6912\n"
    "v_mfma_f32_16x16x32_bf16 v[164:167], v[200:203], v[212:215], 0\n"
    "ds_read_b64_tr_b16 v[236:237], v43 offset:6144\n"
    "v_mfma_f32_16x16x32_bf16 v[168:171], v[200:203], v[216:219], 0\n"
    "ds_read_b64_tr_b16 v[238:239], v43 offset:6912\n"
    "v_mfma_f32_16x16x32_bf16 v[172:175], v[200:203], v[220:223], 0\n"
    "v_add_u32 v40, 49280, v40\n"
    "v_add_u32 v41, 49280, v41\n"
    "v_add_u32 v42, 49280, v42\n"
    "v_mfma_f32_16x16x32_bf16 v[176:179], v[204:207], v[208:211], 0\n"
    "v_add_u32 v43, 49280, v43\n"
    "v_add_u32 v55, 49280, v55\n"
    "v_add_u32 v56, 49280, v56\n"
    "v_mfma_f32_16x16x32_bf16 v[180:183], v[204:207], v[212:215], 0\n"
    "v_mfma_f32_16x16x32_bf16 v[184:187], v[204:207], v[216:219], 0\n"
    "v_mfma_f32_16x16x32_bf16 v[188:191], v[204:207], v[220:223], 0\n"
    "s_add_u32 m0, s74, 98560\n"
    "s_add_u32 s38, s77, 256\n"
    "buffer_load_dwordx4 v49, s[4:7], s38 offen lds\n"
    "buffer_load_dwordx4 v50, s[4:7], s38 offen offset:1024 lds\n"
    "s_add_u32 m0, s75, 98560\n"
    "s_add_u32 s38, s78, 393216\n"
    "buffer_load_dwordx4 v51, s[28:31], s38 offen lds\n"
    "buffer_load_dwordx4 v52, s[28:31], s38 offen offset:1024 lds\n"
    "buffer_load_dwordx4 v53, s[28:31], s38 offen offset:2048 lds\n"
    "s_add_u32 m0, s76, 98560\n"
    "s_add_u32 s38, s79, 163840\n"
    "buffer_load_dwordx4 v54, s[12:15], s38 offen lds\n"
    "s_waitcnt vmcnt(6) lgkmcnt(0)\n"
    "s_barrier\n"
    "v_mfma_f32_16x16x32_bf16 v[128:131], v[224:227], v[240:243], v[128:131]\n"
    "ds_read_b64_tr_b16 v[192:193], v40 offset:0\n"
    "v_mfma_f32_16x16x32_bf16 v[132:135], v[224:227], v[244:247], v[132:135]\n"
    "ds_read_b64_tr_b16 v[194:195], v40 offset:768\n"
    "v_mfma_f32_16x16x32_bf16 v[136:139], v[224:227], v[248:251], v[136:139]\n"
    "ds_read_b128 v[208:211], v55 offset:0\n"
    "s_lshr_b32 s80, s64, 1\n"
    "v_mfma_f32_16x16x32_bf16 v[140:143], v[224:227], v[252:255], v[140:143]\n"
    "ds_read_b128 v[212:215], v55 offset:2048\n"
    "s_add_u32 s42, s80, s65\n"
    "v_mfma_f32_16x16x32_bf16 v[144:147], v[228:231], v[240:243], v[144:147]\n"
    "ds_read_b128 v[216:219], v55 offset:4096\n"
    "s_and_b32 s42, s42, 3\n"
    "v_mfma_f32_16x16x32_bf16 v[148:151], v[228:231], v[244:247], v[148:151]\n"
    "ds_read_b128 v[220:223], v55 offset:6144\n"
    "s_mul_i32 s43, s42, 5120\n"
    "v_mfma_f32_16x16x32_bf16 v[152:155], v[228:231], v[248:251], v[152:155]\n"
    "ds_read_b64_tr_b16 v[196:197], v41 offset:0\n"
    "s_lshl_b32 s81, s41, 11\n"
    "v_mfma_f32_16x16x32_bf16 v[156:159], v[228:231], v[252:255], v[156:159]\n"
    "ds_read_b64_tr_b16 v[198:199], v41 offset:768\n"
    "s_add_u32 s43, s43, s81\n"
    "v_smfmac_f32_16x16x64_bf16 v[60:63], v[44:47], v[224:231], v48\n"
    "ds_read_b64_tr_b16 v[200:201], v42 offset:0\n"
    "s_and_b32 s81, s65, 7\n"
    "v_mfma_f32_16x16x32_bf16 v[160:163], v[232:235], v[240:243], v[160:163]\n"
    "ds_read_b64_tr_b16 v[202:203], v42 offset:768\n"
    "s_mul_i32 s81, s81, 160\n"
    "v_mfma_f32_16x16x32_bf16 v[164:167], v[232:235], v[244:247], v[164:167]\n"
    "ds_read_b64_tr_b16 v[204:205], v43 offset:0\n"
    "s_mul_i32 s44, s42, 491520\n"
    "v_mfma_f32_16x16x32_bf16 v[168:171], v[232:235], v[248:251], v[168:171]\n"
    "ds_read_b64_tr_b16 v[206:207], v43 offset:768\n"
    "s_add_u32 s44, s44, s81\n"
    "v_mfma_f32_16x16x32_bf16 v[172:175], v[232:235], v[252:255], v[172:175]\n"
    "s_mul_i32 s45, s42, 204800\n"
    "v_mfma_f32_16x16x32_bf16 v[176:179], v[236:239], v[240:243], v[176:179]\n"
    "s_add_u32 s45, s45, s81\n"
    "v_mfma_f32_16x16x32_bf16 v[180:183], v[236:239], v[244:247], v[180:183]\n"
    "s_lshr_b32 s82, s65, 3\n"
    "v_mfma_f32_16x16x32_bf16 v[184:187], v[236:239], v[248:251], v[184:187]\n"
    "s_lshl2_add_u32 s82, s66, s82\n"
    "v_mfma_f32_16x16x32_bf16 v[188:191], v[236:239], v[252:255], v[188:191]\n"
    "s_xor_b32 s82, s82, 16\n"
    "s_waitcnt lgkmcnt(0)\n"
    "v_mfma_f32_16x16x32_bf16 v[128:131], v[192:195], v[208:211], v[128:131]\n"
    "ds_read_b64_tr_b16 v[224:225], v40 offset:6144\n"
    "v_mfma_f32_16x16x32_bf16 v[132:135], v[192:195], v[212:215], v[132:135]\n"
    "ds_read_b64_tr_b16 v[226:227], v40 offset:6912\n"
    "v_mfma_f32_16x16x32_bf16 v[136:139], v[192:195], v[216:219], v[136:139]\n"
    "ds_read_b128 v[240:243], v56 offset:0\n"
    "s_lshr_b32 s46, s82, 1\n"
    "v_mfma_f32_16x16x32_bf16 v[140:143], v[192:195], v[220:223], v[140:143]\n"
    "ds_read_b128 v[244:247], v56 offset:2048\n"
    "s_lshl2_add_u32 s46, s46, s42\n"
    "v_mfma_f32_16x16x32_bf16 v[144:147], v[196:199], v[208:211], v[144:147]\n"
    "ds_read_b128 v[248:251], v56 offset:4096\n"
    "s_lshl_b32 s46, s46, 5\n"
    "v_mfma_f32_16x16x32_bf16 v[148:151], v[196:199], v[212:215], v[148:151]\n"
    "ds_read_b128 v[252:255], v56 offset:6144\n"
    "s_add_u32 s46, s46, 2048\n"
    "v_mfma_f32_16x16x32_bf16 v[152:155], v[196:199], v[216:219], v[152:155]\n"
    "ds_read_b64_tr_b16 v[228:229], v41 offset:6144\n"
    "s_lshl1_add_u32 s82, s82, s41\n"
    "v_mfma_f32_16x16x32_bf16 v[156:159], v[196:199], v[220:223], v[156:159]\n"
    "ds_read_b64_tr_b16 v[230:231], v41 offset:6912\n"
    "s_and_b32 s83, s42, 1\n"
    "v_smfmac_f32_16x16x64_bf16 v[60:63], v[44:47], v[192:199], v48\n"
    "ds_read_b64_tr_b16 v[232:233], v42 offset:6144\n"
    "s_lshl1_add_u32 s82, s82, s83\n"
    "v_mfma_f32_16x16x32_bf16 v[160:163], v[200:203], v[208:211], v[160:163]\n"
    "ds_read_b64_tr_b16 v[234:235], v42 offset:6912\n"
    "s_mul_i32 s48, s82, 20480\n"
    "v_mfma_f32_16x16x32_bf16 v[164:167], v[200:203], v[212:215], v[164:167]\n"
    "ds_read_b64_tr_b16 v[236:237], v43 offset:6144\n"
    "s_add_u32 s48, s48, s81\n"
    "v_mfma_f32_16x16x32_bf16 v[168:171], v[200:203], v[216:219], v[168:171]\n"
    "ds_read_b64_tr_b16 v[238:239], v43 offset:6912\n"
    "s_lshr_b32 s47, s42, 1\n"
    "v_mfma_f32_16x16x32_bf16 v[172:175], v[200:203], v[220:223], v[172:175]\n"
    "v_add_u32 v40, 49280, v40\n"
    "v_add_u32 v41, 49280, v41\n"
    "v_add_u32 v42, 49280, v42\n"
    "v_mfma_f32_16x16x32_bf16 v[176:179], v[204:207], v[208:211], v[176:179]\n"
    "v_add_u32 v43, 49280, v43\n"
    "v_add_u32 v55, 49280, v55\n"
    "v_add_u32 v56, 49280, v56\n"
    "v_mfma_f32_16x16x32_bf16 v[180:183], v[204:207], v[212:215], v[180:183]\n"
    "s_lshl_b32 s47, s47, 6\n"
    "v_mfma_f32_16x16x32_bf16 v[184:187], v[204:207], v[216:219], v[184:187]\n"
    "s_add_u32 s47, s47, s48\n"
    "v_mfma_f32_16x16x32_bf16 v[188:191], v[204:207], v[220:223], v[188:191]\n"
    "s_lshl2_add_u32 s49, s82, s42\n"
    "s_add_u32 m0, s74, 0\n"
    "s_add_u32 s38, s77, 384\n"
    "buffer_load_dwordx4 v49, s[4:7], s38 offen lds\n"
    "buffer_load_dwordx4 v50, s[4:7], s38 offen offset:1024 lds\n"
    "s_add_u32 m0, s75, 0\n"
    "s_add_u32 s38, s78, 589824\n"
    "buffer_load_dwordx4 v51, s[28:31], s38 offen lds\n"
    "buffer_load_dwordx4 v52, s[28:31], s38 offen offset:1024 lds\n"
    "buffer_load_dwordx4 v53, s[28:31], s38 offen offset:2048 lds\n"
    "s_add_u32 m0, s76, 0\n"
    "s_add_u32 s38, s79, 245760\n"
    "buffer_load_dwordx4 v54, s[12:15], s38 offen lds\n"
    "s_xor_b32 s50, s49, 4\n"
    "s_mul_i32 s51, s49, 12288\n"
    "s_mul_i32 s52, s50, 12288\n"
    "s_add_u32 s51, s51, 4194304\n"
    "s_add_u32 s52, s52, 4194304\n"
    "s_lshl_b32 s53, s49, 12\n"
    "s_lshl_b32 s54, s50, 12\n"
    "s_add_u32 s53, s53, 1048576\n"
    "s_add_u32 s54, s54, 1048576\n"
    "s_lshl_b32 s49, s49, 10\n"
    "s_lshl_b32 s50, s50, 10\n"
    "s_add_u32 s49, s49, 262144\n"
    "s_waitcnt vmcnt(6) lgkmcnt(0)\n"
    "s_barrier\n"
    "v_mfma_f32_16x16x32_bf16 v[128:131], v[224:227], v[240:243], v[128:131]\n"
    "ds_read_b64_tr_b16 v[192:193], v40 offset:0\n"
    "v_mfma_f32_16x16x32_bf16 v[132:135], v[224:227], v[244:247], v[132:135]\n"
    "ds_read_b64_tr_b16 v[194:195], v40 offset:768\n"
    "v_mfma_f32_16x16x32_bf16 v[136:139], v[224:227], v[248:251], v[136:139]\n"
    "ds_read_b128 v[208:211], v55 offset:0\n"
    "s_add_u32 s50, s50, 262144\n"
    "v_mfma_f32_16x16x32_bf16 v[140:143], v[224:227], v[252:255], v[140:143]\n"
    "ds_read_b128 v[212:215], v55 offset:2048\n"
    "s_lshl1_add_u32 s56, s41, s83\n"
    "v_mfma_f32_16x16x32_bf16 v[144:147], v[228:231], v[240:243], v[144:147]\n"
    "ds_read_b128 v[216:219], v55 offset:4096\n"
    "s_lshl2_add_u32 s56, s56, s42\n"
    "v_mfma_f32_16x16x32_bf16 v[148:151], v[228:231], v[244:247], v[148:151]\n"
    "ds_read_b128 v[220:223], v55 offset:6144\n"
    "s_lshl_b32 s56, s56, 10\n"
    "v_mfma_f32_16x16x32_bf16 v[152:155], v[228:231], v[248:251], v[152:155]\n"
    "ds_read_b64_tr_b16 v[196:197], v41 offset:0\n"
    "s_add_u32 s56, s56, 49152\n"
    "v_mfma_f32_16x16x32_bf16 v[156:159], v[228:231], v[252:255], v[156:159]\n"
    "ds_read_b64_tr_b16 v[198:199], v41 offset:768\n"
    "s_lshl2_add_u32 s80, s41, s42\n"
    "v_smfmac_f32_16x16x64_bf16 v[60:63], v[44:47], v[224:231], v48\n"
    "ds_read_b64_tr_b16 v[200:201], v42 offset:0\n"
    "s_mul_i32 s55, s80, 6144\n"
    "v_mfma_f32_16x16x32_bf16 v[160:163], v[232:235], v[240:243], v[160:163]\n"
    "ds_read_b64_tr_b16 v[202:203], v42 offset:768\n"
    "s_xor_b32 s88, s80, 1\n"
    "v_mfma_f32_16x16x32_bf16 v[164:167], v[232:235], v[244:247], v[164:167]\n"
    "ds_read_b64_tr_b16 v[204:205], v43 offset:0\n"
    "s_mul_i32 s88, s88, 6144\n"
    "v_mfma_f32_16x16x32_bf16 v[168:171], v[232:235], v[248:251], v[168:171]\n"
    "ds_read_b64_tr_b16 v[206:207], v43 offset:768\n"
    "s_xor_b32 s89, s80, 2\n"
    "v_mfma_f32_16x16x32_bf16 v[172:175], v[232:235], v[252:255], v[172:175]\n"
    "s_mul_i32 s89, s89, 6144\n"
    "v_mfma_f32_16x16x32_bf16 v[176:179], v[236:239], v[240:243], v[176:179]\n"
    "s_xor_b32 s96, s80, 3\n"
    "v_mfma_f32_16x16x32_bf16 v[180:183], v[236:239], v[244:247], v[180:183]\n"
    "s_mul_i32 s96, s96, 6144\n"
    "v_mfma_f32_16x16x32_bf16 v[184:187], v[236:239], v[248:251], v[184:187]\n"
    "s_lshr_b32 s80, s64, 1\n"
    "v_mfma_f32_16x16x32_bf16 v[188:191], v[236:239], v[252:255], v[188:191]\n"
    "s_lshl2_add_u32 s81, s67, s80\n"
    "s_waitcnt lgkmcnt(0)\n"
    "v_mfma_f32_16x16x32_bf16 v[128:131], v[192:195], v[208:211], v[128:131]\n"
    "ds_read_b64_tr_b16 v[224:225], v40 offset:6144\n"
    "v_mfma_f32_16x16x32_bf16 v[132:135], v[192:195], v[212:215], v[132:135]\n"
    "ds_read_b64_tr_b16 v[226:227], v40 offset:6912\n"
    "v_mfma_f32_16x16x32_bf16 v[136:139], v[192:195], v[216:219], v[136:139]\n"
    "ds_read_b128 v[240:243], v56 offset:0\n"
    "s_lshl_b32 s81, s81, 5\n"
    "v_mfma_f32_16x16x32_bf16 v[140:143], v[192:195], v[220:223], v[140:143]\n"
    "ds_read_b128 v[244:247], v56 offset:2048\n"
    "s_add_u32 s81, s81, s68\n"
    "v_mfma_f32_16x16x32_bf16 v[144:147], v[196:199], v[208:211], v[144:147]\n"
    "ds_read_b128 v[248:251], v56 offset:4096\n"
    "s_lshl1_add_u32 s81, s81, s41\n"
    "v_mfma_f32_16x16x32_bf16 v[148:151], v[196:199], v[212:215], v[148:151]\n"
    "ds_read_b128 v[252:255], v56 offset:6144\n"
    "s_lshl_b32 s57, s81, 9\n"
    "v_mfma_f32_16x16x32_bf16 v[152:155], v[196:199], v[216:219], v[152:155]\n"
    "ds_read_b64_tr_b16 v[228:229], v41 offset:6144\n"
    "s_add_u32 s57, s57, 1048576\n"
    "v_mfma_f32_16x16x32_bf16 v[156:159], v[196:199], v[220:223], v[156:159]\n"
    "ds_read_b64_tr_b16 v[230:231], v41 offset:6912\n"
    "s_lshl_b32 s58, s81, 7\n"
    "v_smfmac_f32_16x16x64_bf16 v[60:63], v[44:47], v[192:199], v48\n"
    "ds_read_b64_tr_b16 v[232:233], v42 offset:6144\n"
    "s_add_u32 s58, s58, 262144\n"
    "v_mfma_f32_16x16x32_bf16 v[160:163], v[200:203], v[208:211], v[160:163]\n"
    "ds_read_b64_tr_b16 v[234:235], v42 offset:6912\n"
    "s_lshl4_add_u32 s59, s67, s68\n"
    "v_mfma_f32_16x16x32_bf16 v[164:167], v[200:203], v[212:215], v[164:167]\n"
    "ds_read_b64_tr_b16 v[236:237], v43 offset:6144\n"
    "s_lshl_b32 s59, s59, 3\n"
    "v_mfma_f32_16x16x32_bf16 v[168:171], v[200:203], v[216:219], v[168:171]\n"
    "ds_read_b64_tr_b16 v[238:239], v43 offset:6912\n"
    "s_add_u32 s59, s59, 2048\n"
    "v_mfma_f32_16x16x32_bf16 v[172:175], v[200:203], v[220:223], v[172:175]\n"
    "v_add_u32 v40, -98560, v40\n"
    "v_add_u32 v41, -98560, v41\n"
    "v_add_u32 v42, -98560, v42\n"
    "v_mfma_f32_16x16x32_bf16 v[176:179], v[204:207], v[208:211], v[176:179]\n"
    "v_add_u32 v43, -98560, v43\n"
    "v_add_u32 v55, -98560, v55\n"
    "v_add_u32 v56, -98560, v56\n"
    "v_mfma_f32_16x16x32_bf16 v[180:183], v[204:207], v[212:215], v[180:183]\n"
    "s_lshl1_add_u32 s60, s67, s41\n"
    "v_mfma_f32_16x16x32_bf16 v[184:187], v[204:207], v[216:219], v[184:187]\n"
    "s_mul_i32 s60, s60, 192\n"
    "v_mfma_f32_16x16x32_bf16 v[188:191], v[204:207], v[220:223], v[188:191]\n"
    "s_mul_i32 s81, s68, 3\n"
    "s_add_u32 m0, s74, 49280\n"
    "s_add_u32 s38, s77, 512\n"
    "buffer_load_dwordx4 v49, s[4:7], s38 offen lds\n"
    "buffer_load_dwordx4 v50, s[4:7], s38 offen offset:1024 lds\n"
    "s_add_u32 m0, s75, 49280\n"
    "s_add_u32 s38, s78, 786432\n"
    "buffer_load_dwordx4 v51, s[28:31], s38 offen lds\n"
    "buffer_load_dwordx4 v52, s[28:31], s38 offen offset:1024 lds\n"
    "buffer_load_dwordx4 v53, s[28:31], s38 offen offset:2048 lds\n"
    "s_add_u32 m0, s76, 49280\n"
    "s_add_u32 s38, s79, 327680\n"
    "buffer_load_dwordx4 v54, s[12:15], s38 offen lds\n"
    "s_add_u32 s60, s60, s81\n"
    "s_add_u32 s60, s60, s80\n"
    "s_lshl_b32 s60, s60, 10\n"
    "s_add_u32 s60, s60, 4193280\n"
    "s_mul_i32 s61, s64, 3072\n"
    "s_add_u32 s62, s61, 38912\n"
    "s_add_u32 s61, s61, 49280\n"
    "v_mov_b64 v[72:73], 0\n"
    "v_mov_b64 v[74:75], 0\n"
    "v_mov_b64 v[76:77], 0\n"
    "v_mov_b64 v[78:79], 0\n"
    "v_mov_b64 v[80:81], 0\n"
    "s_waitcnt vmcnt(6) lgkmcnt(0)\n"
    "s_barrier\n"
    "v_mfma_f32_16x16x32_bf16 v[128:131], v[224:227], v[240:243], v[128:131]\n"
    "ds_read_b64_tr_b16 v[192:193], v40 offset:0\n"
    "v_mfma_f32_16x16x32_bf16 v[132:135], v[224:227], v[244:247], v[132:135]\n"
    "ds_read_b64_tr_b16 v[194:195], v40 offset:768\n"
    "v_mfma_f32_16x16x32_bf16 v[136:139], v[224:227], v[248:251], v[136:139]\n"
    "ds_read_b128 v[208:211], v55 offset:0\n"
    "v_mov_b64 v[82:83], 0\n"
    "v_mfma_f32_16x16x32_bf16 v[140:143], v[224:227], v[252:255], v[140:143]\n"
    "ds_read_b128 v[212:215], v55 offset:2048\n"
    "v_mov_b64 v[84:85], 0\n"
    "v_mfma_f32_16x16x32_bf16 v[144:147], v[228:231], v[240:243], v[144:147]\n"
    "ds_read_b128 v[216:219], v55 offset:4096\n"
    "v_mov_b64 v[86:87], 0\n"
    "v_mfma_f32_16x16x32_bf16 v[148:151], v[228:231], v[244:247], v[148:151]\n"
    "ds_read_b128 v[220:223], v55 offset:6144\n"
    "v_mov_b64 v[88:89], 0\n"
    "v_mfma_f32_16x16x32_bf16 v[152:155], v[228:231], v[248:251], v[152:155]\n"
    "ds_read_b64_tr_b16 v[196:197], v41 offset:0\n"
    "v_mov_b64 v[90:91], 0\n"
    "v_mfma_f32_16x16x32_bf16 v[156:159], v[228:231], v[252:255], v[156:159]\n"
    "ds_read_b64_tr_b16 v[198:199], v41 offset:768\n"
    "v_mov_b64 v[92:93], 0\n"
    "v_smfmac_f32_16x16x64_bf16 v[60:63], v[44:47], v[224:231], v48\n"
    "ds_read_b64_tr_b16 v[200:201], v42 offset:0\n"
    "v_mov_b64 v[94:95], 0\n"
    "v_mfma_f32_16x16x32_bf16 v[160:163], v[232:235], v[240:243], v[160:163]\n"
    "ds_read_b64_tr_b16 v[202:203], v42 offset:768\n"
    "v_mov_b64 v[96:97], 0\n"
    "v_mfma_f32_16x16x32_bf16 v[164:167], v[232:235], v[244:247], v[164:167]\n"
    "ds_read_b64_tr_b16 v[204:205], v43 offset:0\n"
    "v_mov_b64 v[98:99], 0\n"
    "v_mfma_f32_16x16x32_bf16 v[168:171], v[232:235], v[248:251], v[168:171]\n"
    "ds_read_b64_tr_b16 v[206:207], v43 offset:768\n"
    "v_mov_b64 v[100:101], 0\n"
    "v_mfma_f32_16x16x32_bf16 v[172:175], v[232:235], v[252:255], v[172:175]\n"
    "v_mov_b64 v[102:103], 0\n"
    "v_mfma_f32_16x16x32_bf16 v[176:179], v[236:239], v[240:243], v[176:179]\n"
    "v_lshrrev_b32 v104, 4, v21\n"
    "v_mfma_f32_16x16x32_bf16 v[180:183], v[236:239], v[244:247], v[180:183]\n"
    "s_lshl_b32 s80, s41, 7\n"
    "v_mfma_f32_16x16x32_bf16 v[184:187], v[236:239], v[248:251], v[184:187]\n"
    "s_and_b32 s81, s42, 2\n"
    "v_mfma_f32_16x16x32_bf16 v[188:191], v[236:239], v[252:255], v[188:191]\n"
    "s_lshl_b32 s81, s81, 1\n"
    "s_waitcnt lgkmcnt(0)\n"
    "v_mfma_f32_16x16x32_bf16 v[128:131], v[192:195], v[208:211], v[128:131]\n"
    "ds_read_b64_tr_b16 v[224:225], v40 offset:6144\n"
    "v_mfma_f32_16x16x32_bf16 v[132:135], v[192:195], v[212:215], v[132:135]\n"
    "ds_read_b64_tr_b16 v[226:227], v40 offset:6912\n"
    "v_mfma_f32_16x16x32_bf16 v[136:139], v[192:195], v[216:219], v[136:139]\n"
    "ds_read_b128 v[240:243], v56 offset:0\n"
    "v_mov_b32 v112, 1280\n"
    "v_mfma_f32_16x16x32_bf16 v[140:143], v[192:195], v[220:223], v[140:143]\n"
    "ds_read_b128 v[244:247], v56 offset:2048\n"
    "v_mov_b32 v113, 15360\n"
    "v_mfma_f32_16x16x32_bf16 v[144:147], v[196:199], v[208:211], v[144:147]\n"
    "ds_read_b128 v[248:251], v56 offset:4096\n"
    "v_mov_b32 v114, -10240\n"
    "v_mfma_f32_16x16x32_bf16 v[148:151], v[196:199], v[212:215], v[148:151]\n"
    "ds_read_b128 v[252:255], v56 offset:6144\n"
    "v_mov_b32 v115, 46080\n"
    "v_mfma_f32_16x16x32_bf16 v[152:155], v[196:199], v[216:219], v[152:155]\n"
    "ds_read_b64_tr_b16 v[228:229], v41 offset:6144\n"
    "v_mov_b32 v116, 5120\n"
    "v_mfma_f32_16x16x32_bf16 v[156:159], v[196:199], v[220:223], v[156:159]\n"
    "ds_read_b64_tr_b16 v[230:231], v41 offset:6912\n"
    "v_add_u32 v105, s80, v104\n"
    "v_smfmac_f32_16x16x64_bf16 v[60:63], v[44:47], v[192:199], v48\n"
    "ds_read_b64_tr_b16 v[232:233], v42 offset:6144\n"
    "v_mul_u32_u24 v106, 205, v105\n"
    "v_mfma_f32_16x16x32_bf16 v[160:163], v[200:203], v[208:211], v[160:163]\n"
    "ds_read_b64_tr_b16 v[234:235], v42 offset:6912\n"
    "v_lshrrev_b32 v106, 11, v106\n"
    "v_mfma_f32_16x16x32_bf16 v[164:167], v[200:203], v[212:215], v[164:167]\n"
    "ds_read_b64_tr_b16 v[236:237], v43 offset:6144\n"
    "v_mad_i32_i24 v107, v106, -10, v105\n"
    "v_mfma_f32_16x16x32_bf16 v[168:171], v[200:203], v[216:219], v[168:171]\n"
    "ds_read_b64_tr_b16 v[238:239], v43 offset:6912\n"
    "v_lshrrev_b32 v108, 1, v107\n"
    "v_mfma_f32_16x16x32_bf16 v[172:175], v[200:203], v[220:223], v[172:175]\n"
    "v_add_u32 v40, 49280, v40\n"
    "v_add_u32 v41, 49280, v41\n"
    "v_add_u32 v42, 49280, v42\n"
    "v_mfma_f32_16x16x32_bf16 v[176:179], v[204:207], v[208:211], v[176:179]\n"
    "v_add_u32 v43, 49280, v43\n"
    "v_add_u32 v55, 49280, v55\n"
    "v_add_u32 v56, 49280, v56\n"
    "v_mfma_f32_16x16x32_bf16 v[180:183], v[204:207], v[212:215], v[180:183]\n"
    "v_bfi_b32 v108, v108, 0, s81\n"
    "v_mfma_f32_16x16x32_bf16 v[184:187], v[204:207], v[216:219], v[184:187]\n"
    "v_xor_b32 v107, v107, v108\n"
    "v_mfma_f32_16x16x32_bf16 v[188:191], v[204:207], v[220:223], v[188:191]\n"
    "v_subrev_u32 v107, 0, v107\n"
    "s_add_u32 m0, s74, 98560\n"
    "s_add_u32 s38, s77, 640\n"
    "buffer_load_dwordx4 v49, s[4:7], s38 offen lds\n"
    "buffer_load_dwordx4 v50, s[4:7], s38 offen offset:1024 lds\n"
    "s_add_u32 m0, s75, 98560\n"
    "s_add_u32 s38, s78, 983040\n"
    "buffer_load_dwordx4 v51, s[28:31], s38 offen lds\n"
    "buffer_load_dwordx4 v52, s[28:31], s38 offen offset:1024 lds\n"
    "buffer_load_dwordx4 v53, s[28:31], s38 offen offset:2048 lds\n"
    "s_add_u32 m0, s76, 98560\n"
    "s_add_u32 s38, s79, 409600\n"
    "buffer_load_dwordx4 v54, s[12:15], s38 offen lds\n"
    "v_lshlrev_b32 v107, 4, v107\n"
    "v_mad_u32_u24 v2, v106, v112, v107\n"
    "v_bfe_u32 v108, v106, 3, 1\n"
    "v_lshrrev_b32 v109, 4, v106\n"
    "v_mad_u32_u24 v5, v108, v113, v2\n"
    "v_mad_i32_i24 v5, v109, v114, v5\n"
    "v_bfe_u32 v108, v106, 2, 2\n"
    "v_mad_u32_u24 v8, v108, v115, v2\n"
    "v_mad_u32_u24 v8, v109, v116, v8\n"
    "v_add_u32 v105, 64, v105\n"
    "v_mul_u32_u24 v106, 205, v105\n"
    "v_lshrrev_b32 v106, 11, v106\n"
    "s_waitcnt vmcnt(6) lgkmcnt(0)\n"
    "s_barrier\n"
    "v_mfma_f32_16x16x32_bf16 v[128:131], v[224:227], v[240:243], v[128:131]\n"
    "ds_read_b64_tr_b16 v[192:193], v40 offset:0\n"
    "v_mfma_f32_16x16x32_bf16 v[132:135], v[224:227], v[244:247], v[132:135]\n"
    "ds_read_b64_tr_b16 v[194:195], v40 offset:768\n"
    "v_mfma_f32_16x16x32_bf16 v[136:139], v[224:227], v[248:251], v[136:139]\n"
    "ds_read_b128 v[208:211], v55 offset:0\n"
    "v_mad_i32_i24 v107, v106, -10, v105\n"
    "v_mfma_f32_16x16x32_bf16 v[140:143], v[224:227], v[252:255], v[140:143]\n"
    "ds_read_b128 v[212:215], v55 offset:2048\n"
    "v_lshrrev_b32 v108, 1, v107\n"
    "v_mfma_f32_16x16x32_bf16 v[144:147], v[228:231], v[240:243], v[144:147]\n"
    "ds_read_b128 v[216:219], v55 offset:4096\n"
    "v_bfi_b32 v108, v108, 0, s81\n"
    "v_mfma_f32_16x16x32_bf16 v[148:151], v[228:231], v[244:247], v[148:151]\n"
    "ds_read_b128 v[220:223], v55 offset:6144\n"
    "v_xor_b32 v107, v107, v108\n"
    "v_mfma_f32_16x16x32_bf16 v[152:155], v[228:231], v[248:251], v[152:155]\n"
    "ds_read_b64_tr_b16 v[196:197], v41 offset:0\n"
    "v_subrev_u32 v107, 64, v107\n"
    "v_mfma_f32_16x16x32_bf16 v[156:159], v[228:231], v[252:255], v[156:159]\n"
    "ds_read_b64_tr_b16 v[198:199], v41 offset:768\n"
    "v_lshlrev_b32 v107, 4, v107\n"
    "v_smfmac_f32_16x16x64_bf16 v[60:63], v[44:47], v[224:231], v48\n"
    "ds_read_b64_tr_b16 v[200:201], v42 offset:0\n"
    "v_mad_u32_u24 v3, v106, v112, v107\n"
    "v_mfma_f32_16x16x32_bf16 v[160:163], v[232:235], v[240:243], v[160:163]\n"
    "ds_read_b64_tr_b16 v[202:203], v42 offset:768\n"
    "v_bfe_u32 v108, v106, 3, 1\n"
    "v_mfma_f32_16x16x32_bf16 v[164:167], v[232:235], v[244:247], v[164:167]\n"
    "ds_read_b64_tr_b16 v[204:205], v43 offset:0\n"
    "v_lshrrev_b32 v109, 4, v106\n"
    "v_mfma_f32_16x16x32_bf16 v[168:171], v[232:235], v[248:251], v[168:171]\n"
    "ds_read_b64_tr_b16 v[206:207], v43 offset:768\n"
    "v_mad_u32_u24 v6, v108, v113, v3\n"
    "v_mfma_f32_16x16x32_bf16 v[172:175], v[232:235], v[252:255], v[172:175]\n"
    "v_mad_i32_i24 v6, v109, v114, v6\n"
    "v_mfma_f32_16x16x32_bf16 v[176:179], v[236:239], v[240:243], v[176:179]\n"
    "v_bfe_u32 v108, v106, 2, 2\n"
    "v_mfma_f32_16x16x32_bf16 v[180:183], v[236:239], v[244:247], v[180:183]\n"
    "v_mad_u32_u24 v9, v108, v115, v3\n"
    "v_mfma_f32_16x16x32_bf16 v[184:187], v[236:239], v[248:251], v[184:187]\n"
    "v_mad_u32_u24 v9, v109, v116, v9\n"
    "v_mfma_f32_16x16x32_bf16 v[188:191], v[236:239], v[252:255], v[188:191]\n"
    "v_add_u32 v105, 64, v105\n"
    "s_waitcnt lgkmcnt(0)\n"
    "v_mfma_f32_16x16x32_bf16 v[128:131], v[192:195], v[208:211], v[128:131]\n"
    "ds_read_b64_tr_b16 v[224:225], v40 offset:6144\n"
    "v_mfma_f32_16x16x32_bf16 v[132:135], v[192:195], v[212:215], v[132:135]\n"
    "ds_read_b64_tr_b16 v[226:227], v40 offset:6912\n"
    "v_mfma_f32_16x16x32_bf16 v[136:139], v[192:195], v[216:219], v[136:139]\n"
    "ds_read_b128 v[240:243], v56 offset:0\n"
    "v_mul_u32_u24 v106, 205, v105\n"
    "v_mfma_f32_16x16x32_bf16 v[140:143], v[192:195], v[220:223], v[140:143]\n"
    "ds_read_b128 v[244:247], v56 offset:2048\n"
    "v_lshrrev_b32 v106, 11, v106\n"
    "v_mfma_f32_16x16x32_bf16 v[144:147], v[196:199], v[208:211], v[144:147]\n"
    "ds_read_b128 v[248:251], v56 offset:4096\n"
    "v_mad_i32_i24 v107, v106, -10, v105\n"
    "v_mfma_f32_16x16x32_bf16 v[148:151], v[196:199], v[212:215], v[148:151]\n"
    "ds_read_b128 v[252:255], v56 offset:6144\n"
    "v_lshrrev_b32 v108, 1, v107\n"
    "v_mfma_f32_16x16x32_bf16 v[152:155], v[196:199], v[216:219], v[152:155]\n"
    "ds_read_b64_tr_b16 v[228:229], v41 offset:6144\n"
    "v_bfi_b32 v108, v108, 0, s81\n"
    "v_mfma_f32_16x16x32_bf16 v[156:159], v[196:199], v[220:223], v[156:159]\n"
    "ds_read_b64_tr_b16 v[230:231], v41 offset:6912\n"
    "v_xor_b32 v107, v107, v108\n"
    "v_smfmac_f32_16x16x64_bf16 v[60:63], v[44:47], v[192:199], v48\n"
    "ds_read_b64_tr_b16 v[232:233], v42 offset:6144\n"
    "v_subrev_u32 v107, 128, v107\n"
    "v_mfma_f32_16x16x32_bf16 v[160:163], v[200:203], v[208:211], v[160:163]\n"
    "ds_read_b64_tr_b16 v[234:235], v42 offset:6912\n"
    "v_lshlrev_b32 v107, 4, v107\n"
    "v_mfma_f32_16x16x32_bf16 v[164:167], v[200:203], v[212:215], v[164:167]\n"
    "ds_read_b64_tr_b16 v[236:237], v43 offset:6144\n"
    "v_mad_u32_u24 v4, v106, v112, v107\n"
    "v_mfma_f32_16x16x32_bf16 v[168:171], v[200:203], v[216:219], v[168:171]\n"
    "ds_read_b64_tr_b16 v[238:239], v43 offset:6912\n"
    "v_bfe_u32 v108, v106, 3, 1\n"
    "v_mfma_f32_16x16x32_bf16 v[172:175], v[200:203], v[220:223], v[172:175]\n"
    "v_add_u32 v40, 49280, v40\n"
    "v_add_u32 v41, 49280, v41\n"
    "v_add_u32 v42, 49280, v42\n"
    "v_mfma_f32_16x16x32_bf16 v[176:179], v[204:207], v[208:211], v[176:179]\n"
    "v_add_u32 v43, 49280, v43\n"
    "v_add_u32 v55, 49280, v55\n"
    "v_add_u32 v56, 49280, v56\n"
    "v_mfma_f32_16x16x32_bf16 v[180:183], v[204:207], v[212:215], v[180:183]\n"
    "v_lshrrev_b32 v109, 4, v106\n"
    "v_mfma_f32_16x16x32_bf16 v[184:187], v[204:207], v[216:219], v[184:187]\n"
    "v_mad_u32_u24 v7, v108, v113, v4\n"
    "v_mfma_f32_16x16x32_bf16 v[188:191], v[204:207], v[220:223], v[188:191]\n"
    "v_mad_i32_i24 v7, v109, v114, v7\n"
    "s_add_u32 m0, s74, 0\n"
    "s_add_u32 s38, s77, 768\n"
    "buffer_load_dwordx4 v49, s[4:7], s38 offen lds\n"
    "buffer_load_dwordx4 v50, s[4:7], s38 offen offset:1024 lds\n"
    "s_add_u32 m0, s75, 0\n"
    "s_add_u32 s38, s78, 1179648\n"
    "buffer_load_dwordx4 v51, s[28:31], s38 offen lds\n"
    "buffer_load_dwordx4 v52, s[28:31], s38 offen offset:1024 lds\n"
    "buffer_load_dwordx4 v53, s[28:31], s38 offen offset:2048 lds\n"
    "s_add_u32 m0, s76, 0\n"
    "s_add_u32 s38, s79, 491520\n"
    "buffer_load_dwordx4 v54, s[12:15], s38 offen lds\n"
    "v_bfe_u32 v108, v106, 2, 2\n"
    "v_mad_u32_u24 v10, v108, v115, v4\n"
    "v_mad_u32_u24 v10, v109, v116, v10\n"
    "v_lshrrev_b32 v104, 4, v21\n"
    "v_bfe_u32 v105, v104, 3, 2\n"
    "v_lshrrev_b32 v106, 5, v104\n"
    "v_and_b32 v107, 7, v104\n"
    "v_and_b32 v108, 31, v104\n"
    "v_and_b32 v109, 15, v104\n"
    "v_lshrrev_b32 v110, 4, v104\n"
    "s_mul_i32 s80, s41, 3072\n"
    "s_add_u32 s80, s80, 49280\n"
    "s_waitcnt vmcnt(6) lgkmcnt(0)\n"
    "s_barrier\n"
    "v_mfma_f32_16x16x32_bf16 v[128:131], v[224:227], v[240:243], v[128:131]\n"
    "ds_read_b64_tr_b16 v[192:193], v40 offset:0\n"
    "v_mfma_f32_16x16x32_bf16 v[132:135], v[224:227], v[244:247], v[132:135]\n"
    "ds_read_b64_tr_b16 v[194:195], v40 offset:768\n"
    "v_mfma_f32_16x16x32_bf16 v[136:139], v[224:227], v[248:251], v[136:139]\n"
    "ds_read_b128 v[208:211], v55 offset:0\n"
    "v_mul_u32_u24 v111, 768, v105\n"
    "v_mfma_f32_16x16x32_bf16 v[140:143], v[224:227], v[252:255], v[140:143]\n"
    "ds_read_b128 v[212:215], v55 offset:2048\n"
    "v_add_u32 v111, s80, v111\n"
    "v_mfma_f32_16x16x32_bf16 v[144:147], v[228:231], v[240:243], v[144:147]\n"
    "ds_read_b128 v[216:219], v55 offset:4096\n"
    "v_and_b32 v112, 1, v105\n"
    "v_mfma_f32_16x16x32_bf16 v[148:151], v[228:231], v[244:247], v[148:151]\n"
    "ds_read_b128 v[220:223], v55 offset:6144\n"
    "v_xor_b32 v113, v112, v106\n"
    "v_mfma_f32_16x16x32_bf16 v[152:155], v[228:231], v[248:251], v[152:155]\n"
    "ds_read_b64_tr_b16 v[196:197], v41 offset:0\n"
    "v_lshl_add_u32 v113, v113, 3, v107\n"
    "v_mfma_f32_16x16x32_bf16 v[156:159], v[228:231], v[252:255], v[156:159]\n"
    "ds_read_b64_tr_b16 v[198:199], v41 offset:768\n"
    "v_lshl_add_u32 v11, v113, 4, v111\n"
    "v_smfmac_f32_16x16x64_bf16 v[60:63], v[44:47], v[224:231], v48\n"
    "ds_read_b64_tr_b16 v[200:201], v42 offset:0\n"
    "v_lshl_add_u32 v113, v112, 3, v107\n"
    "v_mfma_f32_16x16x32_bf16 v[160:163], v[232:235], v[240:243], v[160:163]\n"
    "ds_read_b64_tr_b16 v[202:203], v42 offset:768\n"
    "v_lshrrev_b32 v114, 1, v105\n"
    "v_mfma_f32_16x16x32_bf16 v[164:167], v[232:235], v[244:247], v[164:167]\n"
    "ds_read_b64_tr_b16 v[204:205], v43 offset:0\n"
    "v_xor_b32 v114, v114, v106\n"
    "v_mfma_f32_16x16x32_bf16 v[168:171], v[232:235], v[248:251], v[168:171]\n"
    "ds_read_b64_tr_b16 v[206:207], v43 offset:768\n"
    "v_lshl_add_u32 v113, v113, 1, v114\n"
    "v_mfma_f32_16x16x32_bf16 v[172:175], v[232:235], v[252:255], v[172:175]\n"
    "v_lshl_add_u32 v113, v113, 2, v111\n"
    "v_mfma_f32_16x16x32_bf16 v[176:179], v[236:239], v[240:243], v[176:179]\n"
    "v_add_u32 v12, 256, v113\n"
    "v_mfma_f32_16x16x32_bf16 v[180:183], v[236:239], v[244:247], v[180:183]\n"
    "v_and_b32 v113, 28, v108\n"
    "v_mfma_f32_16x16x32_bf16 v[184:187], v[236:239], v[248:251], v[184:187]\n"
    "v_mov_b32 v114, 0x90dd8\n"
    "v_mfma_f32_16x16x32_bf16 v[188:191], v[236:239], v[252:255], v[188:191]\n"
    "v_bfe_u32 v113, v114, v113, 4\n"
    "s_waitcnt lgkmcnt(0)\n"
    "v_mfma_f32_16x16x32_bf16 v[128:131], v[192:195], v[208:211], v[128:131]\n"
    "ds_read_b64_tr_b16 v[224:225], v40 offset:6144\n"
    "v_mfma_f32_16x16x32_bf16 v[132:135], v[192:195], v[212:215], v[132:135]\n"
    "ds_read_b64_tr_b16 v[226:227], v40 offset:6912\n"
    "v_mfma_f32_16x16x32_bf16 v[136:139], v[192:195], v[216:219], v[136:139]\n"
    "ds_read_b128 v[240:243], v56 offset:0\n"
    "v_add_u32 v113, v113, v108\n"
    "v_mfma_f32_16x16x32_bf16 v[140:143], v[192:195], v[220:223], v[140:143]\n"
    "ds_read_b128 v[244:247], v56 offset:2048\n"
    "v_cmp_gt_u32 vcc, 20, v108\n"
    "v_mfma_f32_16x16x32_bf16 v[144:147], v[196:199], v[208:211], v[144:147]\n"
    "ds_read_b128 v[248:251], v56 offset:4096\n"
    "s_add_u32 s80, s80, 256\n"
    "v_mfma_f32_16x16x32_bf16 v[148:151], v[196:199], v[212:215], v[148:151]\n"
    "ds_read_b128 v[252:255], v56 offset:6144\n"
    "v_mul_u32_u24 v114, 768, v106\n"
    "v_mfma_f32_16x16x32_bf16 v[152:155], v[196:199], v[216:219], v[152:155]\n"
    "ds_read_b64_tr_b16 v[228:229], v41 offset:6144\n"
    "v_cndmask_b32 v113, 29, v113, vcc\n"
    "v_mfma_f32_16x16x32_bf16 v[156:159], v[196:199], v[220:223], v[156:159]\n"
    "ds_read_b64_tr_b16 v[230:231], v41 offset:6912\n"
    "v_and_b32 v115, 31, v108\n"
    "v_smfmac_f32_16x16x64_bf16 v[60:63], v[44:47], v[192:199], v48\n"
    "ds_read_b64_tr_b16 v[232:233], v42 offset:6144\n"
    "v_cmp_ne_u32 vcc, 24, v115\n"
    "v_mfma_f32_16x16x32_bf16 v[160:163], v[200:203], v[208:211], v[160:163]\n"
    "ds_read_b64_tr_b16 v[234:235], v42 offset:6912\n"
    "s_nop 1\n"
    "v_mfma_f32_16x16x32_bf16 v[164:167], v[200:203], v[212:215], v[164:167]\n"
    "ds_read_b64_tr_b16 v[236:237], v43 offset:6144\n"
    "v_cndmask_b32 v113, 16, v113, vcc\n"
    "v_mfma_f32_16x16x32_bf16 v[168:171], v[200:203], v[216:219], v[168:171]\n"
    "ds_read_b64_tr_b16 v[238:239], v43 offset:6912\n"
    "v_lshl_add_u32 v113, v113, 4, v114\n"
    "v_mfma_f32_16x16x32_bf16 v[172:175], v[200:203], v[220:223], v[172:175]\n"
    "v_add_u32 v40, -98560, v40\n"
    "v_add_u32 v41, -98560, v41\n"
    "v_add_u32 v42, -98560, v42\n"
    "v_mfma_f32_16x16x32_bf16 v[176:179], v[204:207], v[208:211], v[176:179]\n"
    "v_add_u32 v43, -98560, v43\n"
    "v_add_u32 v55, -98560, v55\n"
    "v_add_u32 v56, -98560, v56\n"
    "v_mfma_f32_16x16x32_bf16 v[180:183], v[204:207], v[212:215], v[180:183]\n"
    "v_add_u32 v13, s80, v113\n"
    "v_mfma_f32_16x16x32_bf16 v[184:187], v[204:207], v[216:219], v[184:187]\n"
    "v_and_b32 v113, 3, v104\n"
    "v_mfma_f32_16x16x32_bf16 v[188:191], v[204:207], v[220:223], v[188:191]\n"
    "v_lshlrev_b32 v14, 3, v113\n"
    "s_add_u32 m0, s74, 49280\n"
    "s_add_u32 s38, s77, 896\n"
    "buffer_load_dwordx4 v49, s[4:7], s38 offen lds\n"
    "buffer_load_dwordx4 v50, s[4:7], s38 offen offset:1024 lds\n"
    "s_add_u32 m0, s75, 49280\n"
    "s_add_u32 s38, s78, 1376256\n"
    "buffer_load_dwordx4 v51, s[28:31], s38 offen lds\n"
    "buffer_load_dwordx4 v52, s[28:31], s38 offen offset:1024 lds\n"
    "buffer_load_dwordx4 v53, s[28:31], s38 offen offset:2048 lds\n"
    "s_add_u32 m0, s76, 49280\n"
    "s_add_u32 s38, s79, 573440\n"
    "buffer_load_dwordx4 v54, s[12:15], s38 offen lds\n"
    "v_lshrrev_b32 v113, 2, v104\n"
    "v_mul_u32_u24 v113, 160, v113\n"
    "s_mul_i32 s80, s42, 5120\n"
    "v_add3_u32 v15, v113, v14, s80\n"
    "v_mul_u32_u24 v113, 1280, v109\n"
    "v_lshl_add_u32 v16, v110, 3, v113\n"
    "v_lshlrev_b32 v113, 3, v109\n"
    "v_lshl_add_u32 v18, v110, 8, v113\n"
    "v_and_b32 v114, 1, v110\n"
    "v_lshl_add_u32 v115, v114, 12, v113\n"
    "v_lshl_add_u32 v20, v106, 30, v115\n"
    "v_lshlrev_b32 v114, 14, v114\n"
    "s_waitcnt vmcnt(6) lgkmcnt(0)\n"
    "s_barrier\n"
    "v_mfma_f32_16x16x32_bf16 v[128:131], v[224:227], v[240:243], v[128:131]\n"
    "ds_read_b64_tr_b16 v[192:193], v40 offset:0\n"
    "v_mfma_f32_16x16x32_bf16 v[132:135], v[224:227], v[244:247], v[132:135]\n"
    "ds_read_b64_tr_b16 v[194:195], v40 offset:768\n"
    "v_mfma_f32_16x16x32_bf16 v[136:139], v[224:227], v[248:251], v[136:139]\n"
    "ds_read_b128 v[208:211], v55 offset:0\n"
    "v_lshl_add_u32 v114, v106, 8, v114\n"
    "v_mfma_f32_16x16x32_bf16 v[140:143], v[224:227], v[252:255], v[140:143]\n"
    "ds_read_b128 v[212:215], v55 offset:2048\n"
    "v_lshl_add_u32 v19, v109, 4, v114\n"
    "v_mfma_f32_16x16x32_bf16 v[144:147], v[228:231], v[240:243], v[144:147]\n"
    "ds_read_b128 v[216:219], v55 offset:4096\n"
    "s_lshl_b32 s80, s41, 9\n"
    "v_mfma_f32_16x16x32_bf16 v[148:151], v[228:231], v[244:247], v[148:151]\n"
    "ds_read_b128 v[220:223], v55 offset:6144\n"
    "s_add_u32 s80, s80, 161280\n"
    "v_mfma_f32_16x16x32_bf16 v[152:155], v[228:231], v[248:251], v[152:155]\n"
    "ds_read_b64_tr_b16 v[196:197], v41 offset:0\n"
    "v_add_u32 v23, s80, v113\n"
    "v_mfma_f32_16x16x32_bf16 v[156:159], v[228:231], v[252:255], v[156:159]\n"
    "ds_read_b64_tr_b16 v[198:199], v41 offset:768\n"
    "s_mul_i32 s80, s41, 3072\n"
    "v_smfmac_f32_16x16x64_bf16 v[60:63], v[44:47], v[224:231], v48\n"
    "ds_read_b64_tr_b16 v[200:201], v42 offset:0\n"
    "s_add_u32 s80, s80, 147840\n"
    "v_mfma_f32_16x16x32_bf16 v[160:163], v[232:235], v[240:243], v[160:163]\n"
    "ds_read_b64_tr_b16 v[202:203], v42 offset:768\n"
    "v_mul_u32_u24 v113, 48, v109\n"
    "v_mfma_f32_16x16x32_bf16 v[164:167], v[232:235], v[244:247], v[164:167]\n"
    "ds_read_b64_tr_b16 v[204:205], v43 offset:0\n"
    "v_add_u32 v24, s80, v113\n"
    "v_mfma_f32_16x16x32_bf16 v[168:171], v[232:235], v[248:251], v[168:171]\n"
    "ds_read_b64_tr_b16 v[206:207], v43 offset:768\n"
    "s_lshr_b32 s80, s64, 1\n"
    "v_mfma_f32_16x16x32_bf16 v[172:175], v[232:235], v[252:255], v[172:175]\n"
    "s_mul_i32 s80, s80, 1536\n"
    "v_mfma_f32_16x16x32_bf16 v[176:179], v[236:239], v[240:243], v[176:179]\n"
    "s_mul_i32 s81, s41, 6144\n"
    "v_mfma_f32_16x16x32_bf16 v[180:183], v[236:239], v[244:247], v[180:183]\n"
    "s_add_u32 s82, s80, s81\n"
    "v_mfma_f32_16x16x32_bf16 v[184:187], v[236:239], v[248:251], v[184:187]\n"
    "s_sub_u32 s80, s80, s81\n"
    "v_mfma_f32_16x16x32_bf16 v[188:191], v[236:239], v[252:255], v[188:191]\n"
    "s_add_u32 s82, s82, 147840\n"
    "s_waitcnt lgkmcnt(0)\n"
    "v_mfma_f32_16x16x32_bf16 v[128:131], v[192:195], v[208:211], v[128:131]\n"
    "ds_read_b64_tr_b16 v[224:225], v40 offset:6144\n"
    "v_mfma_f32_16x16x32_bf16 v[132:135], v[192:195], v[212:215], v[132:135]\n"
    "ds_read_b64_tr_b16 v[226:227], v40 offset:6912\n"
    "v_mfma_f32_16x16x32_bf16 v[136:139], v[192:195], v[216:219], v[136:139]\n"
    "ds_read_b128 v[240:243], v56 offset:0\n"
    "s_add_u32 s80, s80, 153984\n"
    "v_mfma_f32_16x16x32_bf16 v[140:143], v[192:195], v[220:223], v[140:143]\n"
    "ds_read_b128 v[244:247], v56 offset:2048\n"
    "v_mul_u32_u24 v113, 48, v108\n"
    "v_mfma_f32_16x16x32_bf16 v[144:147], v[196:199], v[208:211], v[144:147]\n"
    "ds_read_b128 v[248:251], v56 offset:4096\n"
    "v_lshl_add_u32 v113, v106, 2, v113\n"
    "v_mfma_f32_16x16x32_bf16 v[148:151], v[196:199], v[212:215], v[148:151]\n"
    "ds_read_b128 v[252:255], v56 offset:6144\n"
    "v_add_u32 v25, s80, v113\n"
    "v_mfma_f32_16x16x32_bf16 v[152:155], v[196:199], v[216:219], v[152:155]\n"
    "ds_read_b64_tr_b16 v[228:229], v41 offset:6144\n"
    "v_add_u32 v26, s82, v113\n"
    "v_mfma_f32_16x16x32_bf16 v[156:159], v[196:199], v[220:223], v[156:159]\n"
    "ds_read_b64_tr_b16 v[230:231], v41 offset:6912\n"
    "v_mad_u32_u24 v118, v106, 12, v25\n"
    "v_smfmac_f32_16x16x64_bf16 v[60:63], v[44:47], v[192:199], v48\n"
    "ds_read_b64_tr_b16 v[232:233], v42 offset:6144\n"
    "v_mad_u32_u24 v119, v106, 12, v26\n"
    "v_mfma_f32_16x16x32_bf16 v[160:163], v[200:203], v[208:211], v[160:163]\n"
    "ds_read_b64_tr_b16 v[234:235], v42 offset:6912\n"
    "v_mov_b32 v27, 1.0\n"
    "v_mfma_f32_16x16x32_bf16 v[164:167], v[200:203], v[212:215], v[164:167]\n"
    "ds_read_b64_tr_b16 v[236:237], v43 offset:6144\n"
    "s_lshl_b32 s80, s46, 2\n"
    "v_mfma_f32_16x16x32_bf16 v[168:171], v[200:203], v[216:219], v[168:171]\n"
    "ds_read_b64_tr_b16 v[238:239], v43 offset:6912\n"
    "s_add_u32 s80, s80, 16384\n"
    "v_mfma_f32_16x16x32_bf16 v[172:175], v[200:203], v[220:223], v[172:175]\n"
    "v_add_u32 v40, 49280, v40\n"
    "v_add_u32 v41, 49280, v41\n"
    "v_add_u32 v42, 49280, v42\n"
    "v_mfma_f32_16x16x32_bf16 v[176:179], v[204:207], v[208:211], v[176:179]\n"
    "v_add_u32 v43, 49280, v43\n"
    "v_add_u32 v55, 49280, v55\n"
    "v_add_u32 v56, 49280, v56\n"
    "v_mfma_f32_16x16x32_bf16 v[180:183], v[204:207], v[212:215], v[180:183]\n"
    "s_and_b32 s82, s59, 24\n"
    "v_mfma_f32_16x16x32_bf16 v[184:187], v[204:207], v[216:219], v[184:187]\n"
    "s_andn2_b32 s81, s59, 24\n"
    "v_mfma_f32_16x16x32_bf16 v[188:191], v[204:207], v[220:223], v[188:191]\n"
    "s_lshl_b32 s81, s81, 2\n"
    "s_add_u32 m0, s74, 98560\n"
    "s_add_u32 s38, s77, 1024\n"
    "buffer_load_dwordx4 v49, s[4:7], s38 offen lds\n"
    "buffer_load_dwordx4 v50, s[4:7], s38 offen offset:1024 lds\n"
    "s_add_u32 m0, s75, 98560\n"
    "s_add_u32 s38, s78, 1572864\n"
    "buffer_load_dwordx4 v51, s[28:31], s38 offen lds\n"
    "buffer_load_dwordx4 v52, s[28:31], s38 offen offset:1024 lds\n"
    "buffer_load_dwordx4 v53, s[28:31], s38 offen offset:2048 lds\n"
    "s_add_u32 m0, s76, 98560\n"
    "s_add_u32 s38, s79, 655360\n"
    "buffer_load_dwordx4 v54, s[12:15], s38 offen lds\n"
    "s_add_u32 s81, s81, s82\n"
    "s_add_u32 s81, s81, 16384\n"
    "s_waitcnt vmcnt(6) lgkmcnt(0)\n"
    "s_barrier\n"
    "v_mfma_f32_16x16x32_bf16 v[128:131], v[224:227], v[240:243], v[128:131]\n"
    "ds_read_b64_tr_b16 v[192:193], v40 offset:0\n"
    "v_mfma_f32_16x16x32_bf16 v[132:135], v[224:227], v[244:247], v[132:135]\n"
    "ds_read_b64_tr_b16 v[194:195], v40 offset:768\n"
    "v_mfma_f32_16x16x32_bf16 v[136:139], v[224:227], v[248:251], v[136:139]\n"
    "ds_read_b128 v[208:211], v55 offset:0\n"
    "v_mfma_f32_16x16x32_bf16 v[140:143], v[224:227], v[252:255], v[140:143]\n"
    "ds_read_b128 v[212:215], v55 offset:2048\n"
    "v_mfma_f32_16x16x32_bf16 v[144:147], v[228:231], v[240:243], v[144:147]\n"
    "ds_read_b128 v[216:219], v55 offset:4096\n"
    "v_mfma_f32_16x16x32_bf16 v[148:151], v[228:231], v[244:247], v[148:151]\n"
    "ds_read_b128 v[220:223], v55 offset:6144\n"
    "v_mfma_f32_16x16x32_bf16 v[152:155], v[228:231], v[248:251], v[152:155]\n"
    "ds_read_b64_tr_b16 v[196:197], v41 offset:0\n"
    "v_mfma_f32_16x16x32_bf16 v[156:159], v[228:231], v[252:255], v[156:159]\n"
    "ds_read_b64_tr_b16 v[198:199], v41 offset:768\n"
    "v_smfmac_f32_16x16x64_bf16 v[60:63], v[44:47], v[224:231], v48\n"
    "ds_read_b64_tr_b16 v[200:201], v42 offset:0\n"
    "v_mfma_f32_16x16x32_bf16 v[160:163], v[232:235], v[240:243], v[160:163]\n"
    "ds_read_b64_tr_b16 v[202:203], v42 offset:768\n"
    "v_mfma_f32_16x16x32_bf16 v[164:167], v[232:235], v[244:247], v[164:167]\n"
    "ds_read_b64_tr_b16 v[204:205], v43 offset:0\n"
    "v_mfma_f32_16x16x32_bf16 v[168:171], v[232:235], v[248:251], v[168:171]\n"
    "ds_read_b64_tr_b16 v[206:207], v43 offset:768\n"
    "v_mfma_f32_16x16x32_bf16 v[172:175], v[232:235], v[252:255], v[172:175]\n"
    "v_mfma_f32_16x16x32_bf16 v[176:179], v[236:239], v[240:243], v[176:179]\n"
    "v_mfma_f32_16x16x32_bf16 v[180:183], v[236:239], v[244:247], v[180:183]\n"
    "v_mfma_f32_16x16x32_bf16 v[184:187], v[236:239], v[248:251], v[184:187]\n"
    "v_mfma_f32_16x16x32_bf16 v[188:191], v[236:239], v[252:255], v[188:191]\n"
    "s_waitcnt lgkmcnt(0)\n"
    "v_mfma_f32_16x16x32_bf16 v[128:131], v[192:195], v[208:211], v[128:131]\n"
    "ds_read_b64_tr_b16 v[224:225], v40 offset:6144\n"
    "v_mfma_f32_16x16x32_bf16 v[132:135], v[192:195], v[212:215], v[132:135]\n"
    "ds_read_b64_tr_b16 v[226:227], v40 offset:6912\n"
    "v_mfma_f32_16x16x32_bf16 v[136:139], v[192:195], v[216:219], v[136:139]\n"
    "ds_read_b128 v[240:243], v56 offset:0\n"
    "v_mfma_f32_16x16x32_bf16 v[140:143], v[192:195], v[220:223], v[140:143]\n"
    "ds_read_b128 v[244:247], v56 offset:2048\n"
    "v_mfma_f32_16x16x32_bf16 v[144:147], v[196:199], v[208:211], v[144:147]\n"
    "ds_read_b128 v[248:251], v56 offset:4096\n"
    "v_mfma_f32_16x16x32_bf16 v[148:151], v[196:199], v[212:215], v[148:151]\n"
    "ds_read_b128 v[252:255], v56 offset:6144\n"
    "v_mfma_f32_16x16x32_bf16 v[152:155], v[196:199], v[216:219], v[152:155]\n"
    "ds_read_b64_tr_b16 v[228:229], v41 offset:6144\n"
    "v_mfma_f32_16x16x32_bf16 v[156:159], v[196:199], v[220:223], v[156:159]\n"
    "ds_read_b64_tr_b16 v[230:231], v41 offset:6912\n"
    "v_smfmac_f32_16x16x64_bf16 v[60:63], v[44:47], v[192:199], v48\n"
    "ds_read_b64_tr_b16 v[232:233], v42 offset:6144\n"
    "v_mfma_f32_16x16x32_bf16 v[160:163], v[200:203], v[208:211], v[160:163]\n"
    "ds_read_b64_tr_b16 v[234:235], v42 offset:6912\n"
    "v_mfma_f32_16x16x32_bf16 v[164:167], v[200:203], v[212:215], v[164:167]\n"
    "ds_read_b64_tr_b16 v[236:237], v43 offset:6144\n"
    "v_mfma_f32_16x16x32_bf16 v[168:171], v[200:203], v[216:219], v[168:171]\n"
    "ds_read_b64_tr_b16 v[238:239], v43 offset:6912\n"
    "v_mfma_f32_16x16x32_bf16 v[172:175], v[200:203], v[220:223], v[172:175]\n"
    "v_add_u32 v40, 49280, v40\n"
    "v_add_u32 v41, 49280, v41\n"
    "v_add_u32 v42, 49280, v42\n"
    "v_mfma_f32_16x16x32_bf16 v[176:179], v[204:207], v[208:211], v[176:179]\n"
    "v_add_u32 v43, 49280, v43\n"
    "v_add_u32 v55, 49280, v55\n"
    "v_add_u32 v56, 49280, v56\n"
    "v_mfma_f32_16x16x32_bf16 v[180:183], v[204:207], v[212:215], v[180:183]\n"
    "s_lshl_b32 s38, s64, 9\n"
    "s_add_u32 s38, s38, 148480\n"
    "v_lshrrev_b32 v117, 1, v21\n"
    "v_add_u32 v117, s38, v117\n"
    "v_mfma_f32_16x16x32_bf16 v[184:187], v[204:207], v[216:219], v[184:187]\n"
    "v_mfma_f32_16x16x32_bf16 v[188:191], v[204:207], v[220:223], v[188:191]\n"
    "ds_write_b64 v117, v[60:61]\n"
    "s_add_u32 m0, s74, 0\n"
    "s_add_u32 s38, s77, 1152\n"
    "buffer_load_dwordx4 v49, s[4:7], s38 offen lds\n"
    "buffer_load_dwordx4 v50, s[4:7], s38 offen offset:1024 lds\n"
    "s_add_u32 m0, s75, 0\n"
    "s_add_u32 s38, s78, 1769472\n"
    "buffer_load_dwordx4 v51, s[28:31], s38 offen lds\n"
    "buffer_load_dwordx4 v52, s[28:31], s38 offen offset:1024 lds\n"
    "buffer_load_dwordx4 v53, s[28:31], s38 offen offset:2048 lds\n"
    "s_add_u32 m0, s76, 0\n"
    "s_add_u32 s38, s79, 737280\n"
    "buffer_load_dwordx4 v54, s[12:15], s38 offen lds\n"
    "s_waitcnt vmcnt(6) lgkmcnt(0)\n"
    "s_barrier\n"
    "v_mfma_f32_16x16x32_bf16 v[128:131], v[224:227], v[240:243], v[128:131]\n"
    "ds_read_b64_tr_b16 v[192:193], v40 offset:0\n"
    "v_mfma_f32_16x16x32_bf16 v[132:135], v[224:227], v[244:247], v[132:135]\n"
    "ds_read_b64_tr_b16 v[194:195], v40 offset:768\n"
    "v_mfma_f32_16x16x32_bf16 v[136:139], v[224:227], v[248:251], v[136:139]\n"
    "ds_read_b128 v[208:211], v55 offset:0\n"
    "v_mfma_f32_16x16x32_bf16 v[140:143], v[224:227], v[252:255], v[140:143]\n"
    "ds_read_b128 v[212:215], v55 offset:2048\n"
    "v_mfma_f32_16x16x32_bf16 v[144:147], v[228:231], v[240:243], v[144:147]\n"
    "ds_read_b128 v[216:219], v55 offset:4096\n"
    "v_mfma_f32_16x16x32_bf16 v[148:151], v[228:231], v[244:247], v[148:151]\n"
    "ds_read_b128 v[220:223], v55 offset:6144\n"
    "v_mfma_f32_16x16x32_bf16 v[152:155], v[228:231], v[248:251], v[152:155]\n"
    "ds_read_b64_tr_b16 v[196:197], v41 offset:0\n"
    "v_mfma_f32_16x16x32_bf16 v[156:159], v[228:231], v[252:255], v[156:159]\n"
    "ds_read_b64_tr_b16 v[198:199], v41 offset:768\n"
    "v_smfmac_f32_16x16x64_bf16 v[60:63], v[44:47], v[224:231], v48\n"
    "ds_read_b64_tr_b16 v[200:201], v42 offset:0\n"
    "v_mfma_f32_16x16x32_bf16 v[160:163], v[232:235], v[240:243], v[160:163]\n"
    "ds_read_b64_tr_b16 v[202:203], v42 offset:768\n"
    "v_mfma_f32_16x16x32_bf16 v[164:167], v[232:235], v[244:247], v[164:167]\n"
    "ds_read_b64_tr_b16 v[204:205], v43 offset:0\n"
    "v_mfma_f32_16x16x32_bf16 v[168:171], v[232:235], v[248:251], v[168:171]\n"
    "ds_read_b64_tr_b16 v[206:207], v43 offset:768\n"
    "v_mfma_f32_16x16x32_bf16 v[172:175], v[232:235], v[252:255], v[172:175]\n"
    "v_xor_b32 v117, 0x200, v117\n"
    "ds_read_b64 v[110:111], v117\n"
    "v_mfma_f32_16x16x32_bf16 v[176:179], v[236:239], v[240:243], v[176:179]\n"
    "v_mfma_f32_16x16x32_bf16 v[180:183], v[236:239], v[244:247], v[180:183]\n"
    "v_mfma_f32_16x16x32_bf16 v[184:187], v[236:239], v[248:251], v[184:187]\n"
    "v_mfma_f32_16x16x32_bf16 v[188:191], v[236:239], v[252:255], v[188:191]\n"
    "v_smfmac_f32_16x16x64_bf16 v[64:67], v[44:47], v[232:239], v48\n"
    "s_waitcnt lgkmcnt(0)\n"
    "v_mfma_f32_16x16x32_bf16 v[128:131], v[192:195], v[208:211], v[128:131]\n"
    "ds_read_b64_tr_b16 v[224:225], v40 offset:6144\n"
    "v_mfma_f32_16x16x32_bf16 v[132:135], v[192:195], v[212:215], v[132:135]\n"
    "ds_read_b64_tr_b16 v[226:227], v40 offset:6912\n"
    "v_mfma_f32_16x16x32_bf16 v[136:139], v[192:195], v[216:219], v[136:139]\n"
    "ds_read_b128 v[240:243], v56 offset:0\n"
    "v_mfma_f32_16x16x32_bf16 v[140:143], v[192:195], v[220:223], v[140:143]\n"
    "ds_read_b128 v[244:247], v56 offset:2048\n"
    "v_mfma_f32_16x16x32_bf16 v[144:147], v[196:199], v[208:211], v[144:147]\n"
    "ds_read_b128 v[248:251], v56 offset:4096\n"
    "v_mfma_f32_16x16x32_bf16 v[148:151], v[196:199], v[212:215], v[148:151]\n"
    "ds_read_b128 v[252:255], v56 offset:6144\n"
    "v_mfma_f32_16x16x32_bf16 v[152:155], v[196:199], v[216:219], v[152:155]\n"
    "ds_read_b64_tr_b16 v[228:229], v41 offset:6144\n"
    "v_mfma_f32_16x16x32_bf16 v[156:159], v[196:199], v[220:223], v[156:159]\n"
    "ds_read_b64_tr_b16 v[230:231], v41 offset:6912\n"
    "v_smfmac_f32_16x16x64_bf16 v[60:63], v[44:47], v[192:199], v48\n"
    "ds_read_b64_tr_b16 v[232:233], v42 offset:6144\n"
    "v_mfma_f32_16x16x32_bf16 v[160:163], v[200:203], v[208:211], v[160:163]\n"
    "ds_read_b64_tr_b16 v[234:235], v42 offset:6912\n"
    "v_mfma_f32_16x16x32_bf16 v[164:167], v[200:203], v[212:215], v[164:167]\n"
    "ds_read_b64_tr_b16 v[236:237], v43 offset:6144\n"
    "v_mfma_f32_16x16x32_bf16 v[168:171], v[200:203], v[216:219], v[168:171]\n"
    "ds_read_b64_tr_b16 v[238:239], v43 offset:6912\n"
    "v_mfma_f32_16x16x32_bf16 v[172:175], v[200:203], v[220:223], v[172:175]\n"
    "v_add_u32 v40, -98560, v40\n"
    "v_add_u32 v41, -98560, v41\n"
    "v_add_u32 v42, -98560, v42\n"
    "v_mfma_f32_16x16x32_bf16 v[176:179], v[204:207], v[208:211], v[176:179]\n"
    "v_add_u32 v43, -98560, v43\n"
    "v_add_u32 v55, -98560, v55\n"
    "v_add_u32 v56, -98560, v56\n"
    "v_mfma_f32_16x16x32_bf16 v[180:183], v[204:207], v[212:215], v[180:183]\n"
    "v_mfma_f32_16x16x32_bf16 v[184:187], v[204:207], v[216:219], v[184:187]\n"
    "v_mfma_f32_16x16x32_bf16 v[188:191], v[204:207], v[220:223], v[188:191]\n"
    "v_smfmac_f32_16x16x64_bf16 v[64:67], v[44:47], v[200:207], v48\n"
    "v_add_u32 v58, 160256, v21\n"
    "ds_read_b128 v[68:71], v58\n"
    "s_mov_b32 s36, 0x10000\n"
    "s_waitcnt lgkmcnt(0)\n"
    "Cc_%=:\n"
    "v_cmp_ne_u32 vcc, s37, v69\n"
    "s_nop 1\n"
    "s_cbranch_vccnz Sc_%=\n"
    "v_cmp_ne_u32 vcc, s37, v71\n"
    "s_nop 1\n"
    "s_cbranch_vccz Kc_%=\n"
    "Sc_%=:\n"
    "s_sleep 2\n"
    "s_movk_i32 s38, 1024\n"
    "buffer_load_dwordx4 v[68:71], v21, s[8:11], s38 offen sc1\n"
    "s_sub_u32 s36, s36, 1\n"
    "s_cmp_lg_u32 s36, 0\n"
    "s_waitcnt vmcnt(0)\n"
    "s_cbranch_scc1 Cc_%=\n"
    "Kc_%=:\n"
    "s_mov_b32 m0, s61\n"
    "s_add_u32 s38, s62, 24576\n"
    "buffer_load_dwordx4 v21, s[8:11], s62 offen sc1 lds\n"
    "buffer_load_dwordx4 v21, s[8:11], s62 offen offset:1024 sc1 lds\n"
    "buffer_load_dwordx4 v21, s[8:11], s62 offen offset:2048 sc1 lds\n"
    "s_add_u32 m0, s61, 24576\n"
    "v_max_f32 v68, v68, v70\n"
    "buffer_load_dwordx4 v21, s[8:11], s38 offen sc1 lds\n"
    "buffer_load_dwordx4 v21, s[8:11], s38 offen offset:1024 sc1 lds\n"
    "buffer_load_dwordx4 v21, s[8:11], s38 offen offset:2048 sc1 lds\n"
    "s_nop 1\n"
    "v_max_f32_dpp v68, v68, v68 quad_perm:[1,0,3,2] row_mask:0xf bank_mask:0xf\n"
    "s_nop 1\n"
    "v_max_f32_dpp v68, v68, v68 quad_perm:[2,3,0,1] row_mask:0xf bank_mask:0xf\n"
    "s_nop 1\n"
    "v_max_f32_dpp v68, v68, v68 row_half_mirror row_mask:0xf bank_mask:0xf\n"
    "s_nop 1\n"
    "v_max_f32_dpp v68, v68, v68 row_mirror row_mask:0xf bank_mask:0xf\n"
    "s_nop 1\n"
    "v_max_f32_dpp v68, v68, v68 row_bcast:15 row_mask:0xf bank_mask:0xf\n"
    "s_nop 1\n"
    "v_max_f32_dpp v68, v68, v68 row_bcast:31 row_mask:0xf bank_mask:0xf\n"
    "s_nop 1\n"
    "v_readlane_b32 s63, v68, 63\n"
    "s_waitcnt vmcnt(6) lgkmcnt(0)\n"
    "s_barrier\n"
    "v_mfma_f32_16x16x32_bf16 v[128:131], v[224:227], v[240:243], v[128:131]\n"
    "ds_read_b64_tr_b16 v[192:193], v40 offset:0\n"
    "v_mfma_f32_16x16x32_bf16 v[132:135], v[224:227], v[244:247], v[132:135]\n"
    "ds_read_b64_tr_b16 v[194:195], v40 offset:768\n"
    "v_mfma_f32_16x16x32_bf16 v[136:139], v[224:227], v[248:251], v[136:139]\n"
    "ds_read_b128 v[208:211], v55 offset:0\n"
    "v_mfma_f32_16x16x32_bf16 v[140:143], v[224:227], v[252:255], v[140:143]\n"
    "ds_read_b128 v[212:215], v55 offset:2048\n"
    "v_mfma_f32_16x16x32_bf16 v[144:147], v[228:231], v[240:243], v[144:147]\n"
    "ds_read_b128 v[216:219], v55 offset:4096\n"
    "v_mfma_f32_16x16x32_bf16 v[148:151], v[228:231], v[244:247], v[148:151]\n"
    "ds_read_b128 v[220:223], v55 offset:6144\n"
    "v_mfma_f32_16x16x32_bf16 v[152:155], v[228:231], v[248:251], v[152:155]\n"
    "ds_read_b64_tr_b16 v[196:197], v41 offset:0\n"
    "v_mfma_f32_16x16x32_bf16 v[156:159], v[228:231], v[252:255], v[156:159]\n"
    "ds_read_b64_tr_b16 v[198:199], v41 offset:768\n"
    "v_smfmac_f32_16x16x64_bf16 v[60:63], v[44:47], v[224:231], v48\n"
    "ds_read_b64_tr_b16 v[200:201], v42 offset:0\n"
    "v_mfma_f32_16x16x32_bf16 v[160:163], v[232:235], v[240:243], v[160:163]\n"
    "ds_read_b64_tr_b16 v[202:203], v42 offset:768\n"
    "v_mfma_f32_16x16x32_bf16 v[164:167], v[232:235], v[244:247], v[164:167]\n"
    "ds_read_b64_tr_b16 v[204:205], v43 offset:0\n"
    "v_mfma_f32_16x16x32_bf16 v[168:171], v[232:235], v[248:251], v[168:171]\n"
    "ds_read_b64_tr_b16 v[206:207], v43 offset:768\n"
    "v_mfma_f32_16x16x32_bf16 v[172:175], v[232:235], v[252:255], v[172:175]\n"
    "ds_read2_b64 v[120:123], v23 offset1:16\n"
    "ds_read2_b64 v[124:127], v23 offset0:32 offset1:48\n"
    "v_mfma_f32_16x16x32_bf16 v[176:179], v[236:239], v[240:243], v[176:179]\n"
    "v_mfma_f32_16x16x32_bf16 v[180:183], v[236:239], v[244:247], v[180:183]\n"
    "v_mfma_f32_16x16x32_bf16 v[184:187], v[236:239], v[248:251], v[184:187]\n"
    "v_mfma_f32_16x16x32_bf16 v[188:191], v[236:239], v[252:255], v[188:191]\n"
    "v_smfmac_f32_16x16x64_bf16 v[64:67], v[44:47], v[232:239], v48\n"
    "s_waitcnt lgkmcnt(0)\n"
    "v_mfma_f32_16x16x32_bf16 v[128:131], v[192:195], v[208:211], v[128:131]\n"
    "ds_read_b64_tr_b16 v[224:225], v40 offset:6144\n"
    "v_mul_f32 v50, v121, v121\n"
    "v_mul_f32 v52, v123, v123\n"
    "v_mfma_f32_16x16x32_bf16 v[132:135], v[192:195], v[212:215], v[132:135]\n"
    "ds_read_b64_tr_b16 v[226:227], v40 offset:6912\n"
    "v_mul_f32 v54, v125, v125\n"
    "v_mul_f32 v70, v127, v127\n"
    "v_mfma_f32_16x16x32_bf16 v[136:139], v[192:195], v[216:219], v[136:139]\n"
    "ds_read_b128 v[240:243], v56 offset:0\n"
    "v_mov_b64 v[68:69], v[110:111]\n"
    "v_mfma_f32_16x16x32_bf16 v[140:143], v[192:195], v[220:223], v[140:143]\n"
    "ds_read_b128 v[244:247], v56 offset:2048\n"
    "v_mfma_f32_16x16x32_bf16 v[144:147], v[196:199], v[208:211], v[144:147]\n"
    "ds_read_b128 v[248:251], v56 offset:4096\n"
    "v_mfma_f32_16x16x32_bf16 v[148:151], v[196:199], v[212:215], v[148:151]\n"
    "ds_read_b128 v[252:255], v56 offset:6144\n"
    "v_mfma_f32_16x16x32_bf16 v[152:155], v[196:199], v[216:219], v[152:155]\n"
    "ds_read_b64_tr_b16 v[228:229], v41 offset:6144\n"
    "v_mfma_f32_16x16x32_bf16 v[156:159], v[196:199], v[220:223], v[156:159]\n"
    "ds_read_b64_tr_b16 v[230:231], v41 offset:6912\n"
    "v_smfmac_f32_16x16x64_bf16 v[60:63], v[44:47], v[192:199], v48\n"
    "ds_read_b64_tr_b16 v[232:233], v42 offset:6144\n"
    "v_mfma_f32_16x16x32_bf16 v[160:163], v[200:203], v[208:211], v[160:163]\n"
    "ds_read_b64_tr_b16 v[234:235], v42 offset:6912\n"
    "v_mfma_f32_16x16x32_bf16 v[164:167], v[200:203], v[212:215], v[164:167]\n"
    "ds_read_b64_tr_b16 v[236:237], v43 offset:6144\n"
    "v_mfma_f32_16x16x32_bf16 v[168:171], v[200:203], v[216:219], v[168:171]\n"
    "ds_read_b64_tr_b16 v[238:239], v43 offset:6912\n"
    "v_mfma_f32_16x16x32_bf16 v[172:175], v[200:203], v[220:223], v[172:175]\n"
    "v_mov_b64 v[104:105], -1\n"
    "v_mov_b64 v[106:107], -1\n"
    "buffer_store_dwordx4 v[104:107], v19, s[8:11], s57 offen sc1\n"
    "buffer_store_dwordx2 v[104:105], v20, s[8:11], s58 offen sc1\n"
    "v_mfma_f32_16x16x32_bf16 v[176:179], v[204:207], v[208:211], v[176:179]\n"
    "v_mfma_f32_16x16x32_bf16 v[180:183], v[204:207], v[212:215], v[180:183]\n"
    "v_mfma_f32_16x16x32_bf16 v[184:187], v[204:207], v[216:219], v[184:187]\n"
    "v_mfma_f32_16x16x32_bf16 v[188:191], v[204:207], v[220:223], v[188:191]\n"
    "v_smfmac_f32_16x16x64_bf16 v[64:67], v[44:47], v[200:207], v48\n"
    "s_waitcnt lgkmcnt(0)\n"
    "v_smfmac_f32_16x16x64_bf16 v[60:63], v[44:47], v[224:231], v48\n"
    "v_smfmac_f32_16x16x64_bf16 v[64:67], v[44:47], v[232:239], v48\n"
    "v_mfma_f32_16x16x32_bf16 v[128:131], v[224:227], v[240:243], v[128:131]\n"
    "v_mfma_f32_16x16x32_bf16 v[144:147], v[228:231], v[240:243], v[144:147]\n"
    "v_mfma_f32_16x16x32_bf16 v[160:163], v[232:235], v[240:243], v[160:163]\n"
    "v_mfma_f32_16x16x32_bf16 v[176:179], v[236:239], v[240:243], v[176:179]\n"
    "s_nop 3\n"
    "v_add_f32 v64, v64, v68\n"
    "v_add_f32 v65, v65, v69\n"
    "v_mfma_f32_16x16x4_f32 v[192:195], v60, v27, 0\n"
    "v_mfma_f32_16x16x4_f32 v[196:199], v61, v27, 0\n"
    "v_mfma_f32_16x16x4_f32 v[200:203], v64, v27, 0\n"
    "v_mfma_f32_16x16x4_f32 v[204:207], v65, v27, 0\n"
    "v_mfma_f32_16x16x32_bf16 v[132:135], v[224:227], v[244:247], v[132:135]\n"
    "v_mfma_f32_16x16x32_bf16 v[148:151], v[228:231], v[244:247], v[148:151]\n"
    "v_mfma_f32_16x16x32_bf16 v[164:167], v[232:235], v[244:247], v[164:167]\n"
    "v_mfma_f32_16x16x32_bf16 v[180:183], v[236:239], v[244:247], v[180:183]\n"
    "v_mfma_f32_16x16x32_bf16 v[136:139], v[224:227], v[248:251], v[136:139]\n"
    "v_mfma_f32_16x16x32_bf16 v[152:155], v[228:231], v[248:251], v[152:155]\n"
    "v_mfma_f32_16x16x32_bf16 v[168:171], v[232:235], v[248:251], v[168:171]\n"
    "v_mfma_f32_16x16x32_bf16 v[184:187], v[236:239], v[248:251], v[184:187]\n"
    "v_mfma_f32_16x16x32_bf16 v[140:143], v[224:227], v[252:255], v[140:143]\n"
    "v_mfma_f32_16x16x32_bf16 v[156:159], v[228:231], v[252:255], v[156:159]\n"
    "v_mfma_f32_16x16x32_bf16 v[172:175], v[232:235], v[252:255], v[172:175]\n"
    "v_mfma_f32_16x16x32_bf16 v[188:191], v[236:239], v[252:255], v[188:191]\n"
    "v_pk_fma_f32 v[128:129], v[192:193], v[120:121], v[128:129] op_sel_hi:[1,0,1]\n"
    "v_pk_fma_f32 v[130:131], v[194:195], v[120:121], v[130:131] op_sel_hi:[1,0,1]\n"
    "v_pk_fma_f32 v[144:145], v[196:197], v[120:121], v[144:145] op_sel_hi:[1,0,1]\n"
    "v_pk_fma_f32 v[146:147], v[198:199], v[120:121], v[146:147] op_sel_hi:[1,0,1]\n"
    "v_pk_fma_f32 v[160:161], v[200:201], v[120:121], v[160:161] op_sel_hi:[1,0,1]\n"
    "v_pk_fma_f32 v[162:163], v[202:203], v[120:121], v[162:163] op_sel_hi:[1,0,1]\n"
    "v_pk_fma_f32 v[176:177], v[204:205], v[120:121], v[176:177] op_sel_hi:[1,0,1]\n"
    "v_pk_fma_f32 v[178:179], v[206:207], v[120:121], v[178:179] op_sel_hi:[1,0,1]\n"
    "v_pk_mul_f32 v[128:129], v[128:129], v[50:51] op_sel_hi:[1,0]\n"
    "v_pk_mul_f32 v[130:131], v[130:131], v[50:51] op_sel_hi:[1,0]\n"
    "v_pk_mul_f32 v[144:145], v[144:145], v[50:51] op_sel_hi:[1,0]\n"
    "v_pk_mul_f32 v[146:147], v[146:147], v[50:51] op_sel_hi:[1,0]\n"
    "v_pk_mul_f32 v[128:129], v[128:129], v[160:161]\n"
    "v_pk_mul_f32 v[130:131], v[130:131], v[162:163]\n"
    "v_pk_mul_f32 v[144:145], v[144:145], v[176:177]\n"
    "v_pk_mul_f32 v[146:147], v[146:147], v[178:179]\n"
    "v_cvt_pk_bf16_f32 v208, v128, v129\n"
    "v_cvt_pk_bf16_f32 v209, v130, v131\n"
    "v_cvt_pk_bf16_f32 v210, v144, v145\n"
    "v_cvt_pk_bf16_f32 v211, v146, v147\n"
    "buffer_store_dwordx4 v[208:211], v21, s[8:11], s60 offen sc1\n"
    "s_add_u32 s60, s60, 49152\n"
    "v_pk_fma_f32 v[132:133], v[192:193], v[122:123], v[132:133] op_sel_hi:[1,0,1]\n"
    "v_pk_fma_f32 v[134:135], v[194:195], v[122:123], v[134:135] op_sel_hi:[1,0,1]\n"
    "v_pk_fma_f32 v[148:149], v[196:197], v[122:123], v[148:149] op_sel_hi:[1,0,1]\n"
    "v_pk_fma_f32 v[150:151], v[198:199], v[122:123], v[150:151] op_sel_hi:[1,0,1]\n"
    "v_pk_fma_f32 v[164:165], v[200:201], v[122:123], v[164:165] op_sel_hi:[1,0,1]\n"
    "v_pk_fma_f32 v[166:167], v[202:203], v[122:123], v[166:167] op_sel_hi:[1,0,1]\n"
    "v_pk_fma_f32 v[180:181], v[204:205], v[122:123], v[180:181] op_sel_hi:[1,0,1]\n"
    "v_pk_fma_f32 v[182:183], v[206:207], v[122:123], v[182:183] op_sel_hi:[1,0,1]\n"
    "v_pk_mul_f32 v[132:133], v[132:133], v[52:53] op_sel_hi:[1,0]\n"
    "v_pk_mul_f32 v[134:135], v[134:135], v[52:53] op_sel_hi:[1,0]\n"
    "v_pk_mul_f32 v[148:149], v[148:149], v[52:53] op_sel_hi:[1,0]\n"
    "v_pk_mul_f32 v[150:151], v[150:151], v[52:53] op_sel_hi:[1,0]\n"
    "v_pk_mul_f32 v[132:133], v[132:133], v[164:165]\n"
    "v_pk_mul_f32 v[134:135], v[134:135], v[166:167]\n"
    "v_pk_mul_f32 v[148:149], v[148:149], v[180:181]\n"
    "v_pk_mul_f32 v[150:151], v[150:151], v[182:183]\n"
    "v_cvt_pk_bf16_f32 v216, v132, v133\n"
    "v_cvt_pk_bf16_f32 v217, v134, v135\n"
    "v_cvt_pk_bf16_f32 v218, v148, v149\n"
    "v_cvt_pk_bf16_f32 v219, v150, v151\n"
    "buffer_store_dwordx4 v[216:219], v21, s[8:11], s60 offen sc1\n"
    "s_add_u32 s60, s60, 49152\n"
    "v_pk_fma_f32 v[136:137], v[192:193], v[124:125], v[136:137] op_sel_hi:[1,0,1]\n"
    "v_pk_fma_f32 v[138:139], v[194:195], v[124:125], v[138:139] op_sel_hi:[1,0,1]\n"
    "v_pk_fma_f32 v[152:153], v[196:197], v[124:125], v[152:153] op_sel_hi:[1,0,1]\n"
    "v_pk_fma_f32 v[154:155], v[198:199], v[124:125], v[154:155] op_sel_hi:[1,0,1]\n"
    "v_pk_fma_f32 v[168:169], v[200:201], v[124:125], v[168:169] op_sel_hi:[1,0,1]\n"
    "v_pk_fma_f32 v[170:171], v[202:203], v[124:125], v[170:171] op_sel_hi:[1,0,1]\n"
    "v_pk_fma_f32 v[184:185], v[204:205], v[124:125], v[184:185] op_sel_hi:[1,0,1]\n"
    "v_pk_fma_f32 v[186:187], v[206:207], v[124:125], v[186:187] op_sel_hi:[1,0,1]\n"
    "v_pk_mul_f32 v[136:137], v[136:137], v[54:55] op_sel_hi:[1,0]\n"
    "v_pk_mul_f32 v[138:139], v[138:139], v[54:55] op_sel_hi:[1,0]\n"
    "v_pk_mul_f32 v[152:153], v[152:153], v[54:55] op_sel_hi:[1,0]\n"
    "v_pk_mul_f32 v[154:155], v[154:155], v[54:55] op_sel_hi:[1,0]\n"
    "v_pk_mul_f32 v[136:137], v[136:137], v[168:169]\n"
    "v_pk_mul_f32 v[138:139], v[138:139], v[170:171]\n"
    "v_pk_mul_f32 v[152:153], v[152:153], v[184:185]\n"
    "v_pk_mul_f32 v[154:155], v[154:155], v[186:187]\n"
    "v_cvt_pk_bf16_f32 v208, v136, v137\n"
    "v_cvt_pk_bf16_f32 v209, v138, v139\n"
    "v_cvt_pk_bf16_f32 v210, v152, v153\n"
    "v_cvt_pk_bf16_f32 v211, v154, v155\n"
    "buffer_store_dwordx4 v[208:211], v21, s[8:11], s60 offen sc1\n"
    "s_add_u32 s60, s60, 49152\n"
    "v_pk_fma_f32 v[140:141], v[192:193], v[126:127], v[140:141] op_sel_hi:[1,0,1]\n"
    "v_pk_fma_f32 v[142:143], v[194:195], v[126:127], v[142:143] op_sel_hi:[1,0,1]\n"
    "v_pk_fma_f32 v[156:157], v[196:197], v[126:127], v[156:157] op_sel_hi:[1,0,1]\n"
    "v_pk_fma_f32 v[158:159], v[198:199], v[126:127], v[158:159] op_sel_hi:[1,0,1]\n"
    "v_pk_fma_f32 v[172:173], v[200:201], v[126:127], v[172:173] op_sel_hi:[1,0,1]\n"
    "v_pk_fma_f32 v[174:175], v[202:203], v[126:127], v[174:175] op_sel_hi:[1,0,1]\n"
    "v_pk_fma_f32 v[188:189], v[204:205], v[126:127], v[188:189] op_sel_hi:[1,0,1]\n"
    "v_pk_fma_f32 v[190:191], v[206:207], v[126:127], v[190:191] op_sel_hi:[1,0,1]\n"
    "v_pk_mul_f32 v[140:141], v[140:141], v[70:71] op_sel_hi:[1,0]\n"
    "v_pk_mul_f32 v[142:143], v[142:143], v[70:71] op_sel_hi:[1,0]\n"
    "v_pk_mul_f32 v[156:157], v[156:157], v[70:71] op_sel_hi:[1,0]\n"
    "v_pk_mul_f32 v[158:159], v[158:159], v[70:71] op_sel_hi:[1,0]\n"
    "v_pk_mul_f32 v[140:141], v[140:141], v[172:173]\n"
    "v_pk_mul_f32 v[142:143], v[142:143], v[174:175]\n"
    "v_pk_mul_f32 v[156:157], v[156:157], v[188:189]\n"
    "v_pk_mul_f32 v[158:159], v[158:159], v[190:191]\n"
    "v_cvt_pk_bf16_f32 v216, v140, v141\n"
    "v_cvt_pk_bf16_f32 v217, v142, v143\n"
    "v_cvt_pk_bf16_f32 v218, v156, v157\n"
    "v_cvt_pk_bf16_f32 v219, v158, v159\n"
    "buffer_store_dwordx4 v[216:219], v21, s[8:11], s60 offen sc1\n"
    "s_add_u32 s60, s60, 49152\n"
    "s_waitcnt vmcnt(9) lgkmcnt(0)\n"
    "s_barrier\n"
    "ds_read_b128 v[28:31], v118\n"
    "ds_read_b32 v32, v25 offset:32\n"
    "ds_read_b128 v[36:39], v119\n"
    "ds_read_b32 v34, v26 offset:32\n"
    "ds_read_b32 v108, v12 offset:49280\n"
    "ds_read_b128 v[104:107], v11 offset:49280\n"
    "ds_read_b128 v[112:115], v13 offset:49280\n"
    "s_branch ATT_%=\n"
    "VQ_%=:\n"
    "ds_read_b64_tr_b16 v[192:193], v40 offset:0\n"
    "ds_read_b64_tr_b16 v[194:195], v40 offset:512\n"
    "ds_read_b128 v[208:211], v55 offset:0\n"
    "ds_read_b128 v[212:215], v55 offset:2048\n"
    "ds_read_b128 v[216:219], v55 offset:4096\n"
    "ds_read_b128 v[220:223], v55 offset:6144\n"
    "ds_read_b64_tr_b16 v[196:197], v41 offset:0\n"
    "ds_read_b64_tr_b16 v[198:199], v41 offset:512\n"
    "ds_read_b64_tr_b16 v[200:201], v42 offset:0\n"
    "ds_read_b64_tr_b16 v[202:203], v42 offset:512\n"
    "s_add_u32 m0, s76, 49280\n"
    "s_add_u32 s38, s79, 81920\n"
    "buffer_load_dwordx4 v54, s[12:15], s38 offen lds\n"
    "s_waitcnt lgkmcnt(10)\n"
    "v_readfirstlane_b32 s37, v58\n"
    "s_nop 3\n"
    "s_add_u32 s37, s37, 1\n"
    "s_waitcnt lgkmcnt(0)\n"
    "v_mfma_f32_16x16x32_bf16 v[128:131], v[192:195], v[208:211], 0\n"
    "ds_read_b64_tr_b16 v[224:225], v40 offset:4096\n"
    "v_mfma_f32_16x16x32_bf16 v[132:135], v[192:195], v[212:215], 0\n"
    "ds_read_b64_tr_b16 v[226:227], v40 offset:4608\n"
    "v_mfma_f32_16x16x32_bf16 v[136:139], v[192:195], v[216:219], 0\n"
    "ds_read_b128 v[240:243], v56 offset:0\n"
    "v_mfma_f32_16x16x32_bf16 v[140:143], v[192:195], v[220:223], 0\n"
    "ds_read_b128 v[244:247], v56 offset:2048\n"
    "v_mfma_f32_16x16x32_bf16 v[144:147], v[196:199], v[208:211], 0\n"
    "ds_read_b128 v[248:251], v56 offset:4096\n"
    "v_mfma_f32_16x16x32_bf16 v[148:151], v[196:199], v[212:215], 0\n"
    "ds_read_b128 v[252:255], v56 offset:6144\n"
    "v_mfma_f32_16x16x32_bf16 v[152:155], v[196:199], v[216:219], 0\n"
    "ds_read_b64_tr_b16 v[228:229], v41 offset:4096\n"
    "v_mfma_f32_16x16x32_bf16 v[156:159], v[196:199], v[220:223], 0\n"
    "ds_read_b64_tr_b16 v[230:231], v41 offset:4608\n"
    "v_smfmac_f32_16x16x64_bf16 v[60:63], v[44:47], v[192:199], v48\n"
    "ds_read_b64_tr_b16 v[232:233], v42 offset:4096\n"
    "v_mfma_f32_16x16x32_bf16 v[160:163], v[200:203], v[208:211], 0\n"
    "ds_read_b64_tr_b16 v[234:235], v42 offset:4608\n"
    "v_mfma_f32_16x16x32_bf16 v[164:167], v[200:203], v[212:215], 0\n"
    "v_add_u32 v40, 49280, v40\n"
    "v_add_u32 v41, 49280, v41\n"
    "v_add_u32 v42, 49280, v42\n"
    "v_mfma_f32_16x16x32_bf16 v[168:171], v[200:203], v[216:219], 0\n"
    "v_add_u32 v55, 49280, v55\n"
    "v_add_u32 v56, 49280, v56\n"
    "v_mfma_f32_16x16x32_bf16 v[172:175], v[200:203], v[220:223], 0\n"
    "v_smfmac_f32_16x16x64_bf16 v[64:67], v[44:47], v[200:207], v48\n"
    "s_add_u32 m0, s74, 98560\n"
    "s_add_u32 s38, s77, 256\n"
    "buffer_load_dwordx4 v49, s[4:7], s38 offen lds\n"
    "buffer_load_dwordx4 v50, s[4:7], s38 offen offset:1024 lds\n"
    "s_add_u32 m0, s75, 98560\n"
    "s_add_u32 s38, s78, 393216\n"
    "buffer_load_dwordx4 v51, s[28:31], s38 offen lds\n"
    "buffer_load_dwordx4 v52, s[28:31], s38 offen offset:1024 lds\n"
    "buffer_load_dwordx4 v53, s[28:31], s38 offen offset:2048 lds\n"
    "s_add_u32 m0, s76, 98560\n"
    "s_add_u32 s38, s79, 163840\n"
    "buffer_load_dwordx4 v54, s[12:15], s38 offen lds\n"
    "s_waitcnt vmcnt(6) lgkmcnt(0)\n"
    "s_barrier\n"
    "v_mfma_f32_16x16x32_bf16 v[128:131], v[224:227], v[240:243], v[128:131]\n"
    "ds_read_b64_tr_b16 v[192:193], v40 offset:0\n"
    "v_mfma_f32_16x16x32_bf16 v[132:135], v[224:227], v[244:247], v[132:135]\n"
    "ds_read_b64_tr_b16 v[194:195], v40 offset:512\n"
    "v_mfma_f32_16x16x32_bf16 v[136:139], v[224:227], v[248:251], v[136:139]\n"
    "ds_read_b128 v[208:211], v55 offset:0\n"
    "s_lshr_b32 s80, s64, 1\n"
    "v_mfma_f32_16x16x32_bf16 v[140:143], v[224:227], v[252:255], v[140:143]\n"
    "ds_read_b128 v[212:215], v55 offset:2048\n"
    "s_add_u32 s42, s80, s65\n"
    "v_mfma_f32_16x16x32_bf16 v[144:147], v[228:231], v[240:243], v[144:147]\n"
    "ds_read_b128 v[216:219], v55 offset:4096\n"
    "s_and_b32 s42, s42, 3\n"
    "v_mfma_f32_16x16x32_bf16 v[148:151], v[228:231], v[244:247], v[148:151]\n"
    "ds_read_b128 v[220:223], v55 offset:6144\n"
    "s_mul_i32 s43, s42, 5120\n"
    "v_mfma_f32_16x16x32_bf16 v[152:155], v[228:231], v[248:251], v[152:155]\n"
    "ds_read_b64_tr_b16 v[196:197], v41 offset:0\n"
    "s_lshl_b32 s81, s41, 11\n"
    "v_mfma_f32_16x16x32_bf16 v[156:159], v[228:231], v[252:255], v[156:159]\n"
    "ds_read_b64_tr_b16 v[198:199], v41 offset:512\n"
    "s_add_u32 s43, s43, s81\n"
    "v_smfmac_f32_16x16x64_bf16 v[60:63], v[44:47], v[224:231], v48\n"
    "ds_read_b64_tr_b16 v[200:201], v42 offset:0\n"
    "s_and_b32 s81, s65, 7\n"
    "v_mfma_f32_16x16x32_bf16 v[160:163], v[232:235], v[240:243], v[160:163]\n"
    "ds_read_b64_tr_b16 v[202:203], v42 offset:512\n"
    "s_mul_i32 s81, s81, 160\n"
    "v_mfma_f32_16x16x32_bf16 v[164:167], v[232:235], v[244:247], v[164:167]\n"
    "s_mul_i32 s44, s42, 491520\n"
    "v_mfma_f32_16x16x32_bf16 v[168:171], v[232:235], v[248:251], v[168:171]\n"
    "s_add_u32 s44, s44, s81\n"
    "v_mfma_f32_16x16x32_bf16 v[172:175], v[232:235], v[252:255], v[172:175]\n"
    "s_mul_i32 s45, s42, 204800\n"
    "v_smfmac_f32_16x16x64_bf16 v[64:67], v[44:47], v[232:239], v48\n"
    "s_add_u32 s45, s45, s81\n"
    "s_waitcnt lgkmcnt(0)\n"
    "v_mfma_f32_16x16x32_bf16 v[128:131], v[192:195], v[208:211], v[128:131]\n"
    "ds_read_b64_tr_b16 v[224:225], v40 offset:4096\n"
    "v_mfma_f32_16x16x32_bf16 v[132:135], v[192:195], v[212:215], v[132:135]\n"
    "ds_read_b64_tr_b16 v[226:227], v40 offset:4608\n"
    "v_mfma_f32_16x16x32_bf16 v[136:139], v[192:195], v[216:219], v[136:139]\n"
    "ds_read_b128 v[240:243], v56 offset:0\n"
    "s_lshr_b32 s82, s65, 3\n"
    "v_mfma_f32_16x16x32_bf16 v[140:143], v[192:195], v[220:223], v[140:143]\n"
    "ds_read_b128 v[244:247], v56 offset:2048\n"
    "s_lshl2_add_u32 s82, s66, s82\n"
    "v_mfma_f32_16x16x32_bf16 v[144:147], v[196:199], v[208:211], v[144:147]\n"
    "ds_read_b128 v[248:251], v56 offset:4096\n"
    "s_xor_b32 s82, s82, 16\n"
    "v_mfma_f32_16x16x32_bf16 v[148:151], v[196:199], v[212:215], v[148:151]\n"
    "ds_read_b128 v[252:255], v56 offset:6144\n"
    "s_lshr_b32 s46, s82, 1\n"
    "v_mfma_f32_16x16x32_bf16 v[152:155], v[196:199], v[216:219], v[152:155]\n"
    "ds_read_b64_tr_b16 v[228:229], v41 offset:4096\n"
    "s_lshl2_add_u32 s46, s46, s42\n"
    "v_mfma_f32_16x16x32_bf16 v[156:159], v[196:199], v[220:223], v[156:159]\n"
    "ds_read_b64_tr_b16 v[230:231], v41 offset:4608\n"
    "s_lshl_b32 s46, s46, 5\n"
    "v_smfmac_f32_16x16x64_bf16 v[60:63], v[44:47], v[192:199], v48\n"
    "ds_read_b64_tr_b16 v[232:233], v42 offset:4096\n"
    "s_add_u32 s46, s46, 2048\n"
    "v_mfma_f32_16x16x32_bf16 v[160:163], v[200:203], v[208:211], v[160:163]\n"
    "ds_read_b64_tr_b16 v[234:235], v42 offset:4608\n"
    "s_lshl1_add_u32 s82, s82, s41\n"
    "v_mfma_f32_16x16x32_bf16 v[164:167], v[200:203], v[212:215], v[164:167]\n"
    "v_add_u32 v40, 49280, v40\n"
    "v_add_u32 v41, 49280, v41\n"
    "v_add_u32 v42, 49280, v42\n"
    "v_mfma_f32_16x16x32_bf16 v[168:171], v[200:203], v[216:219], v[168:171]\n"
    "v_add_u32 v55, 49280, v55\n"
    "v_add_u32 v56, 49280, v56\n"
    "v_mfma_f32_16x16x32_bf16 v[172:175], v[200:203], v[220:223], v[172:175]\n"
    "s_and_b32 s83, s42, 1\n"
    "v_smfmac_f32_16x16x64_bf16 v[64:67], v[44:47], v[200:207], v48\n"
    "s_lshl1_add_u32 s82, s82, s83\n"
    "s_add_u32 m0, s74, 0\n"
    "s_add_u32 s38, s77, 384\n"
    "buffer_load_dwordx4 v49, s[4:7], s38 offen lds\n"
    "buffer_load_dwordx4 v50, s[4:7], s38 offen offset:1024 lds\n"
    "s_add_u32 m0, s75, 0\n"
    "s_add_u32 s38, s78, 589824\n"
    "buffer_load_dwordx4 v51, s[28:31], s38 offen lds\n"
    "buffer_load_dwordx4 v52, s[28:31], s38 offen offset:1024 lds\n"
    "buffer_load_dwordx4 v53, s[28:31], s38 offen offset:2048 lds\n"
    "s_add_u32 m0, s76, 0\n"
    "s_add_u32 s38, s79, 245760\n"
    "buffer_load_dwordx4 v54, s[12:15], s38 offen lds\n"
    "s_mul_i32 s48, s82, 20480\n"
    "s_add_u32 s48, s48, s81\n"
    "s_lshr_b32 s47, s42, 1\n"
    "s_lshl_b32 s47, s47, 6\n"
    "s_add_u32 s47, s47, s48\n"
    "s_lshl2_add_u32 s49, s82, s42\n"
    "s_xor_b32 s50, s49, 4\n"
    "s_mul_i32 s51, s49, 12288\n"
    "s_mul_i32 s52, s50, 12288\n"
    "s_add_u32 s51, s51, 4194304\n"
    "s_add_u32 s52, s52, 4194304\n"
    "s_lshl_b32 s53, s49, 12\n"
    "s_waitcnt vmcnt(6) lgkmcnt(0)\n"
    "s_barrier\n"
    "v_mfma_f32_16x16x32_bf16 v[128:131], v[224:227], v[240:243], v[128:131]\n"
    "ds_read_b64_tr_b16 v[192:193], v40 offset:0\n"
    "v_mfma_f32_16x16x32_bf16 v[132:135], v[224:227], v[244:247], v[132:135]\n"
    "ds_read_b64_tr_b16 v[194:195], v40 offset:512\n"
    "v_mfma_f32_16x16x32_bf16 v[136:139], v[224:227], v[248:251], v[136:139]\n"
    "ds_read_b128 v[208:211], v55 offset:0\n"
    "s_lshl_b32 s54, s50, 12\n"
    "v_mfma_f32_16x16x32_bf16 v[140:143], v[224:227], v[252:255], v[140:143]\n"
    "ds_read_b128 v[212:215], v55 offset:2048\n"
    "s_add_u32 s53, s53, 1048576\n"
    "v_mfma_f32_16x16x32_bf16 v[144:147], v[228:231], v[240:243], v[144:147]\n"
    "ds_read_b128 v[216:219], v55 offset:4096\n"
    "s_add_u32 s54, s54, 1048576\n"
    "v_mfma_f32_16x16x32_bf16 v[148:151], v[228:231], v[244:247], v[148:151]\n"
    "ds_read_b128 v[220:223], v55 offset:6144\n"
    "s_lshl_b32 s49, s49, 10\n"
    "v_mfma_f32_16x16x32_bf16 v[152:155], v[228:231], v[248:251], v[152:155]\n"
    "ds_read_b64_tr_b16 v[196:197], v41 offset:0\n"
    "s_lshl_b32 s50, s50, 10\n"
    "v_mfma_f32_16x16x32_bf16 v[156:159], v[228:231], v[252:255], v[156:159]\n"
    "ds_read_b64_tr_b16 v[198:199], v41 offset:512\n"
    "s_add_u32 s49, s49, 262144\n"
    "v_smfmac_f32_16x16x64_bf16 v[60:63], v[44:47], v[224:231], v48\n"
    "ds_read_b64_tr_b16 v[200:201], v42 offset:0\n"
    "s_add_u32 s50, s50, 262144\n"
    "v_mfma_f32_16x16x32_bf16 v[160:163], v[232:235], v[240:243], v[160:163]\n"
    "ds_read_b64_tr_b16 v[202:203], v42 offset:512\n"
    "s_lshl1_add_u32 s56, s41, s83\n"
    "v_mfma_f32_16x16x32_bf16 v[164:167], v[232:235], v[244:247], v[164:167]\n"
    "s_lshl2_add_u32 s56, s56, s42\n"
    "v_mfma_f32_16x16x32_bf16 v[168:171], v[232:235], v[248:251], v[168:171]\n"
    "s_lshl_b32 s56, s56, 10\n"
    "v_mfma_f32_16x16x32_bf16 v[172:175], v[232:235], v[252:255], v[172:175]\n"
    "s_add_u32 s56, s56, 49152\n"
    "v_smfmac_f32_16x16x64_bf16 v[64:67], v[44:47], v[232:239], v48\n"
    "s_lshl2_add_u32 s80, s41, s42\n"
    "s_waitcnt lgkmcnt(0)\n"
    "v_mfma_f32_16x16x32_bf16 v[128:131], v[192:195], v[208:211], v[128:131]\n"
    "ds_read_b64_tr_b16 v[224:225], v40 offset:4096\n"
    "v_mfma_f32_16x16x32_bf16 v[132:135], v[192:195], v[212:215], v[132:135]\n"
    "ds_read_b64_tr_b16 v[226:227], v40 offset:4608\n"
    "v_mfma_f32_16x16x32_bf16 v[136:139], v[192:195], v[216:219], v[136:139]\n"
    "ds_read_b128 v[240:243], v56 offset:0\n"
    "s_mul_i32 s55, s80, 6144\n"
    "v_mfma_f32_16x16x32_bf16 v[140:143], v[192:195], v[220:223], v[140:143]\n"
    "ds_read_b128 v[244:247], v56 offset:2048\n"
    "s_xor_b32 s88, s80, 1\n"
    "v_mfma_f32_16x16x32_bf16 v[144:147], v[196:199], v[208:211], v[144:147]\n"
    "ds_read_b128 v[248:251], v56 offset:4096\n"
    "s_mul_i32 s88, s88, 6144\n"
    "v_mfma_f32_16x16x32_bf16 v[148:151], v[196:199], v[212:215], v[148:151]\n"
    "ds_read_b128 v[252:255], v56 offset:6144\n"
    "s_xor_b32 s89, s80, 2\n"
    "v_mfma_f32_16x16x32_bf16 v[152:155], v[196:199], v[216:219], v[152:155]\n"
    "ds_read_b64_tr_b16 v[228:229], v41 offset:4096\n"
    "s_mul_i32 s89, s89, 6144\n"
    "v_mfma_f32_16x16x32_bf16 v[156:159], v[196:199], v[220:223], v[156:159]\n"
    "ds_read_b64_tr_b16 v[230:231], v41 offset:4608\n"
    "s_xor_b32 s96, s80, 3\n"
    "v_smfmac_f32_16x16x64_bf16 v[60:63], v[44:47], v[192:199], v48\n"
    "ds_read_b64_tr_b16 v[232:233], v42 offset:4096\n"
    "s_mul_i32 s96, s96, 6144\n"
    "v_mfma_f32_16x16x32_bf16 v[160:163], v[200:203], v[208:211], v[160:163]\n"
    "ds_read_b64_tr_b16 v[234:235], v42 offset:4608\n"
    "s_lshr_b32 s80, s64, 1\n"
    "v_mfma_f32_16x16x32_bf16 v[164:167], v[200:203], v[212:215], v[164:167]\n"
    "v_add_u32 v40, -98560, v40\n"
    "v_add_u32 v41, -98560, v41\n"
    "v_add_u32 v42, -98560, v42\n"
    "v_mfma_f32_16x16x32_bf16 v[168:171], v[200:203], v[216:219], v[168:171]\n"
    "v_add_u32 v55, -98560, v55\n"
    "v_add_u32 v56, -98560, v56\n"
    "v_mfma_f32_16x16x32_bf16 v[172:175], v[200:203], v[220:223], v[172:175]\n"
    "s_lshl2_add_u32 s81, s67, s80\n"
    "v_smfmac_f32_16x16x64_bf16 v[64:67], v[44:47], v[200:207], v48\n"
    "s_lshl_b32 s81, s81, 5\n"
    "s_add_u32 m0, s74, 49280\n"
    "s_add_u32 s38, s77, 512\n"
    "buffer_load_dwordx4 v49, s[4:7], s38 offen lds\n"
    "buffer_load_dwordx4 v50, s[4:7], s38 offen offset:1024 lds\n"
    "s_add_u32 m0, s75, 49280\n"
    "s_add_u32 s38, s78, 786432\n"
    "buffer_load_dwordx4 v51, s[28:31], s38 offen lds\n"
    "buffer_load_dwordx4 v52, s[28:31], s38 offen offset:1024 lds\n"
    "buffer_load_dwordx4 v53, s[28:31], s38 offen offset:2048 lds\n"
    "s_add_u32 m0, s76, 49280\n"
    "s_add_u32 s38, s79, 327680\n"
    "buffer_load_dwordx4 v54, s[12:15], s38 offen lds\n"
    "s_add_u32 s81, s81, s68\n"
    "s_lshl1_add_u32 s81, s81, s41\n"
    "s_lshl_b32 s57, s81, 9\n"
    "s_add_u32 s57, s57, 1048576\n"
    "s_lshl_b32 s58, s81, 7\n"
    "s_add_u32 s58, s58, 262144\n"
    "s_lshl4_add_u32 s59, s67, s68\n"
    "s_lshl_b32 s59, s59, 3\n"
    "s_add_u32 s59, s59, 2048\n"
    "s_lshl1_add_u32 s60, s67, s41\n"
    "s_mul_i32 s60, s60, 192\n"
    "s_mul_i32 s81, s68, 3\n"
    "s_waitcnt vmcnt(6) lgkmcnt(0)\n"
    "s_barrier\n"
    "v_mfma_f32_16x16x32_bf16 v[128:131], v[224:227], v[240:243], v[128:131]\n"
    "ds_read_b64_tr_b16 v[192:193], v40 offset:0\n"
    "v_mfma_f32_16x16x32_bf16 v[132:135], v[224:227], v[244:247], v[132:135]\n"
    "ds_read_b64_tr_b16 v[194:195], v40 offset:512\n"
    "v_mfma_f32_16x16x32_bf16 v[136:139], v[224:227], v[248:251], v[136:139]\n"
    "ds_read_b128 v[208:211], v55 offset:0\n"
    "s_add_u32 s60, s60, s81\n"
    "v_mfma_f32_16x16x32_bf16 v[140:143], v[224:227], v[252:255], v[140:143]\n"
    "ds_read_b128 v[212:215], v55 offset:2048\n"
    "s_add_u32 s60, s60, s80\n"
    "v_mfma_f32_16x16x32_bf16 v[144:147], v[228:231], v[240:243], v[144:147]\n"
    "ds_read_b128 v[216:219], v55 offset:4096\n"
    "s_lshl_b32 s60, s60, 10\n"
    "v_mfma_f32_16x16x32_bf16 v[148:151], v[228:231], v[244:247], v[148:151]\n"
    "ds_read_b128 v[220:223], v55 offset:6144\n"
    "s_add_u32 s60, s60, 4193280\n"
    "v_mfma_f32_16x16x32_bf16 v[152:155], v[228:231], v[248:251], v[152:155]\n"
    "ds_read_b64_tr_b16 v[196:197], v41 offset:0\n"
    "s_mul_i32 s61, s64, 3072\n"
    "v_mfma_f32_16x16x32_bf16 v[156:159], v[228:231], v[252:255], v[156:159]\n"
    "ds_read_b64_tr_b16 v[198:199], v41 offset:512\n"
    "s_add_u32 s62, s61, 38912\n"
    "v_smfmac_f32_16x16x64_bf16 v[60:63], v[44:47], v[224:231], v48\n"
    "ds_read_b64_tr_b16 v[200:201], v42 offset:0\n"
    "s_add_u32 s61, s61, 49280\n"
    "v_mfma_f32_16x16x32_bf16 v[160:163], v[232:235], v[240:243], v[160:163]\n"
    "ds_read_b64_tr_b16 v[202:203], v42 offset:512\n"
    "v_mov_b64 v[72:73], 0\n"
    "v_mfma_f32_16x16x32_bf16 v[164:167], v[232:235], v[244:247], v[164:167]\n"
    "v_mov_b64 v[74:75], 0\n"
    "v_mfma_f32_16x16x32_bf16 v[168:171], v[232:235], v[248:251], v[168:171]\n"
    "v_mov_b64 v[76:77], 0\n"
    "v_mfma_f32_16x16x32_bf16 v[172:175], v[232:235], v[252:255], v[172:175]\n"
    "v_mov_b64 v[78:79], 0\n"
    "v_smfmac_f32_16x16x64_bf16 v[64:67], v[44:47], v[232:239], v48\n"
    "v_mov_b64 v[80:81], 0\n"
    "s_waitcnt lgkmcnt(0)\n"
    "v_mfma_f32_16x16x32_bf16 v[128:131], v[192:195], v[208:211], v[128:131]\n"
    "ds_read_b64_tr_b16 v[224:225], v40 offset:4096\n"
    "v_mfma_f32_16x16x32_bf16 v[132:135], v[192:195], v[212:215], v[132:135]\n"
    "ds_read_b64_tr_b16 v[226:227], v40 offset:4608\n"
    "v_mfma_f32_16x16x32_bf16 v[136:139], v[192:195], v[216:219], v[136:139]\n"
    "ds_read_b128 v[240:243], v56 offset:0\n"
    "v_mov_b64 v[82:83], 0\n"
    "v_mfma_f32_16x16x32_bf16 v[140:143], v[192:195], v[220:223], v[140:143]\n"
    "ds_read_b128 v[244:247], v56 offset:2048\n"
    "v_mov_b64 v[84:85], 0\n"
    "v_mfma_f32_16x16x32_bf16 v[144:147], v[196:199], v[208:211], v[144:147]\n"
    "ds_read_b128 v[248:251], v56 offset:4096\n"
    "v_mov_b64 v[86:87], 0\n"
    "v_mfma_f32_16x16x32_bf16 v[148:151], v[196:199], v[212:215], v[148:151]\n"
    "ds_read_b128 v[252:255], v56 offset:6144\n"
    "v_mov_b64 v[88:89], 0\n"
    "v_mfma_f32_16x16x32_bf16 v[152:155], v[196:199], v[216:219], v[152:155]\n"
    "ds_read_b64_tr_b16 v[228:229], v41 offset:4096\n"
    "v_mov_b64 v[90:91], 0\n"
    "v_mfma_f32_16x16x32_bf16 v[156:159], v[196:199], v[220:223], v[156:159]\n"
    "ds_read_b64_tr_b16 v[230:231], v41 offset:4608\n"
    "v_mov_b64 v[92:93], 0\n"
    "v_smfmac_f32_16x16x64_bf16 v[60:63], v[44:47], v[192:199], v48\n"
    "ds_read_b64_tr_b16 v[232:233], v42 offset:4096\n"
    "v_mov_b64 v[94:95], 0\n"
    "v_mfma_f32_16x16x32_bf16 v[160:163], v[200:203], v[208:211], v[160:163]\n"
    "ds_read_b64_tr_b16 v[234:235], v42 offset:4608\n"
    "v_mov_b64 v[96:97], 0\n"
    "v_mfma_f32_16x16x32_bf16 v[164:167], v[200:203], v[212:215], v[164:167]\n"
    "v_add_u32 v40, 49280, v40\n"
    "v_add_u32 v41, 49280, v41\n"
    "v_add_u32 v42, 49280, v42\n"
    "v_mfma_f32_16x16x32_bf16 v[168:171], v[200:203], v[216:219], v[168:171]\n"
    "v_add_u32 v55, 49280, v55\n"
    "v_add_u32 v56, 49280, v56\n"
    "v_mfma_f32_16x16x32_bf16 v[172:175], v[200:203], v[220:223], v[172:175]\n"
    "v_mov_b64 v[98:99], 0\n"
    "v_smfmac_f32_16x16x64_bf16 v[64:67], v[44:47], v[200:207], v48\n"
    "v_mov_b64 v[100:101], 0\n"
    "s_add_u32 m0, s74, 98560\n"
    "s_add_u32 s38, s77, 640\n"
    "buffer_load_dwordx4 v49, s[4:7], s38 offen lds\n"
    "buffer_load_dwordx4 v50, s[4:7], s38 offen offset:1024 lds\n"
    "s_add_u32 m0, s75, 98560\n"
    "s_add_u32 s38, s78, 983040\n"
    "buffer_load_dwordx4 v51, s[28:31], s38 offen lds\n"
    "buffer_load_dwordx4 v52, s[28:31], s38 offen offset:1024 lds\n"
    "buffer_load_dwordx4 v53, s[28:31], s38 offen offset:2048 lds\n"
    "s_add_u32 m0, s76, 98560\n"
    "s_add_u32 s38, s79, 409600\n"
    "buffer_load_dwordx4 v54, s[12:15], s38 offen lds\n"
    "v_mov_b64 v[102:103], 0\n"
    "v_lshrrev_b32 v104, 4, v21\n"
    "s_lshl_b32 s80, s41, 7\n"
    "s_and_b32 s81, s42, 2\n"
    "s_lshl_b32 s81, s81, 1\n"
    "v_mov_b32 v112, 1280\n"
    "v_mov_b32 v113, 15360\n"
    "v_mov_b32 v114, -10240\n"
    "v_mov_b32 v115, 46080\n"
    "v_mov_b32 v116, 5120\n"
    "v_add_u32 v105, s80, v104\n"
    "v_mul_u32_u24 v106, 205, v105\n"
    "s_waitcnt vmcnt(6) lgkmcnt(0)\n"
    "s_barrier\n"
    "v_mfma_f32_16x16x32_bf16 v[128:131], v[224:227], v[240:243], v[128:131]\n"
    "ds_read_b64_tr_b16 v[192:193], v40 offset:0\n"
    "v_mfma_f32_16x16x32_bf16 v[132:135], v[224:227], v[244:247], v[132:135]\n"
    "ds_read_b64_tr_b16 v[194:195], v40 offset:512\n"
    "v_mfma_f32_16x16x32_bf16 v[136:139], v[224:227], v[248:251], v[136:139]\n"
    "ds_read_b128 v[208:211], v55 offset:0\n"
    "v_lshrrev_b32 v106, 11, v106\n"
    "v_mfma_f32_16x16x32_bf16 v[140:143], v[224:227], v[252:255], v[140:143]\n"
    "ds_read_b128 v[212:215], v55 offset:2048\n"
    "v_mad_i32_i24 v107, v106, -10, v105\n"
    "v_mfma_f32_16x16x32_bf16 v[144:147], v[228:231], v[240:243], v[144:147]\n"
    "ds_read_b128 v[216:219], v55 offset:4096\n"
    "v_lshrrev_b32 v108, 1, v107\n"
    "v_mfma_f32_16x16x32_bf16 v[148:151], v[228:231], v[244:247], v[148:151]\n"
    "ds_read_b128 v[220:223], v55 offset:6144\n"
    "v_bfi_b32 v108, v108, 0, s81\n"
    "v_mfma_f32_16x16x32_bf16 v[152:155], v[228:231], v[248:251], v[152:155]\n"
    "ds_read_b64_tr_b16 v[196:197], v41 offset:0\n"
    "v_xor_b32 v107, v107, v108\n"
    "v_mfma_f32_16x16x32_bf16 v[156:159], v[228:231], v[252:255], v[156:159]\n"
    "ds_read_b64_tr_b16 v[198:199], v41 offset:512\n"
    "v_subrev_u32 v107, 0, v107\n"
    "v_smfmac_f32_16x16x64_bf16 v[60:63], v[44:47], v[224:231], v48\n"
    "ds_read_b64_tr_b16 v[200:201], v42 offset:0\n"
    "v_lshlrev_b32 v107, 4, v107\n"
    "v_mfma_f32_16x16x32_bf16 v[160:163], v[232:235], v[240:243], v[160:163]\n"
    "ds_read_b64_tr_b16 v[202:203], v42 offset:512\n"
    "v_mad_u32_u24 v2, v106, v112, v107\n"
    "v_mfma_f32_16x16x32_bf16 v[164:167], v[232:235], v[244:247], v[164:167]\n"
    "v_bfe_u32 v108, v106, 3, 1\n"
    "v_mfma_f32_16x16x32_bf16 v[168:171], v[232:235], v[248:251], v[168:171]\n"
    "v_lshrrev_b32 v109, 4, v106\n"
    "v_mfma_f32_16x16x32_bf16 v[172:175], v[232:235], v[252:255], v[172:175]\n"
    "v_mad_u32_u24 v5, v108, v113, v2\n"
    "v_smfmac_f32_16x16x64_bf16 v[64:67], v[44:47], v[232:239], v48\n"
    "v_mad_i32_i24 v5, v109, v114, v5\n"
    "s_waitcnt lgkmcnt(0)\n"
    "v_mfma_f32_16x16x32_bf16 v[128:131], v[192:195], v[208:211], v[128:131]\n"
    "ds_read_b64_tr_b16 v[224:225], v40 offset:4096\n"
    "v_mfma_f32_16x16x32_bf16 v[132:135], v[192:195], v[212:215], v[132:135]\n"
    "ds_read_b64_tr_b16 v[226:227], v40 offset:4608\n"
    "v_mfma_f32_16x16x32_bf16 v[136:139], v[192:195], v[216:219], v[136:139]\n"
    "ds_read_b128 v[240:243], v56 offset:0\n"
    "v_bfe_u32 v108, v106, 2, 2\n"
    "v_mfma_f32_16x16x32_bf16 v[140:143], v[192:195], v[220:223], v[140:143]\n"
    "ds_read_b128 v[244:247], v56 offset:2048\n"
    "v_mad_u32_u24 v8, v108, v115, v2\n"
    "v_mfma_f32_16x16x32_bf16 v[144:147], v[196:199], v[208:211], v[144:147]\n"
    "ds_read_b128 v[248:251], v56 offset:4096\n"
    "v_mad_u32_u24 v8, v109, v116, v8\n"
    "v_mfma_f32_16x16x32_bf16 v[148:151], v[196:199], v[212:215], v[148:151]\n"
    "ds_read_b128 v[252:255], v56 offset:6144\n"
    "v_add_u32 v105, 64, v105\n"
    "v_mfma_f32_16x16x32_bf16 v[152:155], v[196:199], v[216:219], v[152:155]\n"
    "ds_read_b64_tr_b16 v[228:229], v41 offset:4096\n"
    "v_mul_u32_u24 v106, 205, v105\n"
    "v_mfma_f32_16x16x32_bf16 v[156:159], v[196:199], v[220:223], v[156:159]\n"
    "ds_read_b64_tr_b16 v[230:231], v41 offset:4608\n"
    "v_lshrrev_b32 v106, 11, v106\n"
    "v_smfmac_f32_16x16x64_bf16 v[60:63], v[44:47], v[192:199], v48\n"
    "ds_read_b64_tr_b16 v[232:233], v42 offset:4096\n"
    "v_mad_i32_i24 v107, v106, -10, v105\n"
    "v_mfma_f32_16x16x32_bf16 v[160:163], v[200:203], v[208:211], v[160:163]\n"
    "ds_read_b64_tr_b16 v[234:235], v42 offset:4608\n"
    "v_lshrrev_b32 v108, 1, v107\n"
    "v_mfma_f32_16x16x32_bf16 v[164:167], v[200:203], v[212:215], v[164:167]\n"
    "v_add_u32 v40, 49280, v40\n"
    "v_add_u32 v41, 49280, v41\n"
    "v_add_u32 v42, 49280, v42\n"
    "v_mfma_f32_16x16x32_bf16 v[168:171], v[200:203], v[216:219], v[168:171]\n"
    "v_add_u32 v55, 49280, v55\n"
    "v_add_u32 v56, 49280, v56\n"
    "v_mfma_f32_16x16x32_bf16 v[172:175], v[200:203], v[220:223], v[172:175]\n"
    "v_bfi_b32 v108, v108, 0, s81\n"
    "v_smfmac_f32_16x16x64_bf16 v[64:67], v[44:47], v[200:207], v48\n"
    "v_xor_b32 v107, v107, v108\n"
    "s_add_u32 m0, s74, 0\n"
    "s_add_u32 s38, s77, 768\n"
    "buffer_load_dwordx4 v49, s[4:7], s38 offen lds\n"
    "buffer_load_dwordx4 v50, s[4:7], s38 offen offset:1024 lds\n"
    "s_add_u32 m0, s75, 0\n"
    "s_add_u32 s38, s78, 1179648\n"
    "buffer_load_dwordx4 v51, s[28:31], s38 offen lds\n"
    "buffer_load_dwordx4 v52, s[28:31], s38 offen offset:1024 lds\n"
    "buffer_load_dwordx4 v53, s[28:31], s38 offen offset:2048 lds\n"
    "s_add_u32 m0, s76, 0\n"
    "s_add_u32 s38, s79, 491520\n"
    "buffer_load_dwordx4 v54, s[12:15], s38 offen lds\n"
    "v_subrev_u32 v107, 64, v107\n"
    "v_lshlrev_b32 v107, 4, v107\n"
    "v_mad_u32_u24 v3, v106, v112, v107\n"
    "v_bfe_u32 v108, v106, 3, 1\n"
    "v_lshrrev_b32 v109, 4, v106\n"
    "v_mad_u32_u24 v6, v108, v113, v3\n"
    "v_mad_i32_i24 v6, v109, v114, v6\n"
    "v_bfe_u32 v108, v106, 2, 2\n"
    "v_mad_u32_u24 v9, v108, v115, v3\n"
    "v_mad_u32_u24 v9, v109, v116, v9\n"
    "v_add_u32 v105, 64, v105\n"
    "v_mul_u32_u24 v106, 205, v105\n"
    "s_waitcnt vmcnt(6) lgkmcnt(0)\n"
    "s_barrier\n"
    "v_mfma_f32_16x16x32_bf16 v[128:131], v[224:227], v[240:243], v[128:131]\n"
    "ds_read_b64_tr_b16 v[192:193], v40 offset:0\n"
    "v_mfma_f32_16x16x32_bf16 v[132:135], v[224:227], v[244:247], v[132:135]\n"
    "ds_read_b64_tr_b16 v[194:195], v40 offset:512\n"
    "v_mfma_f32_16x16x32_bf16 v[136:139], v[224:227], v[248:251], v[136:139]\n"
    "ds_read_b128 v[208:211], v55 offset:0\n"
    "v_lshrrev_b32 v106, 11, v106\n"
    "v_mfma_f32_16x16x32_bf16 v[140:143], v[224:227], v[252:255], v[140:143]\n"
    "ds_read_b128 v[212:215], v55 offset:2048\n"
    "v_mad_i32_i24 v107, v106, -10, v105\n"
    "v_mfma_f32_16x16x32_bf16 v[144:147], v[228:231], v[240:243], v[144:147]\n"
    "ds_read_b128 v[216:219], v55 offset:4096\n"
    "v_lshrrev_b32 v108, 1, v107\n"
    "v_mfma_f32_16x16x32_bf16 v[148:151], v[228:231], v[244:247], v[148:151]\n"
    "ds_read_b128 v[220:223], v55 offset:6144\n"
    "v_bfi_b32 v108, v108, 0, s81\n"
    "v_mfma_f32_16x16x32_bf16 v[152:155], v[228:231], v[248:251], v[152:155]\n"
    "ds_read_b64_tr_b16 v[196:197], v41 offset:0\n"
    "v_xor_b32 v107, v107, v108\n"
    "v_mfma_f32_16x16x32_bf16 v[156:159], v[228:231], v[252:255], v[156:159]\n"
    "ds_read_b64_tr_b16 v[198:199], v41 offset:512\n"
    "v_subrev_u32 v107, 128, v107\n"
    "v_smfmac_f32_16x16x64_bf16 v[60:63], v[44:47], v[224:231], v48\n"
    "ds_read_b64_tr_b16 v[200:201], v42 offset:0\n"
    "v_lshlrev_b32 v107, 4, v107\n"
    "v_mfma_f32_16x16x32_bf16 v[160:163], v[232:235], v[240:243], v[160:163]\n"
    "ds_read_b64_tr_b16 v[202:203], v42 offset:512\n"
    "v_mad_u32_u24 v4, v106, v112, v107\n"
    "v_mfma_f32_16x16x32_bf16 v[164:167], v[232:235], v[244:247], v[164:167]\n"
    "v_bfe_u32 v108, v106, 3, 1\n"
    "v_mfma_f32_16x16x32_bf16 v[168:171], v[232:235], v[248:251], v[168:171]\n"
    "v_lshrrev_b32 v109, 4, v106\n"
    "v_mfma_f32_16x16x32_bf16 v[172:175], v[232:235], v[252:255], v[172:175]\n"
    "v_mad_u32_u24 v7, v108, v113, v4\n"
    "v_smfmac_f32_16x16x64_bf16 v[64:67], v[44:47], v[232:239], v48\n"
    "v_mad_i32_i24 v7, v109, v114, v7\n"
    "s_waitcnt lgkmcnt(0)\n"
    "v_mfma_f32_16x16x32_bf16 v[128:131], v[192:195], v[208:211], v[128:131]\n"
    "ds_read_b64_tr_b16 v[224:225], v40 offset:4096\n"
    "v_mfma_f32_16x16x32_bf16 v[132:135], v[192:195], v[212:215], v[132:135]\n"
    "ds_read_b64_tr_b16 v[226:227], v40 offset:4608\n"
    "v_mfma_f32_16x16x32_bf16 v[136:139], v[192:195], v[216:219], v[136:139]\n"
    "ds_read_b128 v[240:243], v56 offset:0\n"
    "v_bfe_u32 v108, v106, 2, 2\n"
    "v_mfma_f32_16x16x32_bf16 v[140:143], v[192:195], v[220:223], v[140:143]\n"
    "ds_read_b128 v[244:247], v56 offset:2048\n"
    "v_mad_u32_u24 v10, v108, v115, v4\n"
    "v_mfma_f32_16x16x32_bf16 v[144:147], v[196:199], v[208:211], v[144:147]\n"
    "ds_read_b128 v[248:251], v56 offset:4096\n"
    "v_mad_u32_u24 v10, v109, v116, v10\n"
    "v_mfma_f32_16x16x32_bf16 v[148:151], v[196:199], v[212:215], v[148:151]\n"
    "ds_read_b128 v[252:255], v56 offset:6144\n"
    "v_lshrrev_b32 v104, 4, v21\n"
    "v_mfma_f32_16x16x32_bf16 v[152:155], v[196:199], v[216:219], v[152:155]\n"
    "ds_read_b64_tr_b16 v[228:229], v41 offset:4096\n"
    "v_bfe_u32 v105, v104, 3, 2\n"
    "v_mfma_f32_16x16x32_bf16 v[156:159], v[196:199], v[220:223], v[156:159]\n"
    "ds_read_b64_tr_b16 v[230:231], v41 offset:4608\n"
    "v_lshrrev_b32 v106, 5, v104\n"
    "v_smfmac_f32_16x16x64_bf16 v[60:63], v[44:47], v[192:199], v48\n"
    "ds_read_b64_tr_b16 v[232:233], v42 offset:4096\n"
    "v_and_b32 v107, 7, v104\n"
    "v_mfma_f32_16x16x32_bf16 v[160:163], v[200:203], v[208:211], v[160:163]\n"
    "ds_read_b64_tr_b16 v[234:235], v42 offset:4608\n"
    "v_and_b32 v108, 31, v104\n"
    "v_mfma_f32_16x16x32_bf16 v[164:167], v[200:203], v[212:215], v[164:167]\n"
    "v_add_u32 v40, -98560, v40\n"
    "v_add_u32 v41, -98560, v41\n"
    "v_add_u32 v42, -98560, v42\n"
    "v_mfma_f32_16x16x32_bf16 v[168:171], v[200:203], v[216:219], v[168:171]\n"
    "v_add_u32 v55, -98560, v55\n"
    "v_add_u32 v56, -98560, v56\n"
    "v_mfma_f32_16x16x32_bf16 v[172:175], v[200:203], v[220:223], v[172:175]\n"
    "v_and_b32 v109, 15, v104\n"
    "v_smfmac_f32_16x16x64_bf16 v[64:67], v[44:47], v[200:207], v48\n"
    "v_lshrrev_b32 v110, 4, v104\n"
    "s_add_u32 m0, s74, 49280\n"
    "s_add_u32 s38, s77, 896\n"
    "buffer_load_dwordx4 v49, s[4:7], s38 offen lds\n"
    "buffer_load_dwordx4 v50, s[4:7], s38 offen offset:1024 lds\n"
    "s_add_u32 m0, s75, 49280\n"
    "s_add_u32 s38, s78, 1376256\n"
    "buffer_load_dwordx4 v51, s[28:31], s38 offen lds\n"
    "buffer_load_dwordx4 v52, s[28:31], s38 offen offset:1024 lds\n"
    "buffer_load_dwordx4 v53, s[28:31], s38 offen offset:2048 lds\n"
    "s_add_u32 m0, s76, 49280\n"
    "s_add_u32 s38, s79, 573440\n"
    "buffer_load_dwordx4 v54, s[12:15], s38 offen lds\n"
    "s_mul_i32 s80, s41, 3072\n"
    "s_add_u32 s80, s80, 49280\n"
    "v_mul_u32_u24 v111, 768, v105\n"
    "v_add_u32 v111, s80, v111\n"
    "v_and_b32 v112, 1, v105\n"
    "v_xor_b32 v113, v112, v106\n"
    "v_lshl_add_u32 v113, v113, 3, v107\n"
    "v_lshl_add_u32 v11, v113, 4, v111\n"
    "v_lshl_add_u32 v113, v112, 3, v107\n"
    "v_lshrrev_b32 v114, 1, v105\n"
    "v_xor_b32 v114, v114, v106\n"
    "v_lshl_add_u32 v113, v113, 1, v114\n"
    "s_waitcnt vmcnt(6) lgkmcnt(0)\n"
    "s_barrier\n"
    "v_mfma_f32_16x16x32_bf16 v[128:131], v[224:227], v[240:243], v[128:131]\n"
    "ds_read_b64_tr_b16 v[192:193], v40 offset:0\n"
    "v_mfma_f32_16x16x32_bf16 v[132:135], v[224:227], v[244:247], v[132:135]\n"
    "ds_read_b64_tr_b16 v[194:195], v40 offset:512\n"
    "v_mfma_f32_16x16x32_bf16 v[136:139], v[224:227], v[248:251], v[136:139]\n"
    "ds_read_b128 v[208:211], v55 offset:0\n"
    "v_lshl_add_u32 v113, v113, 2, v111\n"
    "v_mfma_f32_16x16x32_bf16 v[140:143], v[224:227], v[252:255], v[140:143]\n"
    "ds_read_b128 v[212:215], v55 offset:2048\n"
    "v_add_u32 v12, 256, v113\n"
    "v_mfma_f32_16x16x32_bf16 v[144:147], v[228:231], v[240:243], v[144:147]\n"
    "ds_read_b128 v[216:219], v55 offset:4096\n"
    "v_and_b32 v113, 28, v108\n"
    "v_mfma_f32_16x16x32_bf16 v[148:151], v[228:231], v[244:247], v[148:151]\n"
    "ds_read_b128 v[220:223], v55 offset:6144\n"
    "v_mov_b32 v114, 0x90dd8\n"
    "v_mfma_f32_16x16x32_bf16 v[152:155], v[228:231], v[248:251], v[152:155]\n"
    "ds_read_b64_tr_b16 v[196:197], v41 offset:0\n"
    "v_bfe_u32 v113, v114, v113, 4\n"
    "v_mfma_f32_16x16x32_bf16 v[156:159], v[228:231], v[252:255], v[156:159]\n"
    "ds_read_b64_tr_b16 v[198:199], v41 offset:512\n"
    "v_add_u32 v113, v113, v108\n"
    "v_smfmac_f32_16x16x64_bf16 v[60:63], v[44:47], v[224:231], v48\n"
    "ds_read_b64_tr_b16 v[200:201], v42 offset:0\n"
    "v_cmp_gt_u32 vcc, 20, v108\n"
    "v_mfma_f32_16x16x32_bf16 v[160:163], v[232:235], v[240:243], v[160:163]\n"
    "ds_read_b64_tr_b16 v[202:203], v42 offset:512\n"
    "s_add_u32 s80, s80, 256\n"
    "v_mfma_f32_16x16x32_bf16 v[164:167], v[232:235], v[244:247], v[164:167]\n"
    "v_mul_u32_u24 v114, 768, v106\n"
    "v_mfma_f32_16x16x32_bf16 v[168:171], v[232:235], v[248:251], v[168:171]\n"
    "v_cndmask_b32 v113, 29, v113, vcc\n"
    "v_mfma_f32_16x16x32_bf16 v[172:175], v[232:235], v[252:255], v[172:175]\n"
    "v_and_b32 v115, 31, v108\n"
    "v_smfmac_f32_16x16x64_bf16 v[64:67], v[44:47], v[232:239], v48\n"
    "v_cmp_ne_u32 vcc, 24, v115\n"
    "s_waitcnt lgkmcnt(0)\n"
    "v_mfma_f32_16x16x32_bf16 v[128:131], v[192:195], v[208:211], v[128:131]\n"
    "ds_read_b64_tr_b16 v[224:225], v40 offset:4096\n"
    "v_mfma_f32_16x16x32_bf16 v[132:135], v[192:195], v[212:215], v[132:135]\n"
    "ds_read_b64_tr_b16 v[226:227], v40 offset:4608\n"
    "v_mfma_f32_16x16x32_bf16 v[136:139], v[192:195], v[216:219], v[136:139]\n"
    "ds_read_b128 v[240:243], v56 offset:0\n"
    "s_nop 1\n"
    "v_mfma_f32_16x16x32_bf16 v[140:143], v[192:195], v[220:223], v[140:143]\n"
    "ds_read_b128 v[244:247], v56 offset:2048\n"
    "v_cndmask_b32 v113, 16, v113, vcc\n"
    "v_mfma_f32_16x16x32_bf16 v[144:147], v[196:199], v[208:211], v[144:147]\n"
    "ds_read_b128 v[248:251], v56 offset:4096\n"
    "v_lshl_add_u32 v113, v113, 4, v114\n"
    "v_mfma_f32_16x16x32_bf16 v[148:151], v[196:199], v[212:215], v[148:151]\n"
    "ds_read_b128 v[252:255], v56 offset:6144\n"
    "v_add_u32 v13, s80, v113\n"
    "v_mfma_f32_16x16x32_bf16 v[152:155], v[196:199], v[216:219], v[152:155]\n"
    "ds_read_b64_tr_b16 v[228:229], v41 offset:4096\n"
    "v_and_b32 v113, 3, v104\n"
    "v_mfma_f32_16x16x32_bf16 v[156:159], v[196:199], v[220:223], v[156:159]\n"
    "ds_read_b64_tr_b16 v[230:231], v41 offset:4608\n"
    "v_lshlrev_b32 v14, 3, v113\n"
    "v_smfmac_f32_16x16x64_bf16 v[60:63], v[44:47], v[192:199], v48\n"
    "ds_read_b64_tr_b16 v[232:233], v42 offset:4096\n"
    "v_lshrrev_b32 v113, 2, v104\n"
    "v_mfma_f32_16x16x32_bf16 v[160:163], v[200:203], v[208:211], v[160:163]\n"
    "ds_read_b64_tr_b16 v[234:235], v42 offset:4608\n"
    "v_mul_u32_u24 v113, 160, v113\n"
    "v_mfma_f32_16x16x32_bf16 v[164:167], v[200:203], v[212:215], v[164:167]\n"
    "v_add_u32 v40, 49280, v40\n"
    "v_add_u32 v41, 49280, v41\n"
    "v_add_u32 v42, 49280, v42\n"
    "v_mfma_f32_16x16x32_bf16 v[168:171], v[200:203], v[216:219], v[168:171]\n"
    "v_add_u32 v55, 49280, v55\n"
    "v_add_u32 v56, 49280, v56\n"
    "v_mfma_f32_16x16x32_bf16 v[172:175], v[200:203], v[220:223], v[172:175]\n"
    "s_mul_i32 s80, s42, 5120\n"
    "v_smfmac_f32_16x16x64_bf16 v[64:67], v[44:47], v[200:207], v48\n"
    "v_add3_u32 v15, v113, v14, s80\n"
    "s_add_u32 m0, s74, 98560\n"
    "s_add_u32 s38, s77, 1024\n"
    "buffer_load_dwordx4 v49, s[4:7], s38 offen lds\n"
    "buffer_load_dwordx4 v50, s[4:7], s38 offen offset:1024 lds\n"
    "s_add_u32 m0, s75, 98560\n"
    "s_add_u32 s38, s78, 1572864\n"
    "buffer_load_dwordx4 v51, s[28:31], s38 offen lds\n"
    "buffer_load_dwordx4 v52, s[28:31], s38 offen offset:1024 lds\n"
    "buffer_load_dwordx4 v53, s[28:31], s38 offen offset:2048 lds\n"
    "s_add_u32 m0, s76, 98560\n"
    "s_add_u32 s38, s79, 655360\n"
    "buffer_load_dwordx4 v54, s[12:15], s38 offen lds\n"
    "v_mul_u32_u24 v113, 1280, v109\n"
    "v_lshl_add_u32 v16, v110, 3, v113\n"
    "v_lshlrev_b32 v113, 3, v109\n"
    "v_lshl_add_u32 v18, v110, 8, v113\n"
    "v_and_b32 v114, 1, v110\n"
    "v_lshl_add_u32 v115, v114, 12, v113\n"
    "v_lshl_add_u32 v20, v106, 30, v115\n"
    "v_lshlrev_b32 v114, 14, v114\n"
    "v_lshl_add_u32 v114, v106, 8, v114\n"
    "v_lshl_add_u32 v19, v109, 4, v114\n"
    "s_lshl_b32 s80, s41, 9\n"
    "s_add_u32 s80, s80, 161280\n"
    "s_waitcnt vmcnt(6) lgkmcnt(0)\n"
    "s_barrier\n"
    "v_mfma_f32_16x16x32_bf16 v[128:131], v[224:227], v[240:243], v[128:131]\n"
    "ds_read_b64_tr_b16 v[192:193], v40 offset:0\n"
    "v_mfma_f32_16x16x32_bf16 v[132:135], v[224:227], v[244:247], v[132:135]\n"
    "ds_read_b64_tr_b16 v[194:195], v40 offset:512\n"
    "v_mfma_f32_16x16x32_bf16 v[136:139], v[224:227], v[248:251], v[136:139]\n"
    "ds_read_b128 v[208:211], v55 offset:0\n"
    "v_mfma_f32_16x16x32_bf16 v[140:143], v[224:227], v[252:255], v[140:143]\n"
    "ds_read_b128 v[212:215], v55 offset:2048\n"
    "v_mfma_f32_16x16x32_bf16 v[144:147], v[228:231], v[240:243], v[144:147]\n"
    "ds_read_b128 v[216:219], v55 offset:4096\n"
    "v_mfma_f32_16x16x32_bf16 v[148:151], v[228:231], v[244:247], v[148:151]\n"
    "ds_read_b128 v[220:223], v55 offset:6144\n"
    "v_mfma_f32_16x16x32_bf16 v[152:155], v[228:231], v[248:251], v[152:155]\n"
    "ds_read_b64_tr_b16 v[196:197], v41 offset:0\n"
    "v_mfma_f32_16x16x32_bf16 v[156:159], v[228:231], v[252:255], v[156:159]\n"
    "ds_read_b64_tr_b16 v[198:199], v41 offset:512\n"
    "v_smfmac_f32_16x16x64_bf16 v[60:63], v[44:47], v[224:231], v48\n"
    "ds_read_b64_tr_b16 v[200:201], v42 offset:0\n"
    "v_mfma_f32_16x16x32_bf16 v[160:163], v[232:235], v[240:243], v[160:163]\n"
    "ds_read_b64_tr_b16 v[202:203], v42 offset:512\n"
    "v_mfma_f32_16x16x32_bf16 v[164:167], v[232:235], v[244:247], v[164:167]\n"
    "v_mfma_f32_16x16x32_bf16 v[168:171], v[232:235], v[248:251], v[168:171]\n"
    "v_mfma_f32_16x16x32_bf16 v[172:175], v[232:235], v[252:255], v[172:175]\n"
    "v_smfmac_f32_16x16x64_bf16 v[64:67], v[44:47], v[232:239], v48\n"
    "s_waitcnt lgkmcnt(0)\n"
    "v_mfma_f32_16x16x32_bf16 v[128:131], v[192:195], v[208:211], v[128:131]\n"
    "ds_read_b64_tr_b16 v[224:225], v40 offset:4096\n"
    "v_mfma_f32_16x16x32_bf16 v[132:135], v[192:195], v[212:215], v[132:135]\n"
    "ds_read_b64_tr_b16 v[226:227], v40 offset:4608\n"
    "v_mfma_f32_16x16x32_bf16 v[136:139], v[192:195], v[216:219], v[136:139]\n"
    "ds_read_b128 v[240:243], v56 offset:0\n"
    "v_mfma_f32_16x16x32_bf16 v[140:143], v[192:195], v[220:223], v[140:143]\n"
    "ds_read_b128 v[244:247], v56 offset:2048\n"
    "v_mfma_f32_16x16x32_bf16 v[144:147], v[196:199], v[208:211], v[144:147]\n"
    "ds_read_b128 v[248:251], v56 offset:4096\n"
    "v_mfma_f32_16x16x32_bf16 v[148:151], v[196:199], v[212:215], v[148:151]\n"
    "ds_read_b128 v[252:255], v56 offset:6144\n"
    "v_mfma_f32_16x16x32_bf16 v[152:155], v[196:199], v[216:219], v[152:155]\n"
    "ds_read_b64_tr_b16 v[228:229], v41 offset:4096\n"
    "v_mfma_f32_16x16x32_bf16 v[156:159], v[196:199], v[220:223], v[156:159]\n"
    "ds_read_b64_tr_b16 v[230:231], v41 offset:4608\n"
    "v_smfmac_f32_16x16x64_bf16 v[60:63], v[44:47], v[192:199], v48\n"
    "ds_read_b64_tr_b16 v[232:233], v42 offset:4096\n"
    "v_mfma_f32_16x16x32_bf16 v[160:163], v[200:203], v[208:211], v[160:163]\n"
    "ds_read_b64_tr_b16 v[234:235], v42 offset:4608\n"
    "v_mfma_f32_16x16x32_bf16 v[164:167], v[200:203], v[212:215], v[164:167]\n"
    "v_add_u32 v40, 49280, v40\n"
    "v_add_u32 v41, 49280, v41\n"
    "v_add_u32 v42, 49280, v42\n"
    "v_mfma_f32_16x16x32_bf16 v[168:171], v[200:203], v[216:219], v[168:171]\n"
    "v_add_u32 v55, 49280, v55\n"
    "v_add_u32 v56, 49280, v56\n"
    "v_mfma_f32_16x16x32_bf16 v[172:175], v[200:203], v[220:223], v[172:175]\n"
    "v_smfmac_f32_16x16x64_bf16 v[64:67], v[44:47], v[200:207], v48\n"
    "s_add_u32 m0, s74, 0\n"
    "s_add_u32 s38, s77, 1152\n"
    "buffer_load_dwordx4 v49, s[4:7], s38 offen lds\n"
    "buffer_load_dwordx4 v50, s[4:7], s38 offen offset:1024 lds\n"
    "s_add_u32 m0, s75, 0\n"
    "s_add_u32 s38, s78, 1769472\n"
    "buffer_load_dwordx4 v51, s[28:31], s38 offen lds\n"
    "buffer_load_dwordx4 v52, s[28:31], s38 offen offset:1024 lds\n"
    "buffer_load_dwordx4 v53, s[28:31], s38 offen offset:2048 lds\n"
    "s_add_u32 m0, s76, 0\n"
    "s_add_u32 s38, s79, 737280\n"
    "buffer_load_dwordx4 v54, s[12:15], s38 offen lds\n"
    "v_add_u32 v23, s80, v113\n"
    "s_mul_i32 s80, s41, 3072\n"
    "s_add_u32 s80, s80, 147840\n"
    "v_mul_u32_u24 v113, 48, v109\n"
    "v_add_u32 v24, s80, v113\n"
    "s_lshr_b32 s80, s64, 1\n"
    "s_mul_i32 s80, s80, 1536\n"
    "s_mul_i32 s81, s41, 6144\n"
    "s_add_u32 s82, s80, s81\n"
    "s_sub_u32 s80, s80, s81\n"
    "s_add_u32 s82, s82, 147840\n"
    "s_add_u32 s80, s80, 153984\n"
    "v_mul_u32_u24 v113, 48, v108\n"
    "v_lshl_add_u32 v113, v106, 2, v113\n"
    "v_add_u32 v25, s80, v113\n"
    "v_add_u32 v26, s82, v113\n"
    "v_mad_u32_u24 v118, v106, 12, v25\n"
    "v_mad_u32_u24 v119, v106, 12, v26\n"
    "v_mov_b32 v27, 1.0\n"
    "s_lshl_b32 s80, s46, 2\n"
    "s_add_u32 s80, s80, 16384\n"
    "s_and_b32 s82, s59, 24\n"
    "s_andn2_b32 s81, s59, 24\n"
    "s_lshl_b32 s81, s81, 2\n"
    "s_add_u32 s81, s81, s82\n"
    "s_add_u32 s81, s81, 16384\n"
    "s_waitcnt vmcnt(6) lgkmcnt(0)\n"
    "s_barrier\n"
    "v_mfma_f32_16x16x32_bf16 v[128:131], v[224:227], v[240:243], v[128:131]\n"
    "ds_read_b64_tr_b16 v[192:193], v40 offset:0\n"
    "v_mfma_f32_16x16x32_bf16 v[132:135], v[224:227], v[244:247], v[132:135]\n"
    "ds_read_b64_tr_b16 v[194:195], v40 offset:512\n"
    "v_mfma_f32_16x16x32_bf16 v[136:139], v[224:227], v[248:251], v[136:139]\n"
    "ds_read_b128 v[208:211], v55 offset:0\n"
    "v_mfma_f32_16x16x32_bf16 v[140:143], v[224:227], v[252:255], v[140:143]\n"
    "ds_read_b128 v[212:215], v55 offset:2048\n"
    "v_mfma_f32_16x16x32_bf16 v[144:147], v[228:231], v[240:243], v[144:147]\n"
    "ds_read_b128 v[216:219], v55 offset:4096\n"
    "v_mfma_f32_16x16x32_bf16 v[148:151], v[228:231], v[244:247], v[148:151]\n"
    "ds_read_b128 v[220:223], v55 offset:6144\n"
    "v_mfma_f32_16x16x32_bf16 v[152:155], v[228:231], v[248:251], v[152:155]\n"
    "ds_read_b64_tr_b16 v[196:197], v41 offset:0\n"
    "v_mfma_f32_16x16x32_bf16 v[156:159], v[228:231], v[252:255], v[156:159]\n"
    "ds_read_b64_tr_b16 v[198:199], v41 offset:512\n"
    "v_smfmac_f32_16x16x64_bf16 v[60:63], v[44:47], v[224:231], v48\n"
    "ds_read_b64_tr_b16 v[200:201], v42 offset:0\n"
    "v_mfma_f32_16x16x32_bf16 v[160:163], v[232:235], v[240:243], v[160:163]\n"
    "ds_read_b64_tr_b16 v[202:203], v42 offset:512\n"
    "v_mfma_f32_16x16x32_bf16 v[164:167], v[232:235], v[244:247], v[164:167]\n"
    "v_mfma_f32_16x16x32_bf16 v[168:171], v[232:235], v[248:251], v[168:171]\n"
    "v_mfma_f32_16x16x32_bf16 v[172:175], v[232:235], v[252:255], v[172:175]\n"
    "v_smfmac_f32_16x16x64_bf16 v[64:67], v[44:47], v[232:239], v48\n"
    "s_waitcnt lgkmcnt(0)\n"
    "v_mfma_f32_16x16x32_bf16 v[128:131], v[192:195], v[208:211], v[128:131]\n"
    "ds_read_b64_tr_b16 v[224:225], v40 offset:4096\n"
    "v_mfma_f32_16x16x32_bf16 v[132:135], v[192:195], v[212:215], v[132:135]\n"
    "ds_read_b64_tr_b16 v[226:227], v40 offset:4608\n"
    "v_mfma_f32_16x16x32_bf16 v[136:139], v[192:195], v[216:219], v[136:139]\n"
    "ds_read_b128 v[240:243], v56 offset:0\n"
    "v_mfma_f32_16x16x32_bf16 v[140:143], v[192:195], v[220:223], v[140:143]\n"
    "ds_read_b128 v[244:247], v56 offset:2048\n"
    "v_mfma_f32_16x16x32_bf16 v[144:147], v[196:199], v[208:211], v[144:147]\n"
    "ds_read_b128 v[248:251], v56 offset:4096\n"
    "v_mfma_f32_16x16x32_bf16 v[148:151], v[196:199], v[212:215], v[148:151]\n"
    "ds_read_b128 v[252:255], v56 offset:6144\n"
    "v_mfma_f32_16x16x32_bf16 v[152:155], v[196:199], v[216:219], v[152:155]\n"
    "ds_read_b64_tr_b16 v[228:229], v41 offset:4096\n"
    "v_mfma_f32_16x16x32_bf16 v[156:159], v[196:199], v[220:223], v[156:159]\n"
    "ds_read_b64_tr_b16 v[230:231], v41 offset:4608\n"
    "v_smfmac_f32_16x16x64_bf16 v[60:63], v[44:47], v[192:199], v48\n"
    "ds_read_b64_tr_b16 v[232:233], v42 offset:4096\n"
    "v_mfma_f32_16x16x32_bf16 v[160:163], v[200:203], v[208:211], v[160:163]\n"
    "ds_read_b64_tr_b16 v[234:235], v42 offset:4608\n"
    "v_mfma_f32_16x16x32_bf16 v[164:167], v[200:203], v[212:215], v[164:167]\n"
    "v_add_u32 v40, -98560, v40\n"
    "v_add_u32 v41, -98560, v41\n"
    "v_add_u32 v42, -98560, v42\n"
    "v_mfma_f32_16x16x32_bf16 v[168:171], v[200:203], v[216:219], v[168:171]\n"
    "v_add_u32 v55, -98560, v55\n"
    "v_add_u32 v56, -98560, v56\n"
    "v_mfma_f32_16x16x32_bf16 v[172:175], v[200:203], v[220:223], v[172:175]\n"
    "v_smfmac_f32_16x16x64_bf16 v[64:67], v[44:47], v[200:207], v48\n"
    "v_add_u32 v58, 160256, v21\n"
    "ds_read_b128 v[68:71], v58\n"
    "s_mov_b32 s36, 0x10000\n"
    "s_waitcnt lgkmcnt(0)\n"
    "Cb_%=:\n"
    "v_cmp_ne_u32 vcc, s37, v69\n"
    "s_nop 1\n"
    "s_cbranch_vccnz Sb_%=\n"
    "v_cmp_ne_u32 vcc, s37, v71\n"
    "s_nop 1\n"
    "s_cbranch_vccz Kb_%=\n"
    "Sb_%=:\n"
    "s_sleep 2\n"
    "s_movk_i32 s38, 1024\n"
    "buffer_load_dwordx4 v[68:71], v21, s[8:11], s38 offen sc1\n"
    "s_sub_u32 s36, s36, 1\n"
    "s_cmp_lg_u32 s36, 0\n"
    "s_waitcnt vmcnt(0)\n"
    "s_cbranch_scc1 Cb_%=\n"
    "Kb_%=:\n"
    "s_mov_b32 m0, s61\n"
    "s_add_u32 s38, s62, 24576\n"
    "buffer_load_dwordx4 v21, s[8:11], s62 offen sc1 lds\n"
    "buffer_load_dwordx4 v21, s[8:11], s62 offen offset:1024 sc1 lds\n"
    "buffer_load_dwordx4 v21, s[8:11], s62 offen offset:2048 sc1 lds\n"
    "s_add_u32 m0, s61, 24576\n"
    "v_max_f32 v68, v68, v70\n"
    "buffer_load_dwordx4 v21, s[8:11], s38 offen sc1 lds\n"
    "buffer_load_dwordx4 v21, s[8:11], s38 offen offset:1024 sc1 lds\n"
    "buffer_load_dwordx4 v21, s[8:11], s38 offen offset:2048 sc1 lds\n"
    "s_nop 1\n"
    "v_max_f32_dpp v68, v68, v68 quad_perm:[1,0,3,2] row_mask:0xf bank_mask:0xf\n"
    "s_nop 1\n"
    "v_max_f32_dpp v68, v68, v68 quad_perm:[2,3,0,1] row_mask:0xf bank_mask:0xf\n"
    "s_nop 1\n"
    "v_max_f32_dpp v68, v68, v68 row_half_mirror row_mask:0xf bank_mask:0xf\n"
    "s_nop 1\n"
    "v_max_f32_dpp v68, v68, v68 row_mirror row_mask:0xf bank_mask:0xf\n"
    "s_nop 1\n"
    "v_max_f32_dpp v68, v68, v68 row_bcast:15 row_mask:0xf bank_mask:0xf\n"
    "s_nop 1\n"
    "v_max_f32_dpp v68, v68, v68 row_bcast:31 row_mask:0xf bank_mask:0xf\n"
    "s_nop 1\n"
    "v_readlane_b32 s63, v68, 63\n"
    "s_waitcnt vmcnt(6) lgkmcnt(0)\n"
    "s_barrier\n"
    "v_mfma_f32_16x16x32_bf16 v[128:131], v[224:227], v[240:243], v[128:131]\n"
    "ds_read_b64_tr_b16 v[192:193], v40 offset:0\n"
    "v_mfma_f32_16x16x32_bf16 v[132:135], v[224:227], v[244:247], v[132:135]\n"
    "ds_read_b64_tr_b16 v[194:195], v40 offset:512\n"
    "v_mfma_f32_16x16x32_bf16 v[136:139], v[224:227], v[248:251], v[136:139]\n"
    "ds_read_b128 v[208:211], v55 offset:0\n"
    "v_mfma_f32_16x16x32_bf16 v[140:143], v[224:227], v[252:255], v[140:143]\n"
    "ds_read_b128 v[212:215], v55 offset:2048\n"
    "v_mfma_f32_16x16x32_bf16 v[144:147], v[228:231], v[240:243], v[144:147]\n"
    "ds_read_b128 v[216:219], v55 offset:4096\n"
    "v_mfma_f32_16x16x32_bf16 v[148:151], v[228:231], v[244:247], v[148:151]\n"
    "ds_read_b128 v[220:223], v55 offset:6144\n"
    "v_mfma_f32_16x16x32_bf16 v[152:155], v[228:231], v[248:251], v[152:155]\n"
    "ds_read_b64_tr_b16 v[196:197], v41 offset:0\n"
    "v_mfma_f32_16x16x32_bf16 v[156:159], v[228:231], v[252:255], v[156:159]\n"
    "ds_read_b64_tr_b16 v[198:199], v41 offset:512\n"
    "v_smfmac_f32_16x16x64_bf16 v[60:63], v[44:47], v[224:231], v48\n"
    "ds_read_b64_tr_b16 v[200:201], v42 offset:0\n"
    "v_mfma_f32_16x16x32_bf16 v[160:163], v[232:235], v[240:243], v[160:163]\n"
    "ds_read_b64_tr_b16 v[202:203], v42 offset:512\n"
    "v_mfma_f32_16x16x32_bf16 v[164:167], v[232:235], v[244:247], v[164:167]\n"
    "ds_read2_b64 v[120:123], v23 offset1:16\n"
    "ds_read2_b64 v[124:127], v23 offset0:32 offset1:48\n"
    "v_mfma_f32_16x16x32_bf16 v[168:171], v[232:235], v[248:251], v[168:171]\n"
    "v_mfma_f32_16x16x32_bf16 v[172:175], v[232:235], v[252:255], v[172:175]\n"
    "v_smfmac_f32_16x16x64_bf16 v[64:67], v[44:47], v[232:239], v48\n"
    "s_waitcnt lgkmcnt(0)\n"
    "v_mfma_f32_16x16x32_bf16 v[128:131], v[192:195], v[208:211], v[128:131]\n"
    "ds_read_b64_tr_b16 v[224:225], v40 offset:4096\n"
    "v_mul_f32 v50, 0x3ea52b5b, v121\n"
    "v_mul_f32 v52, 0x3ea52b5b, v123\n"
    "v_mfma_f32_16x16x32_bf16 v[132:135], v[192:195], v[212:215], v[132:135]\n"
    "ds_read_b64_tr_b16 v[226:227], v40 offset:4608\n"
    "v_mul_f32 v54, 0x3ea52b5b, v125\n"
    "v_mul_f32 v70, 0x3ea52b5b, v127\n"
    "v_mfma_f32_16x16x32_bf16 v[136:139], v[192:195], v[216:219], v[136:139]\n"
    "ds_read_b128 v[240:243], v56 offset:0\n"
    "v_mov_b64 v[68:69], v[110:111]\n"
    "v_mfma_f32_16x16x32_bf16 v[140:143], v[192:195], v[220:223], v[140:143]\n"
    "ds_read_b128 v[244:247], v56 offset:2048\n"
    "v_mfma_f32_16x16x32_bf16 v[144:147], v[196:199], v[208:211], v[144:147]\n"
    "ds_read_b128 v[248:251], v56 offset:4096\n"
    "v_mfma_f32_16x16x32_bf16 v[148:151], v[196:199], v[212:215], v[148:151]\n"
    "ds_read_b128 v[252:255], v56 offset:6144\n"
    "v_mfma_f32_16x16x32_bf16 v[152:155], v[196:199], v[216:219], v[152:155]\n"
    "ds_read_b64_tr_b16 v[228:229], v41 offset:4096\n"
    "v_mfma_f32_16x16x32_bf16 v[156:159], v[196:199], v[220:223], v[156:159]\n"
    "ds_read_b64_tr_b16 v[230:231], v41 offset:4608\n"
    "v_smfmac_f32_16x16x64_bf16 v[60:63], v[44:47], v[192:199], v48\n"
    "ds_read_b64_tr_b16 v[232:233], v42 offset:4096\n"
    "v_mfma_f32_16x16x32_bf16 v[160:163], v[200:203], v[208:211], v[160:163]\n"
    "ds_read_b64_tr_b16 v[234:235], v42 offset:4608\n"
    "v_mfma_f32_16x16x32_bf16 v[164:167], v[200:203], v[212:215], v[164:167]\n"
    "v_mov_b64 v[104:105], -1\n"
    "v_mov_b64 v[106:107], -1\n"
    "buffer_store_dwordx4 v[104:107], v19, s[8:11], s57 offen sc1\n"
    "buffer_store_dwordx2 v[104:105], v20, s[8:11], s58 offen sc1\n"
    "v_mfma_f32_16x16x32_bf16 v[168:171], v[200:203], v[216:219], v[168:171]\n"
    "v_mfma_f32_16x16x32_bf16 v[172:175], v[200:203], v[220:223], v[172:175]\n"
    "v_smfmac_f32_16x16x64_bf16 v[64:67], v[44:47], v[200:207], v48\n"
    "s_waitcnt lgkmcnt(0)\n"
    "v_smfmac_f32_16x16x64_bf16 v[60:63], v[44:47], v[224:231], v48\n"
    "v_smfmac_f32_16x16x64_bf16 v[64:67], v[44:47], v[232:239], v48\n"
    "v_mfma_f32_16x16x32_bf16 v[128:131], v[224:227], v[240:243], v[128:131]\n"
    "v_mfma_f32_16x16x32_bf16 v[144:147], v[228:231], v[240:243], v[144:147]\n"
    "v_mfma_f32_16x16x32_bf16 v[160:163], v[232:235], v[240:243], v[160:163]\n"
    "v_lshrrev_b32 v58, 8, v21\n"
    "v_and_b32 v59, 1, v58\n"
    "v_lshl_add_u32 v57, v58, 3, v24\n"
    "v_mul_u32_u24 v59, 6140, v59\n"
    "v_lshl_add_u32 v58, v58, 2, v24\n"
    "v_add_u32 v58, v58, v59\n"
    "v_mfma_f32_16x16x4_f32 v[192:195], v60, v27, 0\n"
    "v_mfma_f32_16x16x4_f32 v[196:199], v61, v27, 0\n"
    "v_mfma_f32_16x16x4_f32 v[200:203], v64, v27, 0\n"
    "v_mfma_f32_16x16x32_bf16 v[132:135], v[224:227], v[244:247], v[132:135]\n"
    "v_mfma_f32_16x16x32_bf16 v[148:151], v[228:231], v[244:247], v[148:151]\n"
    "v_mfma_f32_16x16x32_bf16 v[164:167], v[232:235], v[244:247], v[164:167]\n"
    "v_mfma_f32_16x16x32_bf16 v[136:139], v[224:227], v[248:251], v[136:139]\n"
    "v_mfma_f32_16x16x32_bf16 v[152:155], v[228:231], v[248:251], v[152:155]\n"
    "v_mfma_f32_16x16x32_bf16 v[168:171], v[232:235], v[248:251], v[168:171]\n"
    "v_mfma_f32_16x16x32_bf16 v[140:143], v[224:227], v[252:255], v[140:143]\n"
    "v_mfma_f32_16x16x32_bf16 v[156:159], v[228:231], v[252:255], v[156:159]\n"
    "v_mfma_f32_16x16x32_bf16 v[172:175], v[232:235], v[252:255], v[172:175]\n"
    "v_pk_fma_f32 v[128:129], v[192:193], v[120:121], v[128:129] op_sel_hi:[1,0,1]\n"
    "v_pk_fma_f32 v[130:131], v[194:195], v[120:121], v[130:131] op_sel_hi:[1,0,1]\n"
    "v_pk_fma_f32 v[144:145], v[196:197], v[120:121], v[144:145] op_sel_hi:[1,0,1]\n"
    "v_pk_fma_f32 v[146:147], v[198:199], v[120:121], v[146:147] op_sel_hi:[1,0,1]\n"
    "v_pk_fma_f32 v[160:161], v[200:201], v[120:121], v[160:161] op_sel_hi:[1,0,1]\n"
    "v_pk_fma_f32 v[162:163], v[202:203], v[120:121], v[162:163] op_sel_hi:[1,0,1]\n"
    "v_pk_mul_f32 v[128:129], v[128:129], v[50:51] op_sel_hi:[1,0]\n"
    "v_pk_mul_f32 v[130:131], v[130:131], v[50:51] op_sel_hi:[1,0]\n"
    "v_pk_mul_f32 v[144:145], v[144:145], v[50:51] op_sel_hi:[1,0]\n"
    "v_pk_mul_f32 v[146:147], v[146:147], v[50:51] op_sel_hi:[1,0]\n"
    "v_pk_mul_f32 v[160:161], v[160:161], v[50:51] op_sel_hi:[1,0]\n"
    "v_pk_mul_f32 v[162:163], v[162:163], v[50:51] op_sel_hi:[1,0]\n"
    "v_cvt_pk_bf16_f32 v208, v128, v129\n"
    "v_cvt_pk_bf16_f32 v209, v130, v131\n"
    "v_cvt_pk_bf16_f32 v210, v144, v145\n"
    "v_cvt_pk_bf16_f32 v211, v146, v147\n"
    "v_cvt_pk_bf16_f32 v212, v160, v161\n"
    "v_cvt_pk_bf16_f32 v213, v162, v163\n"
    "ds_write_b64 v57, v[208:209] offset:0\n"
    "ds_write_b64 v57, v[210:211] offset:6144\n"
    "ds_write_b64 v58, v[212:213] offset:32\n"
    "v_pk_fma_f32 v[132:133], v[192:193], v[122:123], v[132:133] op_sel_hi:[1,0,1]\n"
    "v_pk_fma_f32 v[134:135], v[194:195], v[122:123], v[134:135] op_sel_hi:[1,0,1]\n"
    "v_pk_fma_f32 v[148:149], v[196:197], v[122:123], v[148:149] op_sel_hi:[1,0,1]\n"
    "v_pk_fma_f32 v[150:151], v[198:199], v[122:123], v[150:151] op_sel_hi:[1,0,1]\n"
    "v_pk_fma_f32 v[164:165], v[200:201], v[122:123], v[164:165] op_sel_hi:[1,0,1]\n"
    "v_pk_fma_f32 v[166:167], v[202:203], v[122:123], v[166:167] op_sel_hi:[1,0,1]\n"
    "v_pk_mul_f32 v[132:133], v[132:133], v[52:53] op_sel_hi:[1,0]\n"
    "v_pk_mul_f32 v[134:135], v[134:135], v[52:53] op_sel_hi:[1,0]\n"
    "v_pk_mul_f32 v[148:149], v[148:149], v[52:53] op_sel_hi:[1,0]\n"
    "v_pk_mul_f32 v[150:151], v[150:151], v[52:53] op_sel_hi:[1,0]\n"
    "v_pk_mul_f32 v[164:165], v[164:165], v[52:53] op_sel_hi:[1,0]\n"
    "v_pk_mul_f32 v[166:167], v[166:167], v[52:53] op_sel_hi:[1,0]\n"
    "v_cvt_pk_bf16_f32 v216, v132, v133\n"
    "v_cvt_pk_bf16_f32 v217, v134, v135\n"
    "v_cvt_pk_bf16_f32 v218, v148, v149\n"
    "v_cvt_pk_bf16_f32 v219, v150, v151\n"
    "v_cvt_pk_bf16_f32 v220, v164, v165\n"
    "v_cvt_pk_bf16_f32 v221, v166, v167\n"
    "ds_write_b64 v57, v[216:217] offset:768\n"
    "ds_write_b64 v57, v[218:219] offset:6912\n"
    "ds_write_b64 v58, v[220:221] offset:800\n"
    "v_pk_fma_f32 v[136:137], v[192:193], v[124:125], v[136:137] op_sel_hi:[1,0,1]\n"
    "v_pk_fma_f32 v[138:139], v[194:195], v[124:125], v[138:139] op_sel_hi:[1,0,1]\n"
    "v_pk_fma_f32 v[152:153], v[196:197], v[124:125], v[152:153] op_sel_hi:[1,0,1]\n"
    "v_pk_fma_f32 v[154:155], v[198:199], v[124:125], v[154:155] op_sel_hi:[1,0,1]\n"
    "v_pk_fma_f32 v[168:169], v[200:201], v[124:125], v[168:169] op_sel_hi:[1,0,1]\n"
    "v_pk_fma_f32 v[170:171], v[202:203], v[124:125], v[170:171] op_sel_hi:[1,0,1]\n"
    "v_pk_mul_f32 v[136:137], v[136:137], v[54:55] op_sel_hi:[1,0]\n"
    "v_pk_mul_f32 v[138:139], v[138:139], v[54:55] op_sel_hi:[1,0]\n"
    "v_pk_mul_f32 v[152:153], v[152:153], v[54:55] op_sel_hi:[1,0]\n"
    "v_pk_mul_f32 v[154:155], v[154:155], v[54:55] op_sel_hi:[1,0]\n"
    "v_pk_mul_f32 v[168:169], v[168:169], v[54:55] op_sel_hi:[1,0]\n"
    "v_pk_mul_f32 v[170:171], v[170:171], v[54:55] op_sel_hi:[1,0]\n"
    "v_cvt_pk_bf16_f32 v208, v136, v137\n"
    "v_cvt_pk_bf16_f32 v209, v138, v139\n"
    "v_cvt_pk_bf16_f32 v210, v152, v153\n"
    "v_cvt_pk_bf16_f32 v211, v154, v155\n"
    "v_cvt_pk_bf16_f32 v212, v168, v169\n"
    "v_cvt_pk_bf16_f32 v213, v170, v171\n"
    "ds_write_b64 v57, v[208:209] offset:1536\n"
    "ds_write_b64 v57, v[210:211] offset:7680\n"
    "ds_write_b64 v58, v[212:213] offset:1568\n"
    "v_pk_fma_f32 v[140:141], v[192:193], v[126:127], v[140:141] op_sel_hi:[1,0,1]\n"
    "v_pk_fma_f32 v[142:143], v[194:195], v[126:127], v[142:143] op_sel_hi:[1,0,1]\n"
    "v_pk_fma_f32 v[156:157], v[196:197], v[126:127], v[156:157] op_sel_hi:[1,0,1]\n"
    "v_pk_fma_f32 v[158:159], v[198:199], v[126:127], v[158:159] op_sel_hi:[1,0,1]\n"
    "v_pk_fma_f32 v[172:173], v[200:201], v[126:127], v[172:173] op_sel_hi:[1,0,1]\n"
    "v_pk_fma_f32 v[174:175], v[202:203], v[126:127], v[174:175] op_sel_hi:[1,0,1]\n"
    "v_pk_mul_f32 v[140:141], v[140:141], v[70:71] op_sel_hi:[1,0]\n"
    "v_pk_mul_f32 v[142:143], v[142:143], v[70:71] op_sel_hi:[1,0]\n"
    "v_pk_mul_f32 v[156:157], v[156:157], v[70:71] op_sel_hi:[1,0]\n"
    "v_pk_mul_f32 v[158:159], v[158:159], v[70:71] op_sel_hi:[1,0]\n"
    "v_pk_mul_f32 v[172:173], v[172:173], v[70:71] op_sel_hi:[1,0]\n"
    "v_pk_mul_f32 v[174:175], v[174:175], v[70:71] op_sel_hi:[1,0]\n"
    "v_cvt_pk_bf16_f32 v216, v140, v141\n"
    "v_cvt_pk_bf16_f32 v217, v142, v143\n"
    "v_cvt_pk_bf16_f32 v218, v156, v157\n"
    "v_cvt_pk_bf16_f32 v219, v158, v159\n"
    "v_cvt_pk_bf16_f32 v220, v172, v173\n"
    "v_cvt_pk_bf16_f32 v221, v174, v175\n"
    "ds_write_b64 v57, v[216:217] offset:2304\n"
    "ds_write_b64 v57, v[218:219] offset:8448\n"
    "ds_write_b64 v58, v[220:221] offset:2336\n"
    "s_waitcnt vmcnt(5) lgkmcnt(0)\n"
    "s_barrier\n"
    "ds_read_b128 v[28:31], v118\n"
    "ds_read_b32 v32, v25 offset:32\n"
    "ds_read_b128 v[36:39], v119\n"
    "ds_read_b32 v34, v26 offset:32\n"
    "ds_read_b32 v108, v12 offset:49280\n"
    "ds_read_b128 v[104:107], v11 offset:49280\n"
    "ds_read_b128 v[112:115], v13 offset:49280\n"
    "ATT_%=:\n"
    "v_mov_b32 v109, 0x3f80\n"
    "v_mov_b32 v110, 0xdddd\n"
    "v_mov_b64 v[56:57], 0\n"
    "s_lshl_b32 s38, s64, 2\n"
    "s_add_u32 s38, s38, 98432\n"
    "v_mov_b32 v61, s38\n"
    "v_mov_b32 v60, 0x3c23d70a\n"
    "s_waitcnt lgkmcnt(0)\n"
    "v_dot2c_f32_bf16 v56, v28, v28\n"
    "v_dot2c_f32_bf16 v57, v36, v36\n"
    "v_dot2c_f32_bf16 v56, v29, v29\n"
    "v_dot2c_f32_bf16 v57, v37, v37\n"
    "v_dot2c_f32_bf16 v56, v30, v30\n"
    "v_dot2c_f32_bf16 v57, v38, v38\n"
    "v_dot2c_f32_bf16 v56, v31, v31\n"
    "v_dot2c_f32_bf16 v57, v39, v39\n"
    "v_dot2c_f32_bf16 v56, v32, v32\n"
    "v_dot2c_f32_bf16 v57, v34, v34\n"
    "s_nop 2\n"
    "v_permlane32_swap_b32 v56, v57\n"
    "v_add_f32 v56, v56, v57\n"
    "v_mul_f32 v56, s63, v56\n"
    "v_sqrt_f32 v56, v56\n"
    "v_mov_b32 v35, 0\n"
    "v_fmamk_f32 v56, v56, 0x3f8147ae, v60\n"
    "v_cmp_nge_f32 vcc, 0x42480000, v56\n"
    "v_cvt_pk_bf16_f32 v33, -v56, 0\n"
    "s_or_b32 s38, vcc_lo, vcc_hi\n"
    "v_mov_b32 v60, s38\n"
    "v_permlane32_swap_b32 v33, v35\n"
    "ds_write_b32 v61, v60\n"
    "s_nop 0\n"
    "v_mfma_f32_32x32x8_bf16 v[40:55], v[108:109], v[32:33], 0\n"
    "v_mfma_f32_32x32x16_bf16 v[40:55], v[104:107], v[28:31], v[40:55]\n"
    "v_mov_b32 v196, v15\n"
    "v_add_u32 v197, 61440, v15\n"
    "v_add_u32 v198, 122880, v15\n"
    "s_mul_i32 s38, s64, 3584\n"
    "s_add_u32 s38, s38, 61440\n"
    "v_add_u32 v111, s38, v21\n"
    "s_xor_b32 s38, s64, 1\n"
    "s_mul_i32 s38, s38, 3584\n"
    "s_add_u32 s38, s38, 61440\n"
    "v_add_u32 v199, s38, v21\n"
    "s_nop 2\n"
    "v_exp_f32 v40, v40\n"
    "v_exp_f32 v41, v41\n"
    "s_setprio 2\n"
    "ds_read_b128 v[116:119], v13 offset:1536\n"
    "v_exp_f32 v42, v42\n"
    "v_exp_f32 v43, v43\n"
    "v_exp_f32 v44, v44\n"
    "v_exp_f32 v45, v45\n"
    "v_mfma_f32_32x32x8_bf16 v[56:71], v[108:109], v[34:35], 0\n"
    "ds_read_b32 v108, v12 offset:0\n"
    "v_exp_f32 v46, v46\n"
    "v_exp_f32 v47, v47\n"
    "v_exp_f32 v48, v48\n"
    "v_exp_f32 v49, v49\n"
    "v_mfma_f32_32x32x16_bf16 v[56:71], v[104:107], v[36:39], v[56:71]\n"
    "ds_read_b128 v[104:107], v11 offset:0\n"
    "v_exp_f32 v50, v50\n"
    "v_exp_f32 v51, v51\n"
    "v_exp_f32 v52, v52\n"
    "v_exp_f32 v53, v53\n"
    "v_smfmac_f32_32x32x32_bf16 v[72:87], v[112:115], v[40:47], v110\n"
    "s_waitcnt lgkmcnt(2)\n"
    "v_exp_f32 v54, v54\n"
    "v_exp_f32 v55, v55\n"
    "v_exp_f32 v56, v56\n"
    "v_exp_f32 v57, v57\n"
    "v_smfmac_f32_32x32x32_bf16 v[72:87], v[116:119], v[48:55], v110\n"
    "s_waitcnt lgkmcnt(0)\n"
    "v_exp_f32 v58, v58\n"
    "v_exp_f32 v59, v59\n"
    "v_exp_f32 v60, v60\n"
    "v_exp_f32 v61, v61\n"
    "v_mfma_f32_32x32x8_bf16 v[40:55], v[108:109], v[32:33], 0\n"
    "v_exp_f32 v62, v62\n"
    "v_exp_f32 v63, v63\n"
    "v_exp_f32 v64, v64\n"
    "v_exp_f32 v65, v65\n"
    "v_mfma_f32_32x32x16_bf16 v[40:55], v[104:107], v[28:31], v[40:55]\n"
    "v_exp_f32 v66, v66\n"
    "v_exp_f32 v67, v67\n"
    "v_exp_f32 v68, v68\n"
    "v_exp_f32 v69, v69\n"
    "v_smfmac_f32_32x32x32_bf16 v[88:103], v[112:115], v[56:63], v110\n"
    "ds_read_b128 v[112:115], v13 offset:0\n"
    "v_exp_f32 v70, v70\n"
    "v_exp_f32 v71, v71\n"
    "v_exp_f32 v40, v40\n"
    "v_exp_f32 v41, v41\n"
    "v_smfmac_f32_32x32x32_bf16 v[88:103], v[116:119], v[64:71], v110\n"
    "ds_read_b128 v[116:119], v13 offset:7680\n"
    "s_add_u32 m0, s43, 0\n"
    "s_add_u32 s38, s45, 0\n"
    "s_bitcmp1_b32 s64, 0\n"
    "s_cbranch_scc1 WS12_%=\n"
    "buffer_load_dwordx4 v5, s[20:23], s38 offen lds\n"
    "WS12_%=:\n"
    "buffer_load_dwordx4 v6, s[20:23], s38 offen offset:1024 lds\n"
    "buffer_load_dwordx4 v7, s[20:23], s38 offen offset:2048 lds\n"
    "s_waitcnt lgkmcnt(1)\n"
    "v_exp_f32 v42, v42\n"
    "v_exp_f32 v43, v43\n"
    "v_exp_f32 v44, v44\n"
    "v_exp_f32 v45, v45\n"
    "v_mfma_f32_32x32x8_bf16 v[56:71], v[108:109], v[34:35], 0\n"
    "ds_read_b32 v108, v12 offset:6144\n"
    "v_exp_f32 v46, v46\n"
    "v_exp_f32 v47, v47\n"
    "v_exp_f32 v48, v48\n"
    "v_exp_f32 v49, v49\n"
    "v_mfma_f32_32x32x16_bf16 v[56:71], v[104:107], v[36:39], v[56:71]\n"
    "ds_read_b128 v[104:107], v11 offset:6144\n"
    "v_exp_f32 v50, v50\n"
    "v_exp_f32 v51, v51\n"
    "v_exp_f32 v52, v52\n"
    "v_exp_f32 v53, v53\n"
    "v_smfmac_f32_32x32x32_bf16 v[72:87], v[112:115], v[40:47], v110\n"
    "s_waitcnt lgkmcnt(2)\n"
    "v_exp_f32 v54, v54\n"
    "v_exp_f32 v55, v55\n"
    "v_exp_f32 v56, v56\n"
    "v_exp_f32 v57, v57\n"
    "v_smfmac_f32_32x32x32_bf16 v[72:87], v[116:119], v[48:55], v110\n"
    "s_waitcnt lgkmcnt(0)\n"
    "v_exp_f32 v58, v58\n"
    "v_exp_f32 v59, v59\n"
    "v_exp_f32 v60, v60\n"
    "v_exp_f32 v61, v61\n"
    "v_mfma_f32_32x32x8_bf16 v[40:55], v[108:109], v[32:33], 0\n"
    "v_exp_f32 v62, v62\n"
    "v_exp_f32 v63, v63\n"
    "v_exp_f32 v64, v64\n"
    "v_exp_f32 v65, v65\n"
    "v_mfma_f32_32x32x16_bf16 v[40:55], v[104:107], v[28:31], v[40:55]\n"
    "v_exp_f32 v66, v66\n"
    "v_exp_f32 v67, v67\n"
    "v_exp_f32 v68, v68\n"
    "v_exp_f32 v69, v69\n"
    "v_smfmac_f32_32x32x32_bf16 v[88:103], v[112:115], v[56:63], v110\n"
    "ds_read_b128 v[112:115], v13 offset:6144\n"
    "v_exp_f32 v70, v70\n"
    "v_exp_f32 v71, v71\n"
    "v_exp_f32 v40, v40\n"
    "v_exp_f32 v41, v41\n"
    "v_smfmac_f32_32x32x32_bf16 v[88:103], v[116:119], v[64:71], v110\n"
    "ds_read_b128 v[116:119], v13 offset:13824\n"
    "s_add_u32 m0, s43, 20480\n"
    "s_add_u32 s38, s45, 51200\n"
    "s_bitcmp1_b32 s64, 0\n"
    "s_cbranch_scc0 WS13_%=\n"
    "buffer_load_dwordx4 v7, s[20:23], s38 offen offset:2048 lds\n"
    "WS13_%=:\n"
    "buffer_load_dwordx4 v5, s[20:23], s38 offen lds\n"
    "buffer_load_dwordx4 v6, s[20:23], s38 offen offset:1024 lds\n"
    "s_waitcnt lgkmcnt(1)\n"
    "v_exp_f32 v42, v42\n"
    "v_exp_f32 v43, v43\n"
    "v_exp_f32 v44, v44\n"
    "v_exp_f32 v45, v45\n"
    "v_mfma_f32_32x32x8_bf16 v[56:71], v[108:109], v[34:35], 0\n"
    "ds_read_b32 v108, v12 offset:12288\n"
    "v_exp_f32 v46, v46\n"
    "v_exp_f32 v47, v47\n"
    "v_exp_f32 v48, v48\n"
    "v_exp_f32 v49, v49\n"
    "v_mfma_f32_32x32x16_bf16 v[56:71], v[104:107], v[36:39], v[56:71]\n"
    "ds_read_b128 v[104:107], v11 offset:12288\n"
    "v_exp_f32 v50, v50\n"
    "v_exp_f32 v51, v51\n"
    "v_exp_f32 v52, v52\n"
    "v_exp_f32 v53, v53\n"
    "v_smfmac_f32_32x32x32_bf16 v[72:87], v[112:115], v[40:47], v110\n"
    "s_waitcnt lgkmcnt(2)\n"
    "v_exp_f32 v54, v54\n"
    "v_exp_f32 v55, v55\n"
    "v_exp_f32 v56, v56\n"
    "v_exp_f32 v57, v57\n"
    "v_smfmac_f32_32x32x32_bf16 v[72:87], v[116:119], v[48:55], v110\n"
    "s_setprio 1\n"
    "s_waitcnt lgkmcnt(0)\n"
    "v_exp_f32 v58, v58\n"
    "v_exp_f32 v59, v59\n"
    "v_exp_f32 v60, v60\n"
    "v_exp_f32 v61, v61\n"
    "v_mfma_f32_32x32x8_bf16 v[40:55], v[108:109], v[32:33], 0\n"
    "v_exp_f32 v62, v62\n"
    "v_exp_f32 v63, v63\n"
    "v_exp_f32 v64, v64\n"
    "v_exp_f32 v65, v65\n"
    "v_mfma_f32_32x32x16_bf16 v[40:55], v[104:107], v[28:31], v[40:55]\n"
    "v_exp_f32 v66, v66\n"
    "v_exp_f32 v67, v67\n"
    "v_exp_f32 v68, v68\n"
    "v_exp_f32 v69, v69\n"
    "v_smfmac_f32_32x32x32_bf16 v[88:103], v[112:115], v[56:63], v110\n"
    "ds_read_b128 v[112:115], v13 offset:12288\n"
    "v_exp_f32 v70, v70\n"
    "v_exp_f32 v71, v71\n"
    "v_exp_f32 v40, v40\n"
    "v_exp_f32 v41, v41\n"
    "v_smfmac_f32_32x32x32_bf16 v[88:103], v[116:119], v[64:71], v110\n"
    "ds_read_b128 v[116:119], v13 offset:19968\n"
    "s_add_u32 m0, s43, 122880\n"
    "s_add_u32 s38, s44, 0\n"
    "s_bitcmp1_b32 s64, 0\n"
    "s_cbranch_scc1 WS0_%=\n"
    "buffer_load_dwordx4 v2, s[16:19], s38 offen lds\n"
    "WS0_%=:\n"
    "buffer_load_dwordx4 v3, s[16:19], s38 offen offset:1024 lds\n"
    "buffer_load_dwordx4 v4, s[16:19], s38 offen offset:2048 lds\n"
    "s_waitcnt lgkmcnt(1)\n"
    "v_exp_f32 v42, v42\n"
    "v_exp_f32 v43, v43\n"
    "v_exp_f32 v44, v44\n"
    "v_exp_f32 v45, v45\n"
    "v_mfma_f32_32x32x8_bf16 v[56:71], v[108:109], v[34:35], 0\n"
    "ds_read_b32 v108, v12 offset:18432\n"
    "v_exp_f32 v46, v46\n"
    "v_exp_f32 v47, v47\n"
    "v_exp_f32 v48, v48\n"
    "v_exp_f32 v49, v49\n"
    "v_mfma_f32_32x32x16_bf16 v[56:71], v[104:107], v[36:39], v[56:71]\n"
    "ds_read_b128 v[104:107], v11 offset:18432\n"
    "v_exp_f32 v50, v50\n"
    "v_exp_f32 v51, v51\n"
    "v_exp_f32 v52, v52\n"
    "v_exp_f32 v53, v53\n"
    "v_smfmac_f32_32x32x32_bf16 v[72:87], v[112:115], v[40:47], v110\n"
    "s_waitcnt lgkmcnt(2)\n"
    "v_exp_f32 v54, v54\n"
    "v_exp_f32 v55, v55\n"
    "v_exp_f32 v56, v56\n"
    "v_exp_f32 v57, v57\n"
    "v_smfmac_f32_32x32x32_bf16 v[72:87], v[116:119], v[48:55], v110\n"
    "s_setprio 0\n"
    "s_waitcnt lgkmcnt(0)\n"
    "v_exp_f32 v58, v58\n"
    "v_exp_f32 v59, v59\n"
    "v_exp_f32 v60, v60\n"
    "v_exp_f32 v61, v61\n"
    "v_mfma_f32_32x32x8_bf16 v[40:55], v[108:109], v[32:33], 0\n"
    "v_exp_f32 v62, v62\n"
    "v_exp_f32 v63, v63\n"
    "v_exp_f32 v64, v64\n"
    "v_exp_f32 v65, v65\n"
    "v_mfma_f32_32x32x16_bf16 v[40:55], v[104:107], v[28:31], v[40:55]\n"
    "s_setprio 0\n"
    "v_exp_f32 v66, v66\n"
    "v_exp_f32 v67, v67\n"
    "v_exp_f32 v68, v68\n"
    "v_exp_f32 v69, v69\n"
    "v_smfmac_f32_32x32x32_bf16 v[88:103], v[112:115], v[56:63], v110\n"
    "ds_read_b128 v[112:115], v13 offset:18432\n"
    "v_exp_f32 v70, v70\n"
    "v_exp_f32 v71, v71\n"
    "v_exp_f32 v40, v40\n"
    "v_exp_f32 v41, v41\n"
    "v_smfmac_f32_32x32x32_bf16 v[88:103], v[116:119], v[64:71], v110\n"
    "s_waitcnt vmcnt(6) lgkmcnt(0)\n"
    "s_barrier\n"
    "s_setprio 3\n"
    "ds_read_b128 v[116:119], v13 offset:26112\n"
    "s_cmp_lg_u32 s64, 0\n"
    "s_cbranch_scc1 HF_%=\n"
    "v_mov_b32 v200, s37\n"
    "v_mov_b32 v201, s37\n"
    "s_mov_b64 exec, 1\n"
    "buffer_store_dwordx2 v[200:201], off, s[8:11], s59 sc1\n"
    "s_mov_b64 exec, -1\n"
    "HF_%=:\n"
    "v_mov_b32 v202, 98432\n"
    "ds_read_b128 v[208:211], v202\n"
    "ds_read_b128 v[204:207], v202 offset:16\n"
    "v_exp_f32 v42, v42\n"
    "v_exp_f32 v43, v43\n"
    "v_exp_f32 v44, v44\n"
    "v_exp_f32 v45, v45\n"
    "v_mfma_f32_32x32x8_bf16 v[56:71], v[108:109], v[34:35], 0\n"
    "ds_read_b32 v108, v12 offset:24576\n"
    "v_exp_f32 v46, v46\n"
    "v_exp_f32 v47, v47\n"
    "v_exp_f32 v48, v48\n"
    "v_exp_f32 v49, v49\n"
    "v_mfma_f32_32x32x16_bf16 v[56:71], v[104:107], v[36:39], v[56:71]\n"
    "ds_read_b128 v[104:107], v11 offset:24576\n"
    "v_exp_f32 v50, v50\n"
    "v_exp_f32 v51, v51\n"
    "v_exp_f32 v52, v52\n"
    "v_exp_f32 v53, v53\n"
    "v_smfmac_f32_32x32x32_bf16 v[72:87], v[112:115], v[40:47], v110\n"
    "s_waitcnt lgkmcnt(4)\n"
    "v_exp_f32 v54, v54\n"
    "v_exp_f32 v55, v55\n"
    "v_exp_f32 v56, v56\n"
    "v_exp_f32 v57, v57\n"
    "v_smfmac_f32_32x32x32_bf16 v[72:87], v[116:119], v[48:55], v110\n"
    "s_setprio 2\n"
    "s_add_u32 m0, s61, 0\n"
    "s_add_u32 s38, s62, 49152\n"
    "buffer_load_dwordx4 v21, s[8:11], s38 offen sc1 lds\n"
    "buffer_load_dwordx4 v21, s[8:11], s38 offen offset:1024 sc1 lds\n"
    "buffer_load_dwordx4 v21, s[8:11], s38 offen offset:2048 sc1 lds\n"
    "s_waitcnt lgkmcnt(0)\n"
    "v_exp_f32 v58, v58\n"
    "v_exp_f32 v59, v59\n"
    "v_exp_f32 v60, v60\n"
    "v_exp_f32 v61, v61\n"
    "v_mfma_f32_32x32x8_bf16 v[40:55], v[108:109], v[32:33], 0\n"
    "v_or3_b32 v208, v208, v209, v210\n"
    "v_or3_b32 v204, v204, v205, v206\n"
    "v_or3_b32 v208, v208, v211, v204\n"
    "v_or_b32 v208, v208, v207\n"
    "v_cmp_ne_u32 vcc, 0, v208\n"
    "s_nop 1\n"
    "s_cbranch_vccnz SLOW_%=\n"
    "v_exp_f32 v62, v62\n"
    "v_exp_f32 v63, v63\n"
    "v_exp_f32 v64, v64\n"
    "v_exp_f32 v65, v65\n"
    "v_mfma_f32_32x32x16_bf16 v[40:55], v[104:107], v[28:31], v[40:55]\n"
    "v_exp_f32 v66, v66\n"
    "v_exp_f32 v67, v67\n"
    "v_exp_f32 v68, v68\n"
    "v_exp_f32 v69, v69\n"
    "v_smfmac_f32_32x32x32_bf16 v[88:103], v[112:115], v[56:63], v110\n"
    "ds_read_b128 v[112:115], v13 offset:24576\n"
    "v_exp_f32 v70, v70\n"
    "v_exp_f32 v71, v71\n"
    "v_exp_f32 v40, v40\n"
    "v_exp_f32 v41, v41\n"
    "v_smfmac_f32_32x32x32_bf16 v[88:103], v[116:119], v[64:71], v110\n"
    "ds_read_b128 v[116:119], v13 offset:32256\n"
    "s_add_u32 m0, s43, 102400\n"
    "s_add_u32 s38, s44, 40960\n"
    "s_bitcmp1_b32 s64, 0\n"
    "s_cbranch_scc0 WS1_%=\n"
    "buffer_load_dwordx4 v4, s[16:19], s38 offen offset:2048 lds\n"
    "WS1_%=:\n"
    "buffer_load_dwordx4 v2, s[16:19], s38 offen lds\n"
    "buffer_load_dwordx4 v3, s[16:19], s38 offen offset:1024 lds\n"
    "s_waitcnt lgkmcnt(1)\n"
    "v_exp_f32 v42, v42\n"
    "v_exp_f32 v43, v43\n"
    "v_exp_f32 v44, v44\n"
    "v_exp_f32 v45, v45\n"
    "v_mfma_f32_32x32x8_bf16 v[56:71], v[108:109], v[34:35], 0\n"
    "ds_read_b32 v108, v12 offset:30720\n"
    "v_exp_f32 v46, v46\n"
    "v_exp_f32 v47, v47\n"
    "v_exp_f32 v48, v48\n"
    "v_exp_f32 v49, v49\n"
    "v_mfma_f32_32x32x16_bf16 v[56:71], v[104:107], v[36:39], v[56:71]\n"
    "ds_read_b128 v[104:107], v11 offset:30720\n"
    "v_exp_f32 v50, v50\n"
    "v_exp_f32 v51, v51\n"
    "v_exp_f32 v52, v52\n"
    "v_exp_f32 v53, v53\n"
    "v_smfmac_f32_32x32x32_bf16 v[72:87], v[112:115], v[40:47], v110\n"
    "s_waitcnt lgkmcnt(2)\n"
    "v_exp_f32 v54, v54\n"
    "v_exp_f32 v55, v55\n"
    "v_exp_f32 v56, v56\n"
    "v_exp_f32 v57, v57\n"
    "v_smfmac_f32_32x32x32_bf16 v[72:87], v[116:119], v[48:55], v110\n"
    "s_setprio 1\n"
    "s_waitcnt lgkmcnt(0)\n"
    "v_exp_f32 v58, v58\n"
    "v_exp_f32 v59, v59\n"
    "v_exp_f32 v60, v60\n"
    "v_exp_f32 v61, v61\n"
    "v_mfma_f32_32x32x8_bf16 v[40:55], v[108:109], v[32:33], 0\n"
    "v_exp_f32 v62, v62\n"
    "v_exp_f32 v63, v63\n"
    "v_exp_f32 v64, v64\n"
    "v_exp_f32 v65, v65\n"
    "v_mfma_f32_32x32x16_bf16 v[40:55], v[104:107], v[28:31], v[40:55]\n"
    "s_setprio 0\n"
    "v_exp_f32 v66, v66\n"
    "v_exp_f32 v67, v67\n"
    "v_exp_f32 v68, v68\n"
    "v_exp_f32 v69, v69\n"
    "v_smfmac_f32_32x32x32_bf16 v[88:103], v[112:115], v[56:63], v110\n"
    "ds_read_b128 v[112:115], v13 offset:30720\n"
    "v_exp_f32 v70, v70\n"
    "v_exp_f32 v71, v71\n"
    "v_exp_f32 v40, v40\n"
    "v_exp_f32 v41, v41\n"
    "v_smfmac_f32_32x32x32_bf16 v[88:103], v[116:119], v[64:71], v110\n"
    "s_waitcnt vmcnt(7) lgkmcnt(0)\n"
    "s_barrier\n"
    "s_setprio 3\n"
    "ds_read_b128 v[116:119], v13 offset:38400\n"
    "v_exp_f32 v42, v42\n"
    "v_exp_f32 v43, v43\n"
    "v_exp_f32 v44, v44\n"
    "ds_read_b64_tr_b16 v[216:217], v196 offset:0\n"
    "ds_read_b64_tr_b16 v[218:219], v196 offset:2560\n"
    "v_exp_f32 v45, v45\n"
    "v_mfma_f32_32x32x8_bf16 v[56:71], v[108:109], v[34:35], 0\n"
    "ds_read_b32 v108, v12 offset:36864\n"
    "v_exp_f32 v46, v46\n"
    "v_exp_f32 v47, v47\n"
    "v_exp_f32 v48, v48\n"
    "v_exp_f32 v49, v49\n"
    "v_mfma_f32_32x32x16_bf16 v[56:71], v[104:107], v[36:39], v[56:71]\n"
    "ds_read_b128 v[104:107], v11 offset:36864\n"
    "ds_read_b64_tr_b16 v[220:221], v196 offset:32\n"
    "ds_read_b64_tr_b16 v[222:223], v196 offset:2592\n"
    "v_exp_f32 v50, v50\n"
    "v_exp_f32 v51, v51\n"
    "v_exp_f32 v52, v52\n"
    "v_exp_f32 v53, v53\n"
    "v_smfmac_f32_32x32x32_bf16 v[72:87], v[112:115], v[40:47], v110\n"
    "s_waitcnt lgkmcnt(6)\n"
    "ds_read_b64_tr_b16 v[224:225], v196 offset:64\n"
    "ds_read_b64_tr_b16 v[226:227], v196 offset:2624\n"
    "v_exp_f32 v54, v54\n"
    "v_exp_f32 v55, v55\n"
    "v_exp_f32 v56, v56\n"
    "v_exp_f32 v57, v57\n"
    "v_smfmac_f32_32x32x32_bf16 v[72:87], v[116:119], v[48:55], v110\n"
    "s_setprio 2\n"
    "buffer_load_dwordx2 v[200:201], v14, s[8:11], s46 offen sc1\n"
    "s_waitcnt lgkmcnt(4)\n"
    "v_exp_f32 v58, v58\n"
    "v_exp_f32 v59, v59\n"
    "v_exp_f32 v60, v60\n"
    "ds_read_b64_tr_b16 v[228:229], v196 offset:96\n"
    "ds_read_b64_tr_b16 v[230:231], v196 offset:2656\n"
    "v_exp_f32 v61, v61\n"
    "v_mfma_f32_32x32x8_bf16 v[40:55], v[108:109], v[32:33], 0\n"
    "s_add_u32 m0, s43, 143360\n"
    "s_add_u32 s38, s44, 81920\n"
    "s_bitcmp1_b32 s64, 0\n"
    "s_cbranch_scc1 WS2_%=\n"
    "buffer_load_dwordx4 v2, s[16:19], s38 offen lds\n"
    "WS2_%=:\n"
    "buffer_load_dwordx4 v3, s[16:19], s38 offen offset:1024 lds\n"
    "buffer_load_dwordx4 v4, s[16:19], s38 offen offset:2048 lds\n"
    "v_exp_f32 v62, v62\n"
    "v_exp_f32 v63, v63\n"
    "v_exp_f32 v64, v64\n"
    "v_exp_f32 v65, v65\n"
    "v_mfma_f32_32x32x16_bf16 v[40:55], v[104:107], v[28:31], v[40:55]\n"
    "ds_read_b64_tr_b16 v[232:233], v196 offset:128\n"
    "ds_read_b64_tr_b16 v[234:235], v196 offset:2688\n"
    "v_exp_f32 v66, v66\n"
    "v_exp_f32 v67, v67\n"
    "v_exp_f32 v68, v68\n"
    "v_exp_f32 v69, v69\n"
    "v_smfmac_f32_32x32x32_bf16 v[88:103], v[112:115], v[56:63], v110\n"
    "ds_read_b128 v[112:115], v13 offset:36864\n"
    "v_exp_f32 v70, v70\n"
    "v_exp_f32 v71, v71\n"
    "v_exp_f32 v40, v40\n"
    "v_exp_f32 v41, v41\n"
    "v_smfmac_f32_32x32x32_bf16 v[88:103], v[116:119], v[64:71], v110\n"
    "ds_read_b128 v[116:119], v13 offset:44544\n"
    "s_waitcnt lgkmcnt(1)\n"
    "v_exp_f32 v42, v42\n"
    "v_exp_f32 v43, v43\n"
    "v_exp_f32 v44, v44\n"
    "ds_read_b64_tr_b16 v[236:237], v196 offset:20480\n"
    "ds_read_b64_tr_b16 v[238:239], v196 offset:23040\n"
    "v_exp_f32 v45, v45\n"
    "v_mfma_f32_32x32x8_bf16 v[56:71], v[108:109], v[34:35], 0\n"
    "ds_read_b32 v108, v12 offset:43008\n"
    "v_exp_f32 v46, v46\n"
    "v_exp_f32 v47, v47\n"
    "v_exp_f32 v48, v48\n"
    "v_exp_f32 v49, v49\n"
    "v_mfma_f32_32x32x16_bf16 v[56:71], v[104:107], v[36:39], v[56:71]\n"
    "ds_read_b128 v[104:107], v11 offset:43008\n"
    "ds_read_b64_tr_b16 v[240:241], v196 offset:20512\n"
    "ds_read_b64_tr_b16 v[242:243], v196 offset:23072\n"
    "v_exp_f32 v50, v50\n"
    "v_exp_f32 v51, v51\n"
    "v_exp_f32 v52, v52\n"
    "v_exp_f32 v53, v53\n"
    "v_smfmac_f32_32x32x32_bf16 v[72:87], v[112:115], v[40:47], v110\n"
    "s_waitcnt lgkmcnt(6)\n"
    "ds_read_b64_tr_b16 v[244:245], v196 offset:20544\n"
    "ds_read_b64_tr_b16 v[246:247], v196 offset:23104\n"
    "v_exp_f32 v54, v54\n"
    "v_exp_f32 v55, v55\n"
    "v_exp_f32 v56, v56\n"
    "v_exp_f32 v57, v57\n"
    "v_smfmac_f32_32x32x32_bf16 v[72:87], v[116:119], v[48:55], v110\n"
    "s_setprio 1\n"
    "s_waitcnt vmcnt(2)\n"
    "s_mov_b32 s39, 0x10000\n"
    "HC_%=:\n"
    "v_cmp_ne_u32 vcc, s37, v200\n"
    "s_nop 1\n"
    "s_cbranch_vccz HK_%=\n"
    "s_sleep 1\n"
    "buffer_load_dwordx2 v[200:201], v14, s[8:11], s46 offen sc1\n"
    "s_sub_u32 s39, s39, 1\n"
    "s_cmp_lg_u32 s39, 0\n"
    "s_waitcnt vmcnt(0)\n"
    "s_cbranch_scc1 HC_%=\n"
    "HK_%=:\n"
    "s_waitcnt lgkmcnt(4)\n"
    "v_exp_f32 v58, v58\n"
    "v_exp_f32 v59, v59\n"
    "v_exp_f32 v60, v60\n"
    "ds_read_b64_tr_b16 v[248:249], v196 offset:20576\n"
    "ds_read_b64_tr_b16 v[250:251], v196 offset:23136\n"
    "v_exp_f32 v61, v61\n"
    "v_mfma_f32_32x32x8_bf16 v[40:55], v[108:109], v[32:33], 0\n"
    "s_add_u32 s39, s51, 0\n"
    "s_add_u32 s40, s52, 0\n"
    "buffer_load_dwordx4 v[172:175], v21, s[8:11], s39 offen offset:0 sc1\n"
    "buffer_load_dwordx4 v[176:179], v21, s[8:11], s40 offen offset:0 sc1\n"
    "v_exp_f32 v62, v62\n"
    "v_exp_f32 v63, v63\n"
    "v_exp_f32 v64, v64\n"
    "v_exp_f32 v65, v65\n"
    "v_mfma_f32_32x32x16_bf16 v[40:55], v[104:107], v[28:31], v[40:55]\n"
    "s_setprio 0\n"
    "buffer_load_dwordx4 v[180:183], v21, s[8:11], s39 offen offset:1024 sc1\n"
    "buffer_load_dwordx4 v[184:187], v21, s[8:11], s40 offen offset:1024 sc1\n"
    "ds_read_b64_tr_b16 v[252:253], v196 offset:20608\n"
    "ds_read_b64_tr_b16 v[254:255], v196 offset:23168\n"
    "v_exp_f32 v66, v66\n"
    "v_exp_f32 v67, v67\n"
    "v_exp_f32 v68, v68\n"
    "v_exp_f32 v69, v69\n"
    "v_smfmac_f32_32x32x32_bf16 v[88:103], v[112:115], v[56:63], v110\n"
    "ds_read_b128 v[112:115], v13 offset:43008\n"
    "v_exp_f32 v70, v70\n"
    "v_exp_f32 v71, v71\n"
    "v_exp_f32 v40, v40\n"
    "v_exp_f32 v41, v41\n"
    "v_smfmac_f32_32x32x32_bf16 v[88:103], v[116:119], v[64:71], v110\n"
    "s_waitcnt  lgkmcnt(0)\n"
    "s_barrier\n"
    "s_setprio 3\n"
    "ds_read_b128 v[116:119], v13 offset:1536\n"
    "v_exp_f32 v42, v42\n"
    "v_exp_f32 v43, v43\n"
    "v_exp_f32 v44, v44\n"
    "ds_read_b64_tr_b16 v[160:161], v198 offset:0\n"
    "v_exp_f32 v45, v45\n"
    "v_mfma_f32_32x32x8_bf16 v[56:71], v[108:109], v[34:35], 0\n"
    "ds_read_b32 v108, v12 offset:0\n"
    "v_exp_f32 v46, v46\n"
    "v_exp_f32 v47, v47\n"
    "v_exp_f32 v48, v48\n"
    "v_exp_f32 v49, v49\n"
    "v_mfma_f32_32x32x16_bf16 v[56:71], v[104:107], v[36:39], v[56:71]\n"
    "ds_read_b128 v[104:107], v11 offset:0\n"
    "v_exp_f32 v50, v50\n"
    "v_exp_f32 v51, v51\n"
    "v_exp_f32 v52, v52\n"
    "ds_read_b64_tr_b16 v[162:163], v198 offset:2560\n"
    "v_exp_f32 v53, v53\n"
    "v_smfmac_f32_32x32x32_bf16 v[72:87], v[112:115], v[40:47], v110\n"
    "s_waitcnt lgkmcnt(4)\n"
    "v_exp_f32 v54, v54\n"
    "v_exp_f32 v55, v55\n"
    "v_exp_f32 v56, v56\n"
    "v_exp_f32 v57, v57\n"
    "v_smfmac_f32_32x32x32_bf16 v[72:87], v[116:119], v[48:55], v110\n"
    "s_add_u32 m0, s61, 24576\n"
    "s_add_u32 s38, s62, 73728\n"
    "buffer_load_dwordx4 v21, s[8:11], s38 offen sc1 lds\n"
    "buffer_load_dwordx4 v21, s[8:11], s38 offen offset:1024 sc1 lds\n"
    "buffer_load_dwordx4 v21, s[8:11], s38 offen offset:2048 sc1 lds\n"
    "s_waitcnt lgkmcnt(1)\n"
    "v_exp_f32 v58, v58\n"
    "v_exp_f32 v59, v59\n"
    "v_exp_f32 v60, v60\n"
    "ds_read_b64_tr_b16 v[164:165], v198 offset:32\n"
    "v_exp_f32 v61, v61\n"
    "v_mfma_f32_32x32x8_bf16 v[40:55], v[108:109], v[32:33], 0\n"
    "v_exp_f32 v62, v62\n"
    "v_exp_f32 v63, v63\n"
    "v_exp_f32 v64, v64\n"
    "v_exp_f32 v65, v65\n"
    "v_mfma_f32_32x32x16_bf16 v[40:55], v[104:107], v[28:31], v[40:55]\n"
    "v_exp_f32 v66, v66\n"
    "v_exp_f32 v67, v67\n"
    "v_exp_f32 v68, v68\n"
    "ds_read_b64_tr_b16 v[166:167], v198 offset:2592\n"
    "v_exp_f32 v69, v69\n"
    "v_smfmac_f32_32x32x32_bf16 v[88:103], v[112:115], v[56:63], v110\n"
    "ds_read_b128 v[112:115], v13 offset:0\n"
    "v_exp_f32 v70, v70\n"
    "v_exp_f32 v71, v71\n"
    "v_exp_f32 v40, v40\n"
    "v_exp_f32 v41, v41\n"
    "v_smfmac_f32_32x32x32_bf16 v[88:103], v[116:119], v[64:71], v110\n"
    "ds_read_b128 v[116:119], v13 offset:7680\n"
    "s_add_u32 m0, s43, 0\n"
    "s_add_u32 s38, s44, 122880\n"
    "s_bitcmp1_b32 s64, 0\n"
    "s_cbranch_scc0 WS3_%=\n"
    "buffer_load_dwordx4 v4, s[16:19], s38 offen offset:2048 lds\n"
    "WS3_%=:\n"
    "buffer_load_dwordx4 v2, s[16:19], s38 offen lds\n"
    "buffer_load_dwordx4 v3, s[16:19], s38 offen offset:1024 lds\n"
    "s_waitcnt lgkmcnt(1)\n"
    "s_waitcnt vmcnt(7)\n"
    "v_exp_f32 v42, v42\n"
    "v_exp_f32 v43, v43\n"
    "v_exp_f32 v44, v44\n"
    "v_mfma_f32_16x16x32_bf16 v[120:123], v[160:163], v[172:175], 0\n"
    "ds_read_b64_tr_b16 v[168:169], v198 offset:64\n"
    "v_exp_f32 v45, v45\n"
    "v_mfma_f32_32x32x8_bf16 v[56:71], v[108:109], v[34:35], 0\n"
    "ds_read_b32 v108, v12 offset:6144\n"
    "v_exp_f32 v46, v46\n"
    "v_exp_f32 v47, v47\n"
    "v_exp_f32 v48, v48\n"
    "v_exp_f32 v49, v49\n"
    "v_mfma_f32_32x32x16_bf16 v[56:71], v[104:107], v[36:39], v[56:71]\n"
    "ds_read_b128 v[104:107], v11 offset:6144\n"
    "v_exp_f32 v50, v50\n"
    "v_exp_f32 v51, v51\n"
    "v_exp_f32 v52, v52\n"
    "v_mfma_f32_16x16x32_bf16 v[124:127], v[160:163], v[176:179], 0\n"
    "ds_read_b64_tr_b16 v[170:171], v198 offset:2624\n"
    "v_exp_f32 v53, v53\n"
    "v_smfmac_f32_32x32x32_bf16 v[72:87], v[112:115], v[40:47], v110\n"
    "s_waitcnt lgkmcnt(4)\n"
    "v_exp_f32 v54, v54\n"
    "v_exp_f32 v55, v55\n"
    "v_exp_f32 v56, v56\n"
    "v_exp_f32 v57, v57\n"
    "v_smfmac_f32_32x32x32_bf16 v[72:87], v[116:119], v[48:55], v110\n"
    "buffer_load_dwordx4 v[188:191], v21, s[8:11], s39 offen offset:2048 sc1\n"
    "buffer_load_dwordx4 v[192:195], v21, s[8:11], s40 offen offset:2048 sc1\n"
    "s_waitcnt lgkmcnt(1)\n"
    "v_exp_f32 v58, v58\n"
    "v_exp_f32 v59, v59\n"
    "v_exp_f32 v60, v60\n"
    "v_mfma_f32_16x16x32_bf16 v[128:131], v[164:167], v[172:175], 0\n"
    "ds_read_b64_tr_b16 v[160:161], v198 offset:96\n"
    "v_exp_f32 v61, v61\n"
    "v_mfma_f32_32x32x8_bf16 v[40:55], v[108:109], v[32:33], 0\n"
    "v_exp_f32 v62, v62\n"
    "v_exp_f32 v63, v63\n"
    "v_exp_f32 v64, v64\n"
    "v_exp_f32 v65, v65\n"
    "v_mfma_f32_32x32x16_bf16 v[40:55], v[104:107], v[28:31], v[40:55]\n"
    "v_exp_f32 v66, v66\n"
    "v_exp_f32 v67, v67\n"
    "v_exp_f32 v68, v68\n"
    "v_mfma_f32_16x16x32_bf16 v[132:135], v[164:167], v[176:179], 0\n"
    "ds_read_b64_tr_b16 v[162:163], v198 offset:2656\n"
    "v_exp_f32 v69, v69\n"
    "v_smfmac_f32_32x32x32_bf16 v[88:103], v[112:115], v[56:63], v110\n"
    "ds_read_b128 v[112:115], v13 offset:6144\n"
    "v_exp_f32 v70, v70\n"
    "v_exp_f32 v71, v71\n"
    "v_exp_f32 v40, v40\n"
    "v_exp_f32 v41, v41\n"
    "v_smfmac_f32_32x32x32_bf16 v[88:103], v[116:119], v[64:71], v110\n"
    "ds_read_b128 v[116:119], v13 offset:13824\n"
    "s_add_u32 m0, s43, 20480\n"
    "s_add_u32 s38, s44, 163840\n"
    "s_bitcmp1_b32 s64, 0\n"
    "s_cbranch_scc1 WS4_%=\n"
    "buffer_load_dwordx4 v2, s[16:19], s38 offen lds\n"
    "WS4_%=:\n"
    "buffer_load_dwordx4 v3, s[16:19], s38 offen offset:1024 lds\n"
    "buffer_load_dwordx4 v4, s[16:19], s38 offen offset:2048 lds\n"
    "s_waitcnt lgkmcnt(1)\n"
    "v_exp_f32 v42, v42\n"
    "v_exp_f32 v43, v43\n"
    "v_exp_f32 v44, v44\n"
    "v_mfma_f32_16x16x32_bf16 v[136:139], v[168:171], v[172:175], 0\n"
    "ds_read_b64_tr_b16 v[164:165], v198 offset:128\n"
    "v_exp_f32 v45, v45\n"
    "v_mfma_f32_32x32x8_bf16 v[56:71], v[108:109], v[34:35], 0\n"
    "ds_read_b32 v108, v12 offset:12288\n"
    "v_exp_f32 v46, v46\n"
    "v_exp_f32 v47, v47\n"
    "v_exp_f32 v48, v48\n"
    "v_exp_f32 v49, v49\n"
    "v_mfma_f32_32x32x16_bf16 v[56:71], v[104:107], v[36:39], v[56:71]\n"
    "ds_read_b128 v[104:107], v11 offset:12288\n"
    "v_exp_f32 v50, v50\n"
    "v_exp_f32 v51, v51\n"
    "v_exp_f32 v52, v52\n"
    "v_mfma_f32_16x16x32_bf16 v[140:143], v[168:171], v[176:179], 0\n"
    "ds_read_b64_tr_b16 v[166:167], v198 offset:2688\n"
    "v_exp_f32 v53, v53\n"
    "v_smfmac_f32_32x32x32_bf16 v[72:87], v[112:115], v[40:47], v110\n"
    "s_waitcnt lgkmcnt(4)\n"
    "v_exp_f32 v54, v54\n"
    "v_exp_f32 v55, v55\n"
    "v_exp_f32 v56, v56\n"
    "v_exp_f32 v57, v57\n"
    "v_smfmac_f32_32x32x32_bf16 v[72:87], v[116:119], v[48:55], v110\n"
    "s_setprio 2\n"
    "s_waitcnt lgkmcnt(1)\n"
    "v_exp_f32 v58, v58\n"
    "v_exp_f32 v59, v59\n"
    "v_exp_f32 v60, v60\n"
    "v_mfma_f32_16x16x32_bf16 v[144:147], v[160:163], v[172:175], 0\n"
    "ds_read_b64_tr_b16 v[168:169], v197 offset:40960\n"
    "v_exp_f32 v61, v61\n"
    "v_mfma_f32_32x32x8_bf16 v[40:55], v[108:109], v[32:33], 0\n"
    "v_exp_f32 v62, v62\n"
    "v_exp_f32 v63, v63\n"
    "v_exp_f32 v64, v64\n"
    "v_exp_f32 v65, v65\n"
    "v_mfma_f32_32x32x16_bf16 v[40:55], v[104:107], v[28:31], v[40:55]\n"
    "v_exp_f32 v66, v66\n"
    "v_exp_f32 v67, v67\n"
    "v_exp_f32 v68, v68\n"
    "v_mfma_f32_16x16x32_bf16 v[148:151], v[160:163], v[176:179], 0\n"
    "ds_read_b64_tr_b16 v[170:171], v197 offset:43520\n"
    "v_exp_f32 v69, v69\n"
    "v_smfmac_f32_32x32x32_bf16 v[88:103], v[112:115], v[56:63], v110\n"
    "ds_read_b128 v[112:115], v13 offset:12288\n"
    "v_exp_f32 v70, v70\n"
    "v_exp_f32 v71, v71\n"
    "v_exp_f32 v40, v40\n"
    "v_exp_f32 v41, v41\n"
    "v_smfmac_f32_32x32x32_bf16 v[88:103], v[116:119], v[64:71], v110\n"
    "ds_read_b128 v[116:119], v13 offset:19968\n"
    "s_waitcnt lgkmcnt(1)\n"
    "v_exp_f32 v42, v42\n"
    "v_exp_f32 v43, v43\n"
    "v_exp_f32 v44, v44\n"
    "v_mfma_f32_16x16x32_bf16 v[152:155], v[164:167], v[172:175], 0\n"
    "ds_read_b64_tr_b16 v[160:161], v197 offset:40992\n"
    "v_exp_f32 v45, v45\n"
    "v_mfma_f32_32x32x8_bf16 v[56:71], v[108:109], v[34:35], 0\n"
    "ds_read_b32 v108, v12 offset:18432\n"
    "v_exp_f32 v46, v46\n"
    "v_exp_f32 v47, v47\n"
    "v_exp_f32 v48, v48\n"
    "v_exp_f32 v49, v49\n"
    "v_mfma_f32_32x32x16_bf16 v[56:71], v[104:107], v[36:39], v[56:71]\n"
    "ds_read_b128 v[104:107], v11 offset:18432\n"
    "v_exp_f32 v50, v50\n"
    "v_exp_f32 v51, v51\n"
    "v_exp_f32 v52, v52\n"
    "v_mfma_f32_16x16x32_bf16 v[156:159], v[164:167], v[176:179], 0\n"
    "ds_read_b64_tr_b16 v[162:163], v197 offset:43552\n"
    "v_exp_f32 v53, v53\n"
    "v_smfmac_f32_32x32x32_bf16 v[72:87], v[112:115], v[40:47], v110\n"
    "s_waitcnt lgkmcnt(4)\n"
    "v_exp_f32 v54, v54\n"
    "v_exp_f32 v55, v55\n"
    "v_exp_f32 v56, v56\n"
    "v_exp_f32 v57, v57\n"
    "v_smfmac_f32_32x32x32_bf16 v[72:87], v[116:119], v[48:55], v110\n"
    "s_setprio 1\n"
    "buffer_load_dwordx4 v[172:175], v21, s[8:11], s39 offen offset:3072 sc1\n"
    "buffer_load_dwordx4 v[176:179], v21, s[8:11], s40 offen offset:3072 sc1\n"
    "s_waitcnt lgkmcnt(1)\n"
    "s_waitcnt vmcnt(11)\n"
    "v_exp_f32 v58, v58\n"
    "v_exp_f32 v59, v59\n"
    "v_exp_f32 v60, v60\n"
    "v_mfma_f32_16x16x32_bf16 v[120:123], v[168:171], v[180:183], v[120:123]\n"
    "ds_read_b64_tr_b16 v[164:165], v197 offset:41024\n"
    "v_exp_f32 v61, v61\n"
    "v_mfma_f32_32x32x8_bf16 v[40:55], v[108:109], v[32:33], 0\n"
    "v_exp_f32 v62, v62\n"
    "v_exp_f32 v63, v63\n"
    "v_exp_f32 v64, v64\n"
    "v_exp_f32 v65, v65\n"
    "v_mfma_f32_32x32x16_bf16 v[40:55], v[104:107], v[28:31], v[40:55]\n"
    "s_setprio 0\n"
    "v_exp_f32 v66, v66\n"
    "v_exp_f32 v67, v67\n"
    "v_exp_f32 v68, v68\n"
    "v_mfma_f32_16x16x32_bf16 v[124:127], v[168:171], v[184:187], v[124:127]\n"
    "ds_read_b64_tr_b16 v[166:167], v197 offset:43584\n"
    "v_exp_f32 v69, v69\n"
    "v_smfmac_f32_32x32x32_bf16 v[88:103], v[112:115], v[56:63], v110\n"
    "ds_read_b128 v[112:115], v13 offset:18432\n"
    "v_exp_f32 v70, v70\n"
    "v_exp_f32 v71, v71\n"
    "v_exp_f32 v40, v40\n"
    "v_exp_f32 v41, v41\n"
    "v_smfmac_f32_32x32x32_bf16 v[88:103], v[116:119], v[64:71], v110\n"
    "s_waitcnt vmcnt(6) lgkmcnt(0)\n"
    "s_barrier\n"
    "s_setprio 3\n"
    "ds_read_b128 v[116:119], v13 offset:26112\n"
    "v_exp_f32 v42, v42\n"
    "v_exp_f32 v43, v43\n"
    "v_exp_f32 v44, v44\n"
    "v_mfma_f32_16x16x32_bf16 v[128:131], v[160:163], v[180:183], v[128:131]\n"
    "ds_read_b64_tr_b16 v[168:169], v197 offset:41056\n"
    "v_exp_f32 v45, v45\n"
    "v_mfma_f32_32x32x8_bf16 v[56:71], v[108:109], v[34:35], 0\n"
    "ds_read_b32 v108, v12 offset:24576\n"
    "v_exp_f32 v46, v46\n"
    "v_exp_f32 v47, v47\n"
    "v_exp_f32 v48, v48\n"
    "v_exp_f32 v49, v49\n"
    "v_mfma_f32_32x32x16_bf16 v[56:71], v[104:107], v[36:39], v[56:71]\n"
    "ds_read_b128 v[104:107], v11 offset:24576\n"
    "v_exp_f32 v50, v50\n"
    "v_exp_f32 v51, v51\n"
    "v_exp_f32 v52, v52\n"
    "v_mfma_f32_16x16x32_bf16 v[132:135], v[160:163], v[184:187], v[132:135]\n"
    "ds_read_b64_tr_b16 v[170:171], v197 offset:43616\n"
    "v_exp_f32 v53, v53\n"
    "v_smfmac_f32_32x32x32_bf16 v[72:87], v[112:115], v[40:47], v110\n"
    "s_waitcnt lgkmcnt(4)\n"
    "v_exp_f32 v54, v54\n"
    "v_exp_f32 v55, v55\n"
    "v_exp_f32 v56, v56\n"
    "v_exp_f32 v57, v57\n"
    "v_smfmac_f32_32x32x32_bf16 v[72:87], v[116:119], v[48:55], v110\n"
    "s_add_u32 m0, s61, 0\n"
    "s_add_u32 s38, s62, 98304\n"
    "buffer_load_dwordx4 v21, s[8:11], s38 offen sc1 lds\n"
    "buffer_load_dwordx4 v21, s[8:11], s38 offen offset:1024 sc1 lds\n"
    "buffer_load_dwordx4 v21, s[8:11], s38 offen offset:2048 sc1 lds\n"
    "s_waitcnt lgkmcnt(1)\n"
    "v_exp_f32 v58, v58\n"
    "v_exp_f32 v59, v59\n"
    "v_exp_f32 v60, v60\n"
    "v_mfma_f32_16x16x32_bf16 v[136:139], v[164:167], v[180:183], v[136:139]\n"
    "ds_read_b64_tr_b16 v[160:161], v197 offset:41088\n"
    "v_exp_f32 v61, v61\n"
    "v_mfma_f32_32x32x8_bf16 v[40:55], v[108:109], v[32:33], 0\n"
    "v_exp_f32 v62, v62\n"
    "v_exp_f32 v63, v63\n"
    "v_exp_f32 v64, v64\n"
    "v_exp_f32 v65, v65\n"
    "v_mfma_f32_32x32x16_bf16 v[40:55], v[104:107], v[28:31], v[40:55]\n"
    "v_exp_f32 v66, v66\n"
    "v_exp_f32 v67, v67\n"
    "v_exp_f32 v68, v68\n"
    "v_mfma_f32_16x16x32_bf16 v[140:143], v[164:167], v[184:187], v[140:143]\n"
    "ds_read_b64_tr_b16 v[162:163], v197 offset:43648\n"
    "v_exp_f32 v69, v69\n"
    "v_smfmac_f32_32x32x32_bf16 v[88:103], v[112:115], v[56:63], v110\n"
    "ds_read_b128 v[112:115], v13 offset:24576\n"
    "v_exp_f32 v70, v70\n"
    "v_exp_f32 v71, v71\n"
    "v_exp_f32 v40, v40\n"
    "v_exp_f32 v41, v41\n"
    "v_smfmac_f32_32x32x32_bf16 v[88:103], v[116:119], v[64:71], v110\n"
    "ds_read_b128 v[116:119], v13 offset:32256\n"
    "s_add_u32 m0, s43, 122880\n"
    "s_add_u32 s38, s44, 204800\n"
    "s_bitcmp1_b32 s64, 0\n"
    "s_cbranch_scc0 WS5_%=\n"
    "buffer_load_dwordx4 v4, s[16:19], s38 offen offset:2048 lds\n"
    "WS5_%=:\n"
    "buffer_load_dwordx4 v2, s[16:19], s38 offen lds\n"
    "buffer_load_dwordx4 v3, s[16:19], s38 offen offset:1024 lds\n"
    "s_waitcnt lgkmcnt(1)\n"
    "v_exp_f32 v42, v42\n"
    "v_exp_f32 v43, v43\n"
    "v_exp_f32 v44, v44\n"
    "v_mfma_f32_16x16x32_bf16 v[144:147], v[168:171], v[180:183], v[144:147]\n"
    "ds_read_b64_tr_b16 v[164:165], v198 offset:20480\n"
    "v_exp_f32 v45, v45\n"
    "v_mfma_f32_32x32x8_bf16 v[56:71], v[108:109], v[34:35], 0\n"
    "ds_read_b32 v108, v12 offset:30720\n"
    "v_exp_f32 v46, v46\n"
    "v_exp_f32 v47, v47\n"
    "v_exp_f32 v48, v48\n"
    "v_exp_f32 v49, v49\n"
    "v_mfma_f32_32x32x16_bf16 v[56:71], v[104:107], v[36:39], v[56:71]\n"
    "ds_read_b128 v[104:107], v11 offset:30720\n"
    "v_exp_f32 v50, v50\n"
    "v_exp_f32 v51, v51\n"
    "v_exp_f32 v52, v52\n"
    "v_mfma_f32_16x16x32_bf16 v[148:151], v[168:171], v[184:187], v[148:151]\n"
    "ds_read_b64_tr_b16 v[166:167], v198 offset:23040\n"
    "v_exp_f32 v53, v53\n"
    "v_smfmac_f32_32x32x32_bf16 v[72:87], v[112:115], v[40:47], v110\n"
    "s_waitcnt lgkmcnt(4)\n"
    "v_exp_f32 v54, v54\n"
    "v_exp_f32 v55, v55\n"
    "v_exp_f32 v56, v56\n"
    "v_exp_f32 v57, v57\n"
    "v_smfmac_f32_32x32x32_bf16 v[72:87], v[116:119], v[48:55], v110\n"
    "s_waitcnt lgkmcnt(1)\n"
    "v_exp_f32 v58, v58\n"
    "v_exp_f32 v59, v59\n"
    "v_exp_f32 v60, v60\n"
    "v_mfma_f32_16x16x32_bf16 v[152:155], v[160:163], v[180:183], v[152:155]\n"
    "ds_read_b64_tr_b16 v[168:169], v198 offset:20512\n"
    "v_exp_f32 v61, v61\n"
    "v_mfma_f32_32x32x8_bf16 v[40:55], v[108:109], v[32:33], 0\n"
    "v_exp_f32 v62, v62\n"
    "v_exp_f32 v63, v63\n"
    "v_exp_f32 v64, v64\n"
    "v_exp_f32 v65, v65\n"
    "v_mfma_f32_32x32x16_bf16 v[40:55], v[104:107], v[28:31], v[40:55]\n"
    "v_exp_f32 v66, v66\n"
    "v_exp_f32 v67, v67\n"
    "v_exp_f32 v68, v68\n"
    "v_mfma_f32_16x16x32_bf16 v[156:159], v[160:163], v[184:187], v[156:159]\n"
    "ds_read_b64_tr_b16 v[170:171], v198 offset:23072\n"
    "v_exp_f32 v69, v69\n"
    "v_smfmac_f32_32x32x32_bf16 v[88:103], v[112:115], v[56:63], v110\n"
    "ds_read_b128 v[112:115], v13 offset:30720\n"
    "v_exp_f32 v70, v70\n"
    "v_exp_f32 v71, v71\n"
    "v_exp_f32 v40, v40\n"
    "v_exp_f32 v41, v41\n"
    "v_smfmac_f32_32x32x32_bf16 v[88:103], v[116:119], v[64:71], v110\n"
    "ds_read_b128 v[116:119], v13 offset:38400\n"
    "s_add_u32 s39, s51, 4096\n"
    "s_add_u32 s40, s52, 4096\n"
    "buffer_load_dwordx4 v[180:183], v21, s[8:11], s39 offen offset:0 sc1\n"
    "buffer_load_dwordx4 v[184:187], v21, s[8:11], s40 offen offset:0 sc1\n"
    "s_waitcnt lgkmcnt(1)\n"
    "s_waitcnt vmcnt(11)\n"
    "v_exp_f32 v42, v42\n"
    "v_exp_f32 v43, v43\n"
    "v_exp_f32 v44, v44\n"
    "v_mfma_f32_16x16x32_bf16 v[120:123], v[164:167], v[188:191], v[120:123]\n"
    "ds_read_b64_tr_b16 v[160:161], v198 offset:20544\n"
    "v_exp_f32 v45, v45\n"
    "v_mfma_f32_32x32x8_bf16 v[56:71], v[108:109], v[34:35], 0\n"
    "ds_read_b32 v108, v12 offset:36864\n"
    "v_exp_f32 v46, v46\n"
    "v_exp_f32 v47, v47\n"
    "v_exp_f32 v48, v48\n"
    "v_exp_f32 v49, v49\n"
    "v_mfma_f32_32x32x16_bf16 v[56:71], v[104:107], v[36:39], v[56:71]\n"
    "ds_read_b128 v[104:107], v11 offset:36864\n"
    "v_exp_f32 v50, v50\n"
    "v_exp_f32 v51, v51\n"
    "v_exp_f32 v52, v52\n"
    "v_mfma_f32_16x16x32_bf16 v[124:127], v[164:167], v[192:195], v[124:127]\n"
    "ds_read_b64_tr_b16 v[162:163], v198 offset:23104\n"
    "v_exp_f32 v53, v53\n"
    "v_smfmac_f32_32x32x32_bf16 v[72:87], v[112:115], v[40:47], v110\n"
    "s_waitcnt lgkmcnt(4)\n"
    "v_exp_f32 v54, v54\n"
    "v_exp_f32 v55, v55\n"
    "v_exp_f32 v56, v56\n"
    "v_exp_f32 v57, v57\n"
    "v_smfmac_f32_32x32x32_bf16 v[72:87], v[116:119], v[48:55], v110\n"
    "s_setprio 2\n"
    "s_waitcnt lgkmcnt(1)\n"
    "v_exp_f32 v58, v58\n"
    "v_exp_f32 v59, v59\n"
    "v_exp_f32 v60, v60\n"
    "v_mfma_f32_16x16x32_bf16 v[128:131], v[168:171], v[188:191], v[128:131]\n"
    "ds_read_b64_tr_b16 v[164:165], v198 offset:20576\n"
    "v_exp_f32 v61, v61\n"
    "v_mfma_f32_32x32x8_bf16 v[40:55], v[108:109], v[32:33], 0\n"
    "v_exp_f32 v62, v62\n"
    "v_exp_f32 v63, v63\n"
    "v_exp_f32 v64, v64\n"
    "v_exp_f32 v65, v65\n"
    "v_mfma_f32_32x32x16_bf16 v[40:55], v[104:107], v[28:31], v[40:55]\n"
    "v_exp_f32 v66, v66\n"
    "v_exp_f32 v67, v67\n"
    "v_exp_f32 v68, v68\n"
    "v_mfma_f32_16x16x32_bf16 v[132:135], v[168:171], v[192:195], v[132:135]\n"
    "ds_read_b64_tr_b16 v[166:167], v198 offset:23136\n"
    "v_exp_f32 v69, v69\n"
    "v_smfmac_f32_32x32x32_bf16 v[88:103], v[112:115], v[56:63], v110\n"
    "ds_read_b128 v[112:115], v13 offset:36864\n"
    "v_exp_f32 v70, v70\n"
    "v_exp_f32 v71, v71\n"
    "v_exp_f32 v40, v40\n"
    "v_exp_f32 v41, v41\n"
    "v_smfmac_f32_32x32x32_bf16 v[88:103], v[116:119], v[64:71], v110\n"
    "ds_read_b128 v[116:119], v13 offset:44544\n"
    "s_waitcnt lgkmcnt(1)\n"
    "v_exp_f32 v42, v42\n"
    "v_exp_f32 v43, v43\n"
    "v_exp_f32 v44, v44\n"
    "v_mfma_f32_16x16x32_bf16 v[136:139], v[160:163], v[188:191], v[136:139]\n"
    "ds_read_b64_tr_b16 v[168:169], v198 offset:20608\n"
    "v_exp_f32 v45, v45\n"
    "v_mfma_f32_32x32x8_bf16 v[56:71], v[108:109], v[34:35], 0\n"
    "ds_read_b32 v108, v12 offset:43008\n"
    "v_exp_f32 v46, v46\n"
    "v_exp_f32 v47, v47\n"
    "v_exp_f32 v48, v48\n"
    "v_exp_f32 v49, v49\n"
    "v_mfma_f32_32x32x16_bf16 v[56:71], v[104:107], v[36:39], v[56:71]\n"
    "ds_read_b128 v[104:107], v11 offset:43008\n"
    "v_exp_f32 v50, v50\n"
    "v_exp_f32 v51, v51\n"
    "v_exp_f32 v52, v52\n"
    "v_mfma_f32_16x16x32_bf16 v[140:143], v[160:163], v[192:195], v[140:143]\n"
    "ds_read_b64_tr_b16 v[170:171], v198 offset:23168\n"
    "v_exp_f32 v53, v53\n"
    "v_smfmac_f32_32x32x32_bf16 v[72:87], v[112:115], v[40:47], v110\n"
    "s_waitcnt lgkmcnt(4)\n"
    "v_exp_f32 v54, v54\n"
    "v_exp_f32 v55, v55\n"
    "v_exp_f32 v56, v56\n"
    "v_exp_f32 v57, v57\n"
    "v_smfmac_f32_32x32x32_bf16 v[72:87], v[116:119], v[48:55], v110\n"
    "s_setprio 1\n"
    "s_waitcnt lgkmcnt(1)\n"
    "v_exp_f32 v58, v58\n"
    "v_exp_f32 v59, v59\n"
    "v_exp_f32 v60, v60\n"
    "v_mfma_f32_16x16x32_bf16 v[144:147], v[164:167], v[188:191], v[144:147]\n"
    "ds_read_b64_tr_b16 v[160:161], v196 offset:0\n"
    "v_exp_f32 v61, v61\n"
    "v_mfma_f32_32x32x8_bf16 v[40:55], v[108:109], v[32:33], 0\n"
    "v_exp_f32 v62, v62\n"
    "v_exp_f32 v63, v63\n"
    "v_exp_f32 v64, v64\n"
    "v_exp_f32 v65, v65\n"
    "v_mfma_f32_32x32x16_bf16 v[40:55], v[104:107], v[28:31], v[40:55]\n"
    "s_setprio 0\n"
    "v_exp_f32 v66, v66\n"
    "v_exp_f32 v67, v67\n"
    "v_exp_f32 v68, v68\n"
    "v_mfma_f32_16x16x32_bf16 v[148:151], v[164:167], v[192:195], v[148:151]\n"
    "ds_read_b64_tr_b16 v[162:163], v196 offset:2560\n"
    "v_exp_f32 v69, v69\n"
    "v_smfmac_f32_32x32x32_bf16 v[88:103], v[112:115], v[56:63], v110\n"
    "ds_read_b128 v[112:115], v13 offset:43008\n"
    "v_exp_f32 v70, v70\n"
    "v_exp_f32 v71, v71\n"
    "v_exp_f32 v40, v40\n"
    "v_exp_f32 v41, v41\n"
    "v_smfmac_f32_32x32x32_bf16 v[88:103], v[116:119], v[64:71], v110\n"
    "s_waitcnt vmcnt(4) lgkmcnt(0)\n"
    "s_barrier\n"
    "s_setprio 3\n"
    "ds_read_b128 v[116:119], v13 offset:1536\n"
    "s_cmp_eq_u32 s71, 0\n"
    "s_cbranch_scc1 EPA_%=\n"
    "EPB_%=:\n"
    "v_exp_f32 v42, v42\n"
    "v_exp_f32 v43, v43\n"
    "v_exp_f32 v44, v44\n"
    "v_mfma_f32_16x16x32_bf16 v[152:155], v[168:171], v[188:191], v[152:155]\n"
    "ds_read_b64_tr_b16 v[164:165], v196 offset:32\n"
    "v_exp_f32 v45, v45\n"
    "v_mfma_f32_32x32x8_bf16 v[56:71], v[108:109], v[34:35], 0\n"
    "ds_read_b32 v108, v12 offset:0\n"
    "v_exp_f32 v46, v46\n"
    "v_exp_f32 v47, v47\n"
    "v_exp_f32 v48, v48\n"
    "v_exp_f32 v49, v49\n"
    "v_mfma_f32_32x32x16_bf16 v[56:71], v[104:107], v[36:39], v[56:71]\n"
    "ds_read_b128 v[104:107], v11 offset:0\n"
    "v_exp_f32 v50, v50\n"
    "v_exp_f32 v51, v51\n"
    "v_exp_f32 v52, v52\n"
    "v_mfma_f32_16x16x32_bf16 v[156:159], v[168:171], v[192:195], v[156:159]\n"
    "ds_read_b64_tr_b16 v[166:167], v196 offset:2592\n"
    "v_exp_f32 v53, v53\n"
    "v_smfmac_f32_32x32x32_bf16 v[72:87], v[112:115], v[40:47], v110\n"
    "s_waitcnt lgkmcnt(4)\n"
    "v_exp_f32 v54, v54\n"
    "v_exp_f32 v55, v55\n"
    "v_exp_f32 v56, v56\n"
    "v_exp_f32 v57, v57\n"
    "v_smfmac_f32_32x32x32_bf16 v[72:87], v[116:119], v[48:55], v110\n"
    "s_add_u32 m0, s61, 24576\n"
    "s_add_u32 s38, s62, 122880\n"
    "buffer_load_dwordx4 v21, s[8:11], s38 offen sc1 lds\n"
    "buffer_load_dwordx4 v21, s[8:11], s38 offen offset:1024 sc1 lds\n"
    "buffer_load_dwordx4 v21, s[8:11], s38 offen offset:2048 sc1 lds\n"
    "s_waitcnt lgkmcnt(1)\n"
    "v_exp_f32 v58, v58\n"
    "v_exp_f32 v59, v59\n"
    "v_exp_f32 v60, v60\n"
    "v_mfma_f32_16x16x32_bf16 v[120:123], v[160:163], v[172:175], v[120:123]\n"
    "ds_read_b64_tr_b16 v[168:169], v196 offset:64\n"
    "v_exp_f32 v61, v61\n"
    "v_mfma_f32_32x32x8_bf16 v[40:55], v[108:109], v[32:33], 0\n"
    "v_exp_f32 v62, v62\n"
    "v_exp_f32 v63, v63\n"
    "v_exp_f32 v64, v64\n"
    "v_exp_f32 v65, v65\n"
    "v_mfma_f32_32x32x16_bf16 v[40:55], v[104:107], v[28:31], v[40:55]\n"
    "v_exp_f32 v66, v66\n"
    "v_exp_f32 v67, v67\n"
    "v_exp_f32 v68, v68\n"
    "v_mfma_f32_16x16x32_bf16 v[124:127], v[160:163], v[176:179], v[124:127]\n"
    "ds_read_b64_tr_b16 v[170:171], v196 offset:2624\n"
    "v_exp_f32 v69, v69\n"
    "v_smfmac_f32_32x32x32_bf16 v[88:103], v[112:115], v[56:63], v110\n"
    "ds_read_b128 v[112:115], v13 offset:0\n"
    "v_exp_f32 v70, v70\n"
    "v_exp_f32 v71, v71\n"
    "v_exp_f32 v40, v40\n"
    "v_exp_f32 v41, v41\n"
    "v_smfmac_f32_32x32x32_bf16 v[88:103], v[116:119], v[64:71], v110\n"
    "ds_read_b128 v[116:119], v13 offset:7680\n"
    "s_add_u32 m0, s43, 102400\n"
    "s_add_u32 s38, s44, 245760\n"
    "s_bitcmp1_b32 s64, 0\n"
    "s_cbranch_scc1 WS6_%=\n"
    "buffer_load_dwordx4 v2, s[16:19], s38 offen lds\n"
    "WS6_%=:\n"
    "buffer_load_dwordx4 v3, s[16:19], s38 offen offset:1024 lds\n"
    "buffer_load_dwordx4 v4, s[16:19], s38 offen offset:2048 lds\n"
    "s_waitcnt lgkmcnt(1)\n"
    "v_exp_f32 v42, v42\n"
    "v_exp_f32 v43, v43\n"
    "v_exp_f32 v44, v44\n"
    "v_mfma_f32_16x16x32_bf16 v[128:131], v[164:167], v[172:175], v[128:131]\n"
    "ds_read_b64_tr_b16 v[160:161], v196 offset:96\n"
    "v_exp_f32 v45, v45\n"
    "v_mfma_f32_32x32x8_bf16 v[56:71], v[108:109], v[34:35], 0\n"
    "ds_read_b32 v108, v12 offset:6144\n"
    "v_exp_f32 v46, v46\n"
    "v_exp_f32 v47, v47\n"
    "v_exp_f32 v48, v48\n"
    "v_exp_f32 v49, v49\n"
    "v_mfma_f32_32x32x16_bf16 v[56:71], v[104:107], v[36:39], v[56:71]\n"
    "ds_read_b128 v[104:107], v11 offset:6144\n"
    "v_exp_f32 v50, v50\n"
    "v_exp_f32 v51, v51\n"
    "v_exp_f32 v52, v52\n"
    "v_mfma_f32_16x16x32_bf16 v[132:135], v[164:167], v[176:179], v[132:135]\n"
    "ds_read_b64_tr_b16 v[162:163], v196 offset:2656\n"
    "v_exp_f32 v53, v53\n"
    "v_smfmac_f32_32x32x32_bf16 v[72:87], v[112:115], v[40:47], v110\n"
    "s_waitcnt lgkmcnt(4)\n"
    "v_exp_f32 v54, v54\n"
    "v_exp_f32 v55, v55\n"
    "v_exp_f32 v56, v56\n"
    "v_exp_f32 v57, v57\n"
    "v_smfmac_f32_32x32x32_bf16 v[72:87], v[116:119], v[48:55], v110\n"
    "buffer_load_dwordx4 v[188:191], v21, s[8:11], s39 offen offset:1024 sc1\n"
    "buffer_load_dwordx4 v[192:195], v21, s[8:11], s40 offen offset:1024 sc1\n"
    "s_waitcnt lgkmcnt(1)\n"
    "v_exp_f32 v58, v58\n"
    "v_exp_f32 v59, v59\n"
    "v_exp_f32 v60, v60\n"
    "v_mfma_f32_16x16x32_bf16 v[136:139], v[168:171], v[172:175], v[136:139]\n"
    "ds_read_b64_tr_b16 v[164:165], v196 offset:128\n"
    "v_exp_f32 v61, v61\n"
    "v_mfma_f32_32x32x8_bf16 v[40:55], v[108:109], v[32:33], 0\n"
    "v_exp_f32 v62, v62\n"
    "v_exp_f32 v63, v63\n"
    "v_exp_f32 v64, v64\n"
    "v_exp_f32 v65, v65\n"
    "v_mfma_f32_32x32x16_bf16 v[40:55], v[104:107], v[28:31], v[40:55]\n"
    "v_exp_f32 v66, v66\n"
    "v_exp_f32 v67, v67\n"
    "v_exp_f32 v68, v68\n"
    "v_mfma_f32_16x16x32_bf16 v[140:143], v[168:171], v[176:179], v[140:143]\n"
    "ds_read_b64_tr_b16 v[166:167], v196 offset:2688\n"
    "v_exp_f32 v69, v69\n"
    "v_smfmac_f32_32x32x32_bf16 v[88:103], v[112:115], v[56:63], v110\n"
    "ds_read_b128 v[112:115], v13 offset:6144\n"
    "v_exp_f32 v70, v70\n"
    "v_exp_f32 v71, v71\n"
    "v_exp_f32 v40, v40\n"
    "v_exp_f32 v41, v41\n"
    "v_smfmac_f32_32x32x32_bf16 v[88:103], v[116:119], v[64:71], v110\n"
    "ds_read_b128 v[116:119], v13 offset:13824\n"
    "s_add_u32 m0, s43, 143360\n"
    "s_add_u32 s38, s44, 286720\n"
    "s_bitcmp1_b32 s64, 0\n"
    "s_cbranch_scc0 WS7_%=\n"
    "buffer_load_dwordx4 v4, s[16:19], s38 offen offset:2048 lds\n"
    "WS7_%=:\n"
    "buffer_load_dwordx4 v2, s[16:19], s38 offen lds\n"
    "buffer_load_dwordx4 v3, s[16:19], s38 offen offset:1024 lds\n"
    "s_waitcnt lgkmcnt(1)\n"
    "v_exp_f32 v42, v42\n"
    "v_exp_f32 v43, v43\n"
    "v_exp_f32 v44, v44\n"
    "v_mfma_f32_16x16x32_bf16 v[144:147], v[160:163], v[172:175], v[144:147]\n"
    "ds_read_b64_tr_b16 v[168:169], v196 offset:20480\n"
    "v_exp_f32 v45, v45\n"
    "v_mfma_f32_32x32x8_bf16 v[56:71], v[108:109], v[34:35], 0\n"
    "ds_read_b32 v108, v12 offset:12288\n"
    "v_exp_f32 v46, v46\n"
    "v_exp_f32 v47, v47\n"
    "v_exp_f32 v48, v48\n"
    "v_exp_f32 v49, v49\n"
    "v_mfma_f32_32x32x16_bf16 v[56:71], v[104:107], v[36:39], v[56:71]\n"
    "ds_read_b128 v[104:107], v11 offset:12288\n"
    "v_exp_f32 v50, v50\n"
    "v_exp_f32 v51, v51\n"
    "v_exp_f32 v52, v52\n"
    "v_mfma_f32_16x16x32_bf16 v[148:151], v[160:163], v[176:179], v[148:151]\n"
    "ds_read_b64_tr_b16 v[170:171], v196 offset:23040\n"
    "v_exp_f32 v53, v53\n"
    "v_smfmac_f32_32x32x32_bf16 v[72:87], v[112:115], v[40:47], v110\n"
    "s_waitcnt lgkmcnt(4)\n"
    "v_exp_f32 v54, v54\n"
    "v_exp_f32 v55, v55\n"
    "v_exp_f32 v56, v56\n"
    "v_exp_f32 v57, v57\n"
    "v_smfmac_f32_32x32x32_bf16 v[72:87], v[116:119], v[48:55], v110\n"
    "s_setprio 2\n"
    "s_waitcnt lgkmcnt(1)\n"
    "v_exp_f32 v58, v58\n"
    "v_exp_f32 v59, v59\n"
    "v_exp_f32 v60, v60\n"
    "v_mfma_f32_16x16x32_bf16 v[152:155], v[164:167], v[172:175], v[152:155]\n"
    "ds_read_b64_tr_b16 v[160:161], v196 offset:20512\n"
    "v_exp_f32 v61, v61\n"
    "v_mfma_f32_32x32x8_bf16 v[40:55], v[108:109], v[32:33], 0\n"
    "v_exp_f32 v62, v62\n"
    "v_exp_f32 v63, v63\n"
    "v_exp_f32 v64, v64\n"
    "v_exp_f32 v65, v65\n"
    "v_mfma_f32_32x32x16_bf16 v[40:55], v[104:107], v[28:31], v[40:55]\n"
    "v_exp_f32 v66, v66\n"
    "v_exp_f32 v67, v67\n"
    "v_exp_f32 v68, v68\n"
    "v_mfma_f32_16x16x32_bf16 v[156:159], v[164:167], v[176:179], v[156:159]\n"
    "ds_read_b64_tr_b16 v[162:163], v196 offset:23072\n"
    "v_exp_f32 v69, v69\n"
    "v_smfmac_f32_32x32x32_bf16 v[88:103], v[112:115], v[56:63], v110\n"
    "ds_read_b128 v[112:115], v13 offset:12288\n"
    "v_exp_f32 v70, v70\n"
    "v_exp_f32 v71, v71\n"
    "v_exp_f32 v40, v40\n"
    "v_exp_f32 v41, v41\n"
    "v_smfmac_f32_32x32x32_bf16 v[88:103], v[116:119], v[64:71], v110\n"
    "ds_read_b128 v[116:119], v13 offset:19968\n"
    "buffer_load_dwordx4 v[172:175], v21, s[8:11], s39 offen offset:2048 sc1\n"
    "buffer_load_dwordx4 v[176:179], v21, s[8:11], s40 offen offset:2048 sc1\n"
    "s_waitcnt lgkmcnt(1)\n"
    "s_waitcnt vmcnt(11)\n"
    "v_exp_f32 v42, v42\n"
    "v_exp_f32 v43, v43\n"
    "v_exp_f32 v44, v44\n"
    "v_mfma_f32_16x16x32_bf16 v[120:123], v[168:171], v[180:183], v[120:123]\n"
    "ds_read_b64_tr_b16 v[164:165], v196 offset:20544\n"
    "v_exp_f32 v45, v45\n"
    "v_mfma_f32_32x32x8_bf16 v[56:71], v[108:109], v[34:35], 0\n"
    "ds_read_b32 v108, v12 offset:18432\n"
    "v_exp_f32 v46, v46\n"
    "v_exp_f32 v47, v47\n"
    "v_exp_f32 v48, v48\n"
    "v_exp_f32 v49, v49\n"
    "v_mfma_f32_32x32x16_bf16 v[56:71], v[104:107], v[36:39], v[56:71]\n"
    "ds_read_b128 v[104:107], v11 offset:18432\n"
    "v_exp_f32 v50, v50\n"
    "v_exp_f32 v51, v51\n"
    "v_exp_f32 v52, v52\n"
    "v_mfma_f32_16x16x32_bf16 v[124:127], v[168:171], v[184:187], v[124:127]\n"
    "ds_read_b64_tr_b16 v[166:167], v196 offset:23104\n"
    "v_exp_f32 v53, v53\n"
    "v_smfmac_f32_32x32x32_bf16 v[72:87], v[112:115], v[40:47], v110\n"
    "s_waitcnt lgkmcnt(4)\n"
    "v_exp_f32 v54, v54\n"
    "v_exp_f32 v55, v55\n"
    "v_exp_f32 v56, v56\n"
    "v_exp_f32 v57, v57\n"
    "v_smfmac_f32_32x32x32_bf16 v[72:87], v[116:119], v[48:55], v110\n"
    "s_setprio 1\n"
    "s_waitcnt lgkmcnt(1)\n"
    "v_exp_f32 v58, v58\n"
    "v_exp_f32 v59, v59\n"
    "v_exp_f32 v60, v60\n"
    "v_mfma_f32_16x16x32_bf16 v[128:131], v[160:163], v[180:183], v[128:131]\n"
    "ds_read_b64_tr_b16 v[168:169], v196 offset:20576\n"
    "v_exp_f32 v61, v61\n"
    "v_mfma_f32_32x32x8_bf16 v[40:55], v[108:109], v[32:33], 0\n"
    "v_exp_f32 v62, v62\n"
    "v_exp_f32 v63, v63\n"
    "v_exp_f32 v64, v64\n"
    "v_exp_f32 v65, v65\n"
    "v_mfma_f32_32x32x16_bf16 v[40:55], v[104:107], v[28:31], v[40:55]\n"
    "s_setprio 0\n"
    "v_exp_f32 v66, v66\n"
    "v_exp_f32 v67, v67\n"
    "v_exp_f32 v68, v68\n"
    "v_mfma_f32_16x16x32_bf16 v[132:135], v[160:163], v[184:187], v[132:135]\n"
    "ds_read_b64_tr_b16 v[170:171], v196 offset:23136\n"
    "v_exp_f32 v69, v69\n"
    "v_smfmac_f32_32x32x32_bf16 v[88:103], v[112:115], v[56:63], v110\n"
    "ds_read_b128 v[112:115], v13 offset:18432\n"
    "v_exp_f32 v70, v70\n"
    "v_exp_f32 v71, v71\n"
    "v_exp_f32 v40, v40\n"
    "v_exp_f32 v41, v41\n"
    "v_smfmac_f32_32x32x32_bf16 v[88:103], v[116:119], v[64:71], v110\n"
    "s_waitcnt vmcnt(6) lgkmcnt(0)\n"
    "s_barrier\n"
    "s_setprio 3\n"
    "ds_read_b128 v[116:119], v13 offset:26112\n"
    "s_cmp_eq_u32 s71, 0\n"
    "s_cbranch_scc1 EPO_%=\n"
    "EPR_%=:\n"
    "v_exp_f32 v42, v42\n"
    "v_exp_f32 v43, v43\n"
    "v_exp_f32 v44, v44\n"
    "v_mfma_f32_16x16x32_bf16 v[136:139], v[164:167], v[180:183], v[136:139]\n"
    "ds_read_b64_tr_b16 v[160:161], v196 offset:20608\n"
    "v_exp_f32 v45, v45\n"
    "v_mfma_f32_32x32x8_bf16 v[56:71], v[108:109], v[34:35], 0\n"
    "ds_read_b32 v108, v12 offset:24576\n"
    "v_exp_f32 v46, v46\n"
    "v_exp_f32 v47, v47\n"
    "v_exp_f32 v48, v48\n"
    "v_exp_f32 v49, v49\n"
    "v_mfma_f32_32x32x16_bf16 v[56:71], v[104:107], v[36:39], v[56:71]\n"
    "ds_read_b128 v[104:107], v11 offset:24576\n"
    "v_exp_f32 v50, v50\n"
    "v_exp_f32 v51, v51\n"
    "v_exp_f32 v52, v52\n"
    "v_mfma_f32_16x16x32_bf16 v[140:143], v[164:167], v[184:187], v[140:143]\n"
    "ds_read_b64_tr_b16 v[162:163], v196 offset:23168\n"
    "v_exp_f32 v53, v53\n"
    "v_smfmac_f32_32x32x32_bf16 v[72:87], v[112:115], v[40:47], v110\n"
    "s_waitcnt lgkmcnt(4)\n"
    "v_exp_f32 v54, v54\n"
    "v_exp_f32 v55, v55\n"
    "v_exp_f32 v56, v56\n"
    "v_exp_f32 v57, v57\n"
    "v_smfmac_f32_32x32x32_bf16 v[72:87], v[116:119], v[48:55], v110\n"
    "s_add_u32 m0, s61, 0\n"
    "s_add_u32 s38, s62, 147456\n"
    "buffer_load_dwordx4 v21, s[8:11], s38 offen sc1 lds\n"
    "buffer_load_dwordx4 v21, s[8:11], s38 offen offset:1024 sc1 lds\n"
    "buffer_load_dwordx4 v21, s[8:11], s38 offen offset:2048 sc1 lds\n"
    "s_waitcnt lgkmcnt(1)\n"
    "v_exp_f32 v58, v58\n"
    "v_exp_f32 v59, v59\n"
    "v_exp_f32 v60, v60\n"
    "v_mfma_f32_16x16x32_bf16 v[144:147], v[168:171], v[180:183], v[144:147]\n"
    "ds_read_b64_tr_b16 v[164:165], v198 offset:0\n"
    "v_exp_f32 v61, v61\n"
    "v_mfma_f32_32x32x8_bf16 v[40:55], v[108:109], v[32:33], 0\n"
    "v_exp_f32 v62, v62\n"
    "v_exp_f32 v63, v63\n"
    "v_exp_f32 v64, v64\n"
    "v_exp_f32 v65, v65\n"
    "v_mfma_f32_32x32x16_bf16 v[40:55], v[104:107], v[28:31], v[40:55]\n"
    "v_exp_f32 v66, v66\n"
    "v_exp_f32 v67, v67\n"
    "v_exp_f32 v68, v68\n"
    "v_mfma_f32_16x16x32_bf16 v[148:151], v[168:171], v[184:187], v[148:151]\n"
    "ds_read_b64_tr_b16 v[166:167], v198 offset:2560\n"
    "v_exp_f32 v69, v69\n"
    "v_smfmac_f32_32x32x32_bf16 v[88:103], v[112:115], v[56:63], v110\n"
    "ds_read_b128 v[112:115], v13 offset:24576\n"
    "v_exp_f32 v70, v70\n"
    "v_exp_f32 v71, v71\n"
    "v_exp_f32 v40, v40\n"
    "v_exp_f32 v41, v41\n"
    "v_smfmac_f32_32x32x32_bf16 v[88:103], v[116:119], v[64:71], v110\n"
    "ds_read_b128 v[116:119], v13 offset:32256\n"
    "s_add_u32 m0, s43, 0\n"
    "s_add_u32 s38, s44, 327680\n"
    "s_bitcmp1_b32 s64, 0\n"
    "s_cbranch_scc1 WS8_%=\n"
    "buffer_load_dwordx4 v2, s[16:19], s38 offen lds\n"
    "WS8_%=:\n"
    "buffer_load_dwordx4 v3, s[16:19], s38 offen offset:1024 lds\n"
    "buffer_load_dwordx4 v4, s[16:19], s38 offen offset:2048 lds\n"
    "s_waitcnt lgkmcnt(1)\n"
    "v_exp_f32 v42, v42\n"
    "v_exp_f32 v43, v43\n"
    "v_exp_f32 v44, v44\n"
    "v_mfma_f32_16x16x32_bf16 v[152:155], v[160:163], v[180:183], v[152:155]\n"
    "ds_read_b64_tr_b16 v[168:169], v198 offset:32\n"
    "v_exp_f32 v45, v45\n"
    "v_mfma_f32_32x32x8_bf16 v[56:71], v[108:109], v[34:35], 0\n"
    "ds_read_b32 v108, v12 offset:30720\n"
    "v_exp_f32 v46, v46\n"
    "v_exp_f32 v47, v47\n"
    "v_exp_f32 v48, v48\n"
    "v_exp_f32 v49, v49\n"
    "v_mfma_f32_32x32x16_bf16 v[56:71], v[104:107], v[36:39], v[56:71]\n"
    "ds_read_b128 v[104:107], v11 offset:30720\n"
    "v_exp_f32 v50, v50\n"
    "v_exp_f32 v51, v51\n"
    "v_exp_f32 v52, v52\n"
    "v_mfma_f32_16x16x32_bf16 v[156:159], v[160:163], v[184:187], v[156:159]\n"
    "ds_read_b64_tr_b16 v[170:171], v198 offset:2592\n"
    "v_exp_f32 v53, v53\n"
    "v_smfmac_f32_32x32x32_bf16 v[72:87], v[112:115], v[40:47], v110\n"
    "s_waitcnt lgkmcnt(4)\n"
    "v_exp_f32 v54, v54\n"
    "v_exp_f32 v55, v55\n"
    "v_exp_f32 v56, v56\n"
    "v_exp_f32 v57, v57\n"
    "v_smfmac_f32_32x32x32_bf16 v[72:87], v[116:119], v[48:55], v110\n"
    "buffer_load_dwordx4 v[180:183], v21, s[8:11], s39 offen offset:3072 sc1\n"
    "buffer_load_dwordx4 v[184:187], v21, s[8:11], s40 offen offset:3072 sc1\n"
    "s_waitcnt lgkmcnt(1)\n"
    "s_waitcnt vmcnt(11)\n"
    "v_exp_f32 v58, v58\n"
    "v_exp_f32 v59, v59\n"
    "v_exp_f32 v60, v60\n"
    "v_mfma_f32_16x16x32_bf16 v[120:123], v[164:167], v[188:191], v[120:123]\n"
    "ds_read_b64_tr_b16 v[160:161], v198 offset:64\n"
    "v_exp_f32 v61, v61\n"
    "v_mfma_f32_32x32x8_bf16 v[40:55], v[108:109], v[32:33], 0\n"
    "v_exp_f32 v62, v62\n"
    "v_exp_f32 v63, v63\n"
    "v_exp_f32 v64, v64\n"
    "v_exp_f32 v65, v65\n"
    "v_mfma_f32_32x32x16_bf16 v[40:55], v[104:107], v[28:31], v[40:55]\n"
    "v_exp_f32 v66, v66\n"
    "v_exp_f32 v67, v67\n"
    "v_exp_f32 v68, v68\n"
    "v_mfma_f32_16x16x32_bf16 v[124:127], v[164:167], v[192:195], v[124:127]\n"
    "ds_read_b64_tr_b16 v[162:163], v198 offset:2624\n"
    "v_exp_f32 v69, v69\n"
    "v_smfmac_f32_32x32x32_bf16 v[88:103], v[112:115], v[56:63], v110\n"
    "ds_read_b128 v[112:115], v13 offset:30720\n"
    "v_exp_f32 v70, v70\n"
    "v_exp_f32 v71, v71\n"
    "v_exp_f32 v40, v40\n"
    "v_exp_f32 v41, v41\n"
    "v_smfmac_f32_32x32x32_bf16 v[88:103], v[116:119], v[64:71], v110\n"
    "ds_read_b128 v[116:119], v13 offset:38400\n"
    "s_waitcnt lgkmcnt(1)\n"
    "v_exp_f32 v42, v42\n"
    "v_exp_f32 v43, v43\n"
    "v_exp_f32 v44, v44\n"
    "v_mfma_f32_16x16x32_bf16 v[128:131], v[168:171], v[188:191], v[128:131]\n"
    "ds_read_b64_tr_b16 v[164:165], v198 offset:96\n"
    "v_exp_f32 v45, v45\n"
    "v_mfma_f32_32x32x8_bf16 v[56:71], v[108:109], v[34:35], 0\n"
    "ds_read_b32 v108, v12 offset:36864\n"
    "v_exp_f32 v46, v46\n"
    "v_exp_f32 v47, v47\n"
    "v_exp_f32 v48, v48\n"
    "v_exp_f32 v49, v49\n"
    "v_mfma_f32_32x32x16_bf16 v[56:71], v[104:107], v[36:39], v[56:71]\n"
    "ds_read_b128 v[104:107], v11 offset:36864\n"
    "v_exp_f32 v50, v50\n"
    "v_exp_f32 v51, v51\n"
    "v_exp_f32 v52, v52\n"
    "v_mfma_f32_16x16x32_bf16 v[132:135], v[168:171], v[192:195], v[132:135]\n"
    "ds_read_b64_tr_b16 v[166:167], v198 offset:2656\n"
    "v_exp_f32 v53, v53\n"
    "v_smfmac_f32_32x32x32_bf16 v[72:87], v[112:115], v[40:47], v110\n"
    "s_waitcnt lgkmcnt(4)\n"
    "v_exp_f32 v54, v54\n"
    "v_exp_f32 v55, v55\n"
    "v_exp_f32 v56, v56\n"
    "v_exp_f32 v57, v57\n"
    "v_smfmac_f32_32x32x32_bf16 v[72:87], v[116:119], v[48:55], v110\n"
    "s_setprio 2\n"
    "s_waitcnt lgkmcnt(1)\n"
    "v_exp_f32 v58, v58\n"
    "v_exp_f32 v59, v59\n"
    "v_exp_f32 v60, v60\n"
    "v_mfma_f32_16x16x32_bf16 v[136:139], v[160:163], v[188:191], v[136:139]\n"
    "ds_read_b64_tr_b16 v[168:169], v198 offset:128\n"
    "v_exp_f32 v61, v61\n"
    "v_mfma_f32_32x32x8_bf16 v[40:55], v[108:109], v[32:33], 0\n"
    "v_exp_f32 v62, v62\n"
    "v_exp_f32 v63, v63\n"
    "v_exp_f32 v64, v64\n"
    "v_exp_f32 v65, v65\n"
    "v_mfma_f32_32x32x16_bf16 v[40:55], v[104:107], v[28:31], v[40:55]\n"
    "v_exp_f32 v66, v66\n"
    "v_exp_f32 v67, v67\n"
    "v_exp_f32 v68, v68\n"
    "v_mfma_f32_16x16x32_bf16 v[140:143], v[160:163], v[192:195], v[140:143]\n"
    "ds_read_b64_tr_b16 v[170:171], v198 offset:2688\n"
    "v_exp_f32 v69, v69\n"
    "v_smfmac_f32_32x32x32_bf16 v[88:103], v[112:115], v[56:63], v110\n"
    "ds_read_b128 v[112:115], v13 offset:36864\n"
    "v_exp_f32 v70, v70\n"
    "v_exp_f32 v71, v71\n"
    "v_exp_f32 v40, v40\n"
    "v_exp_f32 v41, v41\n"
    "v_smfmac_f32_32x32x32_bf16 v[88:103], v[116:119], v[64:71], v110\n"
    "ds_read_b128 v[116:119], v13 offset:44544\n"
    "buffer_load_dwordx2 v[208:209], v16, s[4:7], s47 offen\n"
    "buffer_load_dwordx2 v[210:211], v16, s[4:7], s47 offen offset:32\n"
    "buffer_load_dwordx2 v[212:213], v16, s[4:7], s48 offen offset:128\n"
    "s_waitcnt lgkmcnt(1)\n"
    "v_exp_f32 v42, v42\n"
    "v_exp_f32 v43, v43\n"
    "v_exp_f32 v44, v44\n"
    "v_mfma_f32_16x16x32_bf16 v[144:147], v[164:167], v[188:191], v[144:147]\n"
    "ds_read_b64_tr_b16 v[160:161], v197 offset:40960\n"
    "v_exp_f32 v45, v45\n"
    "v_mfma_f32_32x32x8_bf16 v[56:71], v[108:109], v[34:35], 0\n"
    "ds_read_b32 v108, v12 offset:43008\n"
    "v_exp_f32 v46, v46\n"
    "v_exp_f32 v47, v47\n"
    "v_exp_f32 v48, v48\n"
    "v_exp_f32 v49, v49\n"
    "v_mfma_f32_32x32x16_bf16 v[56:71], v[104:107], v[36:39], v[56:71]\n"
    "ds_read_b128 v[104:107], v11 offset:43008\n"
    "v_exp_f32 v50, v50\n"
    "v_exp_f32 v51, v51\n"
    "v_exp_f32 v52, v52\n"
    "v_mfma_f32_16x16x32_bf16 v[148:151], v[164:167], v[192:195], v[148:151]\n"
    "ds_read_b64_tr_b16 v[162:163], v197 offset:43520\n"
    "v_exp_f32 v53, v53\n"
    "v_smfmac_f32_32x32x32_bf16 v[72:87], v[112:115], v[40:47], v110\n"
    "s_waitcnt lgkmcnt(4)\n"
    "v_exp_f32 v54, v54\n"
    "v_exp_f32 v55, v55\n"
    "v_exp_f32 v56, v56\n"
    "v_exp_f32 v57, v57\n"
    "v_smfmac_f32_32x32x32_bf16 v[72:87], v[116:119], v[48:55], v110\n"
    "s_setprio 1\n"
    "s_waitcnt lgkmcnt(1)\n"
    "v_exp_f32 v58, v58\n"
    "v_exp_f32 v59, v59\n"
    "v_exp_f32 v60, v60\n"
    "v_mfma_f32_16x16x32_bf16 v[152:155], v[168:171], v[188:191], v[152:155]\n"
    "ds_read_b64_tr_b16 v[164:165], v197 offset:40992\n"
    "v_exp_f32 v61, v61\n"
    "v_mfma_f32_32x32x8_bf16 v[40:55], v[108:109], v[32:33], 0\n"
    "v_exp_f32 v62, v62\n"
    "v_exp_f32 v63, v63\n"
    "v_exp_f32 v64, v64\n"
    "v_exp_f32 v65, v65\n"
    "v_mfma_f32_32x32x16_bf16 v[40:55], v[104:107], v[28:31], v[40:55]\n"
    "s_setprio 0\n"
    "v_exp_f32 v66, v66\n"
    "v_exp_f32 v67, v67\n"
    "v_exp_f32 v68, v68\n"
    "v_mfma_f32_16x16x32_bf16 v[156:159], v[168:171], v[192:195], v[156:159]\n"
    "ds_read_b64_tr_b16 v[166:167], v197 offset:43552\n"
    "v_exp_f32 v69, v69\n"
    "v_smfmac_f32_32x32x32_bf16 v[88:103], v[112:115], v[56:63], v110\n"
    "ds_read_b128 v[112:115], v13 offset:43008\n"
    "v_exp_f32 v70, v70\n"
    "v_exp_f32 v71, v71\n"
    "v_exp_f32 v40, v40\n"
    "v_exp_f32 v41, v41\n"
    "v_smfmac_f32_32x32x32_bf16 v[88:103], v[116:119], v[64:71], v110\n"
    "s_waitcnt vmcnt(7) lgkmcnt(0)\n"
    "s_barrier\n"
    "s_setprio 3\n"
    "ds_read_b128 v[116:119], v13 offset:1536\n"
    "v_exp_f32 v42, v42\n"
    "v_exp_f32 v43, v43\n"
    "v_exp_f32 v44, v44\n"
    "v_mfma_f32_16x16x32_bf16 v[120:123], v[160:163], v[172:175], v[120:123]\n"
    "ds_read_b64_tr_b16 v[168:169], v197 offset:41024\n"
    "v_exp_f32 v45, v45\n"
    "v_mfma_f32_32x32x8_bf16 v[56:71], v[108:109], v[34:35], 0\n"
    "ds_read_b32 v108, v12 offset:0\n"
    "v_exp_f32 v46, v46\n"
    "v_exp_f32 v47, v47\n"
    "v_exp_f32 v48, v48\n"
    "v_exp_f32 v49, v49\n"
    "v_mfma_f32_32x32x16_bf16 v[56:71], v[104:107], v[36:39], v[56:71]\n"
    "ds_read_b128 v[104:107], v11 offset:0\n"
    "v_exp_f32 v50, v50\n"
    "v_exp_f32 v51, v51\n"
    "v_exp_f32 v52, v52\n"
    "v_mfma_f32_16x16x32_bf16 v[124:127], v[160:163], v[176:179], v[124:127]\n"
    "ds_read_b64_tr_b16 v[170:171], v197 offset:43584\n"
    "v_exp_f32 v53, v53\n"
    "v_smfmac_f32_32x32x32_bf16 v[72:87], v[112:115], v[40:47], v110\n"
    "s_waitcnt lgkmcnt(4)\n"
    "v_exp_f32 v54, v54\n"
    "v_exp_f32 v55, v55\n"
    "v_exp_f32 v56, v56\n"
    "v_exp_f32 v57, v57\n"
    "v_smfmac_f32_32x32x32_bf16 v[72:87], v[116:119], v[48:55], v110\n"
    "s_add_u32 m0, s61, 24576\n"
    "s_add_u32 s38, s62, 172032\n"
    "buffer_load_dwordx4 v21, s[8:11], s38 offen sc1 lds\n"
    "buffer_load_dwordx4 v21, s[8:11], s38 offen offset:1024 sc1 lds\n"
    "buffer_load_dwordx4 v21, s[8:11], s38 offen offset:2048 sc1 lds\n"
    "s_waitcnt lgkmcnt(1)\n"
    "v_exp_f32 v58, v58\n"
    "v_exp_f32 v59, v59\n"
    "v_exp_f32 v60, v60\n"
    "v_mfma_f32_16x16x32_bf16 v[128:131], v[164:167], v[172:175], v[128:131]\n"
    "ds_read_b64_tr_b16 v[160:161], v197 offset:41056\n"
    "v_exp_f32 v61, v61\n"
    "v_mfma_f32_32x32x8_bf16 v[40:55], v[108:109], v[32:33], 0\n"
    "v_exp_f32 v62, v62\n"
    "v_exp_f32 v63, v63\n"
    "v_exp_f32 v64, v64\n"
    "v_exp_f32 v65, v65\n"
    "v_mfma_f32_32x32x16_bf16 v[40:55], v[104:107], v[28:31], v[40:55]\n"
    "v_exp_f32 v66, v66\n"
    "v_exp_f32 v67, v67\n"
    "v_exp_f32 v68, v68\n"
    "v_mfma_f32_16x16x32_bf16 v[132:135], v[164:167], v[176:179], v[132:135]\n"
    "ds_read_b64_tr_b16 v[162:163], v197 offset:43616\n"
    "v_exp_f32 v69, v69\n"
    "v_smfmac_f32_32x32x32_bf16 v[88:103], v[112:115], v[56:63], v110\n"
    "ds_read_b128 v[112:115], v13 offset:0\n"
    "v_exp_f32 v70, v70\n"
    "v_exp_f32 v71, v71\n"
    "v_exp_f32 v40, v40\n"
    "v_exp_f32 v41, v41\n"
    "v_smfmac_f32_32x32x32_bf16 v[88:103], v[116:119], v[64:71], v110\n"
    "ds_read_b128 v[116:119], v13 offset:7680\n"
    "s_add_u32 m0, s43, 20480\n"
    "s_add_u32 s38, s44, 368640\n"
    "s_bitcmp1_b32 s64, 0\n"
    "s_cbranch_scc0 WS9_%=\n"
    "buffer_load_dwordx4 v4, s[16:19], s38 offen offset:2048 lds\n"
    "WS9_%=:\n"
    "buffer_load_dwordx4 v2, s[16:19], s38 offen lds\n"
    "buffer_load_dwordx4 v3, s[16:19], s38 offen offset:1024 lds\n"
    "s_waitcnt lgkmcnt(1)\n"
    "v_exp_f32 v42, v42\n"
    "v_exp_f32 v43, v43\n"
    "v_exp_f32 v44, v44\n"
    "v_mfma_f32_16x16x32_bf16 v[136:139], v[168:171], v[172:175], v[136:139]\n"
    "ds_read_b64_tr_b16 v[164:165], v197 offset:41088\n"
    "v_exp_f32 v45, v45\n"
    "v_mfma_f32_32x32x8_bf16 v[56:71], v[108:109], v[34:35], 0\n"
    "ds_read_b32 v108, v12 offset:6144\n"
    "v_exp_f32 v46, v46\n"
    "v_exp_f32 v47, v47\n"
    "v_exp_f32 v48, v48\n"
    "v_exp_f32 v49, v49\n"
    "v_mfma_f32_32x32x16_bf16 v[56:71], v[104:107], v[36:39], v[56:71]\n"
    "ds_read_b128 v[104:107], v11 offset:6144\n"
    "v_exp_f32 v50, v50\n"
    "v_exp_f32 v51, v51\n"
    "v_exp_f32 v52, v52\n"
    "v_mfma_f32_16x16x32_bf16 v[140:143], v[168:171], v[176:179], v[140:143]\n"
    "ds_read_b64_tr_b16 v[166:167], v197 offset:43648\n"
    "v_exp_f32 v53, v53\n"
    "v_smfmac_f32_32x32x32_bf16 v[72:87], v[112:115], v[40:47], v110\n"
    "s_waitcnt lgkmcnt(4)\n"
    "v_exp_f32 v54, v54\n"
    "v_exp_f32 v55, v55\n"
    "v_exp_f32 v56, v56\n"
    "v_exp_f32 v57, v57\n"
    "v_smfmac_f32_32x32x32_bf16 v[72:87], v[116:119], v[48:55], v110\n"
    "s_add_u32 s39, s51, 8192\n"
    "s_add_u32 s40, s52, 8192\n"
    "buffer_load_dwordx4 v[188:191], v21, s[8:11], s39 offen offset:0 sc1\n"
    "buffer_load_dwordx4 v[192:195], v21, s[8:11], s40 offen offset:0 sc1\n"
    "s_waitcnt lgkmcnt(1)\n"
    "v_exp_f32 v58, v58\n"
    "v_exp_f32 v59, v59\n"
    "v_exp_f32 v60, v60\n"
    "v_mfma_f32_16x16x32_bf16 v[144:147], v[160:163], v[172:175], v[144:147]\n"
    "ds_read_b64_tr_b16 v[168:169], v198 offset:20480\n"
    "v_exp_f32 v61, v61\n"
    "v_mfma_f32_32x32x8_bf16 v[40:55], v[108:109], v[32:33], 0\n"
    "v_exp_f32 v62, v62\n"
    "v_exp_f32 v63, v63\n"
    "v_exp_f32 v64, v64\n"
    "v_exp_f32 v65, v65\n"
    "v_mfma_f32_32x32x16_bf16 v[40:55], v[104:107], v[28:31], v[40:55]\n"
    "v_exp_f32 v66, v66\n"
    "v_exp_f32 v67, v67\n"
    "v_exp_f32 v68, v68\n"
    "v_mfma_f32_16x16x32_bf16 v[148:151], v[160:163], v[176:179], v[148:151]\n"
    "ds_read_b64_tr_b16 v[170:171], v198 offset:23040\n"
    "v_exp_f32 v69, v69\n"
    "v_smfmac_f32_32x32x32_bf16 v[88:103], v[112:115], v[56:63], v110\n"
    "ds_read_b128 v[112:115], v13 offset:6144\n"
    "v_exp_f32 v70, v70\n"
    "v_exp_f32 v71, v71\n"
    "v_exp_f32 v40, v40\n"
    "v_exp_f32 v41, v41\n"
    "v_smfmac_f32_32x32x32_bf16 v[88:103], v[116:119], v[64:71], v110\n"
    "ds_read_b128 v[116:119], v13 offset:13824\n"
    "s_add_u32 m0, s43, 122880\n"
    "s_add_u32 s38, s44, 409600\n"
    "s_bitcmp1_b32 s64, 0\n"
    "s_cbranch_scc1 WS10_%=\n"
    "buffer_load_dwordx4 v2, s[16:19], s38 offen lds\n"
    "WS10_%=:\n"
    "buffer_load_dwordx4 v3, s[16:19], s38 offen offset:1024 lds\n"
    "buffer_load_dwordx4 v4, s[16:19], s38 offen offset:2048 lds\n"
    "s_waitcnt lgkmcnt(1)\n"
    "v_exp_f32 v42, v42\n"
    "v_exp_f32 v43, v43\n"
    "v_exp_f32 v44, v44\n"
    "v_mfma_f32_16x16x32_bf16 v[152:155], v[164:167], v[172:175], v[152:155]\n"
    "ds_read_b64_tr_b16 v[160:161], v198 offset:20512\n"
    "v_exp_f32 v45, v45\n"
    "v_mfma_f32_32x32x8_bf16 v[56:71], v[108:109], v[34:35], 0\n"
    "ds_read_b32 v108, v12 offset:12288\n"
    "v_exp_f32 v46, v46\n"
    "v_exp_f32 v47, v47\n"
    "v_exp_f32 v48, v48\n"
    "v_exp_f32 v49, v49\n"
    "v_mfma_f32_32x32x16_bf16 v[56:71], v[104:107], v[36:39], v[56:71]\n"
    "ds_read_b128 v[104:107], v11 offset:12288\n"
    "v_exp_f32 v50, v50\n"
    "v_exp_f32 v51, v51\n"
    "v_exp_f32 v52, v52\n"
    "v_mfma_f32_16x16x32_bf16 v[156:159], v[164:167], v[176:179], v[156:159]\n"
    "ds_read_b64_tr_b16 v[162:163], v198 offset:23072\n"
    "v_exp_f32 v53, v53\n"
    "v_smfmac_f32_32x32x32_bf16 v[72:87], v[112:115], v[40:47], v110\n"
    "s_waitcnt lgkmcnt(4)\n"
    "v_exp_f32 v54, v54\n"
    "v_exp_f32 v55, v55\n"
    "v_exp_f32 v56, v56\n"
    "v_exp_f32 v57, v57\n"
    "v_smfmac_f32_32x32x32_bf16 v[72:87], v[116:119], v[48:55], v110\n"
    "s_setprio 2\n"
    "buffer_load_dwordx4 v[172:175], v21, s[8:11], s39 offen offset:1024 sc1\n"
    "buffer_load_dwordx4 v[176:179], v21, s[8:11], s40 offen offset:1024 sc1\n"
    "s_waitcnt lgkmcnt(1)\n"
    "s_waitcnt vmcnt(14)\n"
    "v_exp_f32 v58, v58\n"
    "v_exp_f32 v59, v59\n"
    "v_exp_f32 v60, v60\n"
    "v_mfma_f32_16x16x32_bf16 v[120:123], v[168:171], v[180:183], v[120:123]\n"
    "ds_read_b64_tr_b16 v[164:165], v198 offset:20544\n"
    "v_exp_f32 v61, v61\n"
    "v_mfma_f32_32x32x8_bf16 v[40:55], v[108:109], v[32:33], 0\n"
    "v_exp_f32 v62, v62\n"
    "v_exp_f32 v63, v63\n"
    "v_exp_f32 v64, v64\n"
    "v_exp_f32 v65, v65\n"
    "v_mfma_f32_32x32x16_bf16 v[40:55], v[104:107], v[28:31], v[40:55]\n"
    "v_exp_f32 v66, v66\n"
    "v_exp_f32 v67, v67\n"
    "v_exp_f32 v68, v68\n"
    "v_mfma_f32_16x16x32_bf16 v[124:127], v[168:171], v[184:187], v[124:127]\n"
    "ds_read_b64_tr_b16 v[166:167], v198 offset:23104\n"
    "v_exp_f32 v69, v69\n"
    "v_smfmac_f32_32x32x32_bf16 v[88:103], v[112:115], v[56:63], v110\n"
    "ds_read_b128 v[112:115], v13 offset:12288\n"
    "v_exp_f32 v70, v70\n"
    "v_exp_f32 v71, v71\n"
    "v_exp_f32 v40, v40\n"
    "v_exp_f32 v41, v41\n"
    "v_smfmac_f32_32x32x32_bf16 v[88:103], v[116:119], v[64:71], v110\n"
    "ds_read_b128 v[116:119], v13 offset:19968\n"
    "s_waitcnt lgkmcnt(1)\n"
    "v_exp_f32 v42, v42\n"
    "v_exp_f32 v43, v43\n"
    "v_exp_f32 v44, v44\n"
    "v_mfma_f32_16x16x32_bf16 v[128:131], v[160:163], v[180:183], v[128:131]\n"
    "ds_read_b64_tr_b16 v[168:169], v198 offset:20576\n"
    "v_exp_f32 v45, v45\n"
    "v_mfma_f32_32x32x8_bf16 v[56:71], v[108:109], v[34:35], 0\n"
    "ds_read_b32 v108, v12 offset:18432\n"
    "v_exp_f32 v46, v46\n"
    "v_exp_f32 v47, v47\n"
    "v_exp_f32 v48, v48\n"
    "v_exp_f32 v49, v49\n"
    "v_mfma_f32_32x32x16_bf16 v[56:71], v[104:107], v[36:39], v[56:71]\n"
    "ds_read_b128 v[104:107], v11 offset:18432\n"
    "v_exp_f32 v50, v50\n"
    "v_exp_f32 v51, v51\n"
    "v_exp_f32 v52, v52\n"
    "v_mfma_f32_16x16x32_bf16 v[132:135], v[160:163], v[184:187], v[132:135]\n"
    "ds_read_b64_tr_b16 v[170:171], v198 offset:23136\n"
    "v_exp_f32 v53, v53\n"
    "v_smfmac_f32_32x32x32_bf16 v[72:87], v[112:115], v[40:47], v110\n"
    "s_waitcnt lgkmcnt(4)\n"
    "v_exp_f32 v54, v54\n"
    "v_exp_f32 v55, v55\n"
    "v_exp_f32 v56, v56\n"
    "v_exp_f32 v57, v57\n"
    "v_smfmac_f32_32x32x32_bf16 v[72:87], v[116:119], v[48:55], v110\n"
    "s_setprio 1\n"
    "s_waitcnt lgkmcnt(1)\n"
    "v_exp_f32 v58, v58\n"
    "v_exp_f32 v59, v59\n"
    "v_exp_f32 v60, v60\n"
    "v_mfma_f32_16x16x32_bf16 v[136:139], v[164:167], v[180:183], v[136:139]\n"
    "ds_read_b64_tr_b16 v[160:161], v198 offset:20608\n"
    "v_exp_f32 v61, v61\n"
    "v_mfma_f32_32x32x8_bf16 v[40:55], v[108:109], v[32:33], 0\n"
    "v_exp_f32 v62, v62\n"
    "v_exp_f32 v63, v63\n"
    "v_exp_f32 v64, v64\n"
    "v_exp_f32 v65, v65\n"
    "v_mfma_f32_32x32x16_bf16 v[40:55], v[104:107], v[28:31], v[40:55]\n"
    "s_setprio 0\n"
    "v_exp_f32 v66, v66\n"
    "v_exp_f32 v67, v67\n"
    "v_exp_f32 v68, v68\n"
    "v_mfma_f32_16x16x32_bf16 v[140:143], v[164:167], v[184:187], v[140:143]\n"
    "ds_read_b64_tr_b16 v[162:163], v198 offset:23168\n"
    "v_exp_f32 v69, v69\n"
    "v_smfmac_f32_32x32x32_bf16 v[88:103], v[112:115], v[56:63], v110\n"
    "ds_read_b128 v[112:115], v13 offset:18432\n"
    "v_exp_f32 v70, v70\n"
    "v_exp_f32 v71, v71\n"
    "v_exp_f32 v40, v40\n"
    "v_exp_f32 v41, v41\n"
    "v_smfmac_f32_32x32x32_bf16 v[88:103], v[116:119], v[64:71], v110\n"
    "s_waitcnt vmcnt(6) lgkmcnt(0)\n"
    "s_barrier\n"
    "s_setprio 3\n"
    "ds_read_b128 v[116:119], v13 offset:26112\n"
    "v_exp_f32 v42, v42\n"
    "v_exp_f32 v43, v43\n"
    "v_exp_f32 v44, v44\n"
    "v_mfma_f32_16x16x32_bf16 v[144:147], v[168:171], v[180:183], v[144:147]\n"
    "ds_read_b64_tr_b16 v[164:165], v196 offset:0\n"
    "v_exp_f32 v45, v45\n"
    "v_mfma_f32_32x32x8_bf16 v[56:71], v[108:109], v[34:35], 0\n"
    "ds_read_b32 v108, v12 offset:24576\n"
    "v_exp_f32 v46, v46\n"
    "v_exp_f32 v47, v47\n"
    "v_exp_f32 v48, v48\n"
    "v_exp_f32 v49, v49\n"
    "v_mfma_f32_32x32x16_bf16 v[56:71], v[104:107], v[36:39], v[56:71]\n"
    "ds_read_b128 v[104:107], v11 offset:24576\n"
    "v_exp_f32 v50, v50\n"
    "v_exp_f32 v51, v51\n"
    "v_exp_f32 v52, v52\n"
    "v_mfma_f32_16x16x32_bf16 v[148:151], v[168:171], v[184:187], v[148:151]\n"
    "ds_read_b64_tr_b16 v[166:167], v196 offset:2560\n"
    "v_exp_f32 v53, v53\n"
    "v_smfmac_f32_32x32x32_bf16 v[72:87], v[112:115], v[40:47], v110\n"
    "s_waitcnt lgkmcnt(4)\n"
    "v_exp_f32 v54, v54\n"
    "v_exp_f32 v55, v55\n"
    "v_exp_f32 v56, v56\n"
    "v_exp_f32 v57, v57\n"
    "v_smfmac_f32_32x32x32_bf16 v[72:87], v[116:119], v[48:55], v110\n"
    "s_add_u32 m0, s43, 40960\n"
    "s_add_u32 s38, s44, 450560\n"
    "s_bitcmp1_b32 s64, 0\n"
    "s_cbranch_scc0 WS11_%=\n"
    "buffer_load_dwordx4 v4, s[16:19], s38 offen offset:2048 lds\n"
    "WS11_%=:\n"
    "buffer_load_dwordx4 v2, s[16:19], s38 offen lds\n"
    "buffer_load_dwordx4 v3, s[16:19], s38 offen offset:1024 lds\n"
    "s_waitcnt lgkmcnt(1)\n"
    "v_exp_f32 v58, v58\n"
    "v_exp_f32 v59, v59\n"
    "v_exp_f32 v60, v60\n"
    "v_mfma_f32_16x16x32_bf16 v[152:155], v[160:163], v[180:183], v[152:155]\n"
    "ds_read_b64_tr_b16 v[168:169], v196 offset:32\n"
    "v_exp_f32 v61, v61\n"
    "v_mfma_f32_32x32x8_bf16 v[40:55], v[108:109], v[32:33], 0\n"
    "v_exp_f32 v62, v62\n"
    "v_exp_f32 v63, v63\n"
    "v_exp_f32 v64, v64\n"
    "v_exp_f32 v65, v65\n"
    "v_mfma_f32_32x32x16_bf16 v[40:55], v[104:107], v[28:31], v[40:55]\n"
    "v_exp_f32 v66, v66\n"
    "v_exp_f32 v67, v67\n"
    "v_exp_f32 v68, v68\n"
    "v_mfma_f32_16x16x32_bf16 v[156:159], v[160:163], v[184:187], v[156:159]\n"
    "ds_read_b64_tr_b16 v[170:171], v196 offset:2592\n"
    "v_exp_f32 v69, v69\n"
    "v_smfmac_f32_32x32x32_bf16 v[88:103], v[112:115], v[56:63], v110\n"
    "ds_read_b128 v[112:115], v13 offset:24576\n"
    "v_exp_f32 v70, v70\n"
    "v_exp_f32 v71, v71\n"
    "v_exp_f32 v40, v40\n"
    "v_exp_f32 v41, v41\n"
    "v_smfmac_f32_32x32x32_bf16 v[88:103], v[116:119], v[64:71], v110\n"
    "ds_read_b128 v[116:119], v13 offset:32256\n"
    "buffer_load_dwordx4 v[180:183], v21, s[8:11], s39 offen offset:2048 sc1\n"
    "buffer_load_dwordx4 v[184:187], v21, s[8:11], s40 offen offset:2048 sc1\n"
    "s_waitcnt lgkmcnt(1)\n"
    "s_waitcnt vmcnt(8)\n"
    "v_exp_f32 v42, v42\n"
    "v_exp_f32 v43, v43\n"
    "v_exp_f32 v44, v44\n"
    "v_mfma_f32_16x16x32_bf16 v[120:123], v[164:167], v[188:191], v[120:123]\n"
    "ds_read_b64_tr_b16 v[160:161], v196 offset:64\n"
    "v_exp_f32 v45, v45\n"
    "v_mfma_f32_32x32x8_bf16 v[56:71], v[108:109], v[34:35], 0\n"
    "ds_read_b32 v108, v12 offset:30720\n"
    "v_exp_f32 v46, v46\n"
    "v_exp_f32 v47, v47\n"
    "v_exp_f32 v48, v48\n"
    "v_exp_f32 v49, v49\n"
    "v_mfma_f32_32x32x16_bf16 v[56:71], v[104:107], v[36:39], v[56:71]\n"
    "ds_read_b128 v[104:107], v11 offset:30720\n"
    "v_exp_f32 v50, v50\n"
    "v_exp_f32 v51, v51\n"
    "v_exp_f32 v52, v52\n"
    "v_mfma_f32_16x16x32_bf16 v[124:127], v[164:167], v[192:195], v[124:127]\n"
    "ds_read_b64_tr_b16 v[162:163], v196 offset:2624\n"
    "v_exp_f32 v53, v53\n"
    "v_smfmac_f32_32x32x32_bf16 v[72:87], v[112:115], v[40:47], v110\n"
    "s_waitcnt lgkmcnt(4)\n"
    "v_exp_f32 v54, v54\n"
    "v_exp_f32 v55, v55\n"
    "v_exp_f32 v56, v56\n"
    "v_exp_f32 v57, v57\n"
    "v_smfmac_f32_32x32x32_bf16 v[72:87], v[116:119], v[48:55], v110\n"
    "s_setprio 2\n"
    "s_add_u32 m0, s43, 102400\n"
    "s_add_u32 s38, s45, 102400\n"
    "s_bitcmp1_b32 s64, 0\n"
    "s_cbranch_scc1 WS14_%=\n"
    "buffer_load_dwordx4 v5, s[20:23], s38 offen lds\n"
    "WS14_%=:\n"
    "buffer_load_dwordx4 v6, s[20:23], s38 offen offset:1024 lds\n"
    "buffer_load_dwordx4 v7, s[20:23], s38 offen offset:2048 lds\n"
    "s_waitcnt lgkmcnt(1)\n"
    "v_exp_f32 v58, v58\n"
    "v_exp_f32 v59, v59\n"
    "v_exp_f32 v60, v60\n"
    "v_mfma_f32_16x16x32_bf16 v[128:131], v[168:171], v[188:191], v[128:131]\n"
    "ds_read_b64_tr_b16 v[164:165], v196 offset:96\n"
    "v_exp_f32 v61, v61\n"
    "v_mfma_f32_32x32x8_bf16 v[40:55], v[108:109], v[32:33], 0\n"
    "v_exp_f32 v62, v62\n"
    "v_exp_f32 v63, v63\n"
    "v_exp_f32 v64, v64\n"
    "v_exp_f32 v65, v65\n"
    "v_mfma_f32_32x32x16_bf16 v[40:55], v[104:107], v[28:31], v[40:55]\n"
    "v_exp_f32 v66, v66\n"
    "v_exp_f32 v67, v67\n"
    "v_exp_f32 v68, v68\n"
    "v_mfma_f32_16x16x32_bf16 v[132:135], v[168:171], v[192:195], v[132:135]\n"
    "ds_read_b64_tr_b16 v[166:167], v196 offset:2656\n"
    "v_exp_f32 v69, v69\n"
    "v_smfmac_f32_32x32x32_bf16 v[88:103], v[112:115], v[56:63], v110\n"
    "ds_read_b128 v[112:115], v13 offset:30720\n"
    "v_exp_f32 v70, v70\n"
    "v_exp_f32 v71, v71\n"
    "v_exp_f32 v40, v40\n"
    "v_exp_f32 v41, v41\n"
    "v_smfmac_f32_32x32x32_bf16 v[88:103], v[116:119], v[64:71], v110\n"
    "ds_read_b128 v[116:119], v13 offset:38400\n"
    "s_waitcnt lgkmcnt(1)\n"
    "v_exp_f32 v42, v42\n"
    "v_exp_f32 v43, v43\n"
    "v_exp_f32 v44, v44\n"
    "v_mfma_f32_16x16x32_bf16 v[136:139], v[160:163], v[188:191], v[136:139]\n"
    "ds_read_b64_tr_b16 v[168:169], v196 offset:128\n"
    "v_exp_f32 v45, v45\n"
    "v_mfma_f32_32x32x8_bf16 v[56:71], v[108:109], v[34:35], 0\n"
    "ds_read_b32 v108, v12 offset:36864\n"
    "v_exp_f32 v46, v46\n"
    "v_exp_f32 v47, v47\n"
    "v_exp_f32 v48, v48\n"
    "v_exp_f32 v49, v49\n"
    "v_mfma_f32_32x32x16_bf16 v[56:71], v[104:107], v[36:39], v[56:71]\n"
    "ds_read_b128 v[104:107], v11 offset:36864\n"
    "v_exp_f32 v50, v50\n"
    "v_exp_f32 v51, v51\n"
    "v_exp_f32 v52, v52\n"
    "v_mfma_f32_16x16x32_bf16 v[140:143], v[160:163], v[192:195], v[140:143]\n"
    "ds_read_b64_tr_b16 v[170:171], v196 offset:2688\n"
    "v_exp_f32 v53, v53\n"
    "v_smfmac_f32_32x32x32_bf16 v[72:87], v[112:115], v[40:47], v110\n"
    "s_waitcnt lgkmcnt(4)\n"
    "v_exp_f32 v54, v54\n"
    "v_exp_f32 v55, v55\n"
    "v_exp_f32 v56, v56\n"
    "v_exp_f32 v57, v57\n"
    "v_smfmac_f32_32x32x32_bf16 v[72:87], v[116:119], v[48:55], v110\n"
    "s_setprio 1\n"
    "s_add_u32 m0, s43, 143360\n"
    "s_add_u32 s38, s45, 153600\n"
    "s_bitcmp1_b32 s64, 0\n"
    "s_cbranch_scc0 WS15_%=\n"
    "buffer_load_dwordx4 v7, s[20:23], s38 offen offset:2048 lds\n"
    "WS15_%=:\n"
    "buffer_load_dwordx4 v5, s[20:23], s38 offen lds\n"
    "buffer_load_dwordx4 v6, s[20:23], s38 offen offset:1024 lds\n"
    "s_waitcnt lgkmcnt(1)\n"
    "v_exp_f32 v58, v58\n"
    "v_exp_f32 v59, v59\n"
    "v_exp_f32 v60, v60\n"
    "v_mfma_f32_16x16x32_bf16 v[144:147], v[164:167], v[188:191], v[144:147]\n"
    "ds_read_b64_tr_b16 v[160:161], v196 offset:20480\n"
    "v_exp_f32 v61, v61\n"
    "v_mfma_f32_32x32x8_bf16 v[40:55], v[108:109], v[32:33], 0\n"
    "v_exp_f32 v62, v62\n"
    "v_exp_f32 v63, v63\n"
    "v_exp_f32 v64, v64\n"
    "v_exp_f32 v65, v65\n"
    "v_mfma_f32_32x32x16_bf16 v[40:55], v[104:107], v[28:31], v[40:55]\n"
    "s_setprio 0\n"
    "v_exp_f32 v66, v66\n"
    "v_exp_f32 v67, v67\n"
    "v_exp_f32 v68, v68\n"
    "v_mfma_f32_16x16x32_bf16 v[148:151], v[164:167], v[192:195], v[148:151]\n"
    "ds_read_b64_tr_b16 v[162:163], v196 offset:23040\n"
    "v_exp_f32 v69, v69\n"
    "v_smfmac_f32_32x32x32_bf16 v[88:103], v[112:115], v[56:63], v110\n"
    "ds_read_b128 v[112:115], v13 offset:36864\n"
    "v_exp_f32 v70, v70\n"
    "v_exp_f32 v71, v71\n"
    "v_exp_f32 v40, v40\n"
    "v_exp_f32 v41, v41\n"
    "v_smfmac_f32_32x32x32_bf16 v[88:103], v[116:119], v[64:71], v110\n"
    "s_waitcnt  lgkmcnt(0)\n"
    "s_barrier\n"
    "s_setprio 2\n"
    "ds_read_b128 v[116:119], v13 offset:44544\n"
    "s_add_u32 m0, s43, 0\n"
    "s_add_u32 s38, s45, 20480\n"
    "s_bitcmp1_b32 s64, 0\n"
    "s_cbranch_scc1 WS16_%=\n"
    "buffer_load_dwordx4 v8, s[20:23], s38 offen lds\n"
    "WS16_%=:\n"
    "buffer_load_dwordx4 v9, s[20:23], s38 offen offset:1024 lds\n"
    "buffer_load_dwordx4 v10, s[20:23], s38 offen offset:2048 lds\n"
    "v_exp_f32 v42, v42\n"
    "v_exp_f32 v43, v43\n"
    "v_exp_f32 v44, v44\n"
    "v_mfma_f32_16x16x32_bf16 v[152:155], v[168:171], v[188:191], v[152:155]\n"
    "v_exp_f32 v45, v45\n"
    "v_mfma_f32_32x32x8_bf16 v[56:71], v[108:109], v[34:35], 0\n"
    "v_exp_f32 v46, v46\n"
    "v_exp_f32 v47, v47\n"
    "v_exp_f32 v48, v48\n"
    "v_exp_f32 v49, v49\n"
    "v_mfma_f32_32x32x16_bf16 v[56:71], v[104:107], v[36:39], v[56:71]\n"
    "v_exp_f32 v50, v50\n"
    "v_exp_f32 v51, v51\n"
    "v_exp_f32 v52, v52\n"
    "v_mfma_f32_16x16x32_bf16 v[156:159], v[168:171], v[192:195], v[156:159]\n"
    "v_exp_f32 v53, v53\n"
    "v_smfmac_f32_32x32x32_bf16 v[72:87], v[112:115], v[40:47], v110\n"
    "s_waitcnt lgkmcnt(0)\n"
    "v_exp_f32 v54, v54\n"
    "v_exp_f32 v55, v55\n"
    "v_exp_f32 v56, v56\n"
    "v_exp_f32 v57, v57\n"
    "v_smfmac_f32_32x32x32_bf16 v[72:87], v[116:119], v[48:55], v110\n"
    "s_setprio 1\n"
    "buffer_load_dwordx4 v[188:191], v21, s[8:11], s39 offen offset:3072 sc1\n"
    "buffer_load_dwordx4 v[192:195], v21, s[8:11], s40 offen offset:3072 sc1\n"
    "s_waitcnt vmcnt(12)\n"
    "v_exp_f32 v58, v58\n"
    "v_exp_f32 v59, v59\n"
    "v_exp_f32 v60, v60\n"
    "v_mfma_f32_16x16x32_bf16 v[120:123], v[160:163], v[172:175], v[120:123]\n"
    "v_exp_f32 v61, v61\n"
    "v_exp_f32 v62, v62\n"
    "v_exp_f32 v63, v63\n"
    "v_exp_f32 v64, v64\n"
    "v_exp_f32 v65, v65\n"
    "s_setprio 0\n"
    "v_lshrrev_b32 v200, 1, v21\n"
    "v_sub_u32 v200, v111, v200\n"
    "s_nop 1\n"
    "ds_write_b128 v111, v[72:75] offset:0\n"
    "ds_write_b128 v111, v[76:79] offset:1024\n"
    "ds_write_b128 v111, v[80:83] offset:2048\n"
    "ds_write_b64 v200, v[84:85] offset:3072\n"
    "v_lshrrev_b32 v200, 1, v21\n"
    "v_sub_u32 v200, v199, v200\n"
    "v_exp_f32 v66, v66\n"
    "v_exp_f32 v67, v67\n"
    "v_exp_f32 v68, v68\n"
    "v_mfma_f32_16x16x32_bf16 v[124:127], v[160:163], v[176:179], v[124:127]\n"
    "v_exp_f32 v69, v69\n"
    "v_smfmac_f32_32x32x32_bf16 v[88:103], v[112:115], v[56:63], v110\n"
    "v_exp_f32 v70, v70\n"
    "v_exp_f32 v71, v71\n"
    "TL_%=:\n"
    "s_nop 1\n"
    "v_smfmac_f32_32x32x32_bf16 v[88:103], v[116:119], v[64:71], v110\n"
    "s_waitcnt vmcnt(10) lgkmcnt(0)\n"
    "s_barrier\n"
    "s_cmp_lg_u32 s64, 0\n"
    "s_cbranch_scc1 HW_%=\n"
    "v_mov_b32 v214, s37\n"
    "s_mov_b64 exec, 1\n"
    "buffer_store_dword v214, off, s[8:11], s81 sc1\n"
    "s_mov_b64 exec, -1\n"
    "HW_%=:\n"
    "s_setprio 1\n"
    "ds_read_b64 v[84:85], v200 offset:3072\n"
    "ds_read_b128 v[72:75], v199 offset:0\n"
    "ds_read_b128 v[76:79], v199 offset:1024\n"
    "ds_read_b128 v[80:83], v199 offset:2048\n"
    "ds_read_b64_tr_b16 v[164:165], v196 offset:20512\n"
    "ds_read_b64_tr_b16 v[166:167], v196 offset:23072\n"
    "ds_read_b64_tr_b16 v[168:169], v196 offset:20544\n"
    "ds_read_b64_tr_b16 v[170:171], v196 offset:23104\n"
    "ds_read_b64_tr_b16 v[160:161], v196 offset:20576\n"
    "ds_read_b64_tr_b16 v[162:163], v196 offset:23136\n"
    "ds_read_b64_tr_b16 v[104:105], v196 offset:20608\n"
    "ds_read_b64_tr_b16 v[106:107], v196 offset:23168\n"
    "s_waitcnt lgkmcnt(11)\n"
    "v_add_f32 v100, v100, v84\n"
    "v_mov_b32 v101, v100\n"
    "s_nop 1\n"
    "v_permlane32_swap_b32 v100, v101\n"
    "v_rcp_f32 v100, v100\n"
    "s_waitcnt lgkmcnt(10)\n"
    "v_pk_add_f32 v[88:89], v[88:89], v[72:73]\n"
    "v_pk_add_f32 v[90:91], v[90:91], v[74:75]\n"
    "s_waitcnt lgkmcnt(9)\n"
    "v_pk_add_f32 v[92:93], v[92:93], v[76:77]\n"
    "v_pk_add_f32 v[94:95], v[94:95], v[78:79]\n"
    "s_waitcnt lgkmcnt(8)\n"
    "v_pk_add_f32 v[96:97], v[96:97], v[80:81]\n"
    "v_pk_add_f32 v[98:99], v[98:99], v[82:83]\n"
    "v_pk_mul_f32 v[88:89], v[88:89], v[100:101] op_sel_hi:[1,0]\n"
    "v_pk_mul_f32 v[90:91], v[90:91], v[100:101] op_sel_hi:[1,0]\n"
    "v_pk_mul_f32 v[92:93], v[92:93], v[100:101] op_sel_hi:[1,0]\n"
    "v_pk_mul_f32 v[94:95], v[94:95], v[100:101] op_sel_hi:[1,0]\n"
    "v_pk_mul_f32 v[96:97], v[96:97], v[100:101] op_sel_hi:[1,0]\n"
    "v_pk_mul_f32 v[98:99], v[98:99], v[100:101] op_sel_hi:[1,0]\n"
    "v_cvt_pk_bf16_f32 v200, v88, v89\n"
    "v_cvt_pk_bf16_f32 v201, v90, v91\n"
    "v_cvt_pk_bf16_f32 v202, v92, v93\n"
    "v_cvt_pk_bf16_f32 v203, v94, v95\n"
    "v_cvt_pk_bf16_f32 v204, v96, v97\n"
    "v_cvt_pk_bf16_f32 v205, v98, v99\n"
    "buffer_load_dword v206, v14, s[8:11], s80 offen sc1\n"
    "buffer_store_dwordx4 v[200:203], v19, s[8:11], s57 offen sc1\n"
    "buffer_store_dwordx2 v[204:205], v20, s[8:11], s58 offen sc1\n"
    "s_setprio 0\n"
    "s_mov_b32 s36, 0x10000\n"
    "ds_read_b64_tr_b16 v[108:109], v198 offset:0\n"
    "ds_read_b64_tr_b16 v[110:111], v198 offset:2560\n"
    "ds_read_b64_tr_b16 v[112:113], v198 offset:32\n"
    "ds_read_b64_tr_b16 v[114:115], v198 offset:2592\n"
    "s_waitcnt vmcnt(5)\n"
    "s_barrier\n"
    "s_waitcnt lgkmcnt(10)\n"
    "v_mfma_f32_16x16x32_bf16 v[128:131], v[164:167], v[172:175], v[128:131]\n"
    "v_mfma_f32_16x16x32_bf16 v[132:135], v[164:167], v[176:179], v[132:135]\n"
    "ds_read_b64_tr_b16 v[80:81], v197 offset:40960\n"
    "ds_read_b64_tr_b16 v[82:83], v197 offset:43520\n"
    "ds_read_b64_tr_b16 v[84:85], v197 offset:40992\n"
    "ds_read_b64_tr_b16 v[86:87], v197 offset:43552\n"
    "ds_read_b64_tr_b16 v[116:117], v198 offset:64\n"
    "ds_read_b64_tr_b16 v[118:119], v198 offset:2624\n"
    "s_waitcnt lgkmcnt(14)\n"
    "v_mfma_f32_16x16x32_bf16 v[136:139], v[168:171], v[172:175], v[136:139]\n"
    "v_mfma_f32_16x16x32_bf16 v[140:143], v[168:171], v[176:179], v[140:143]\n"
    "ds_read_b64_tr_b16 v[164:165], v198 offset:96\n"
    "ds_read_b64_tr_b16 v[166:167], v198 offset:2656\n"
    "s_waitcnt lgkmcnt(14)\n"
    "v_mfma_f32_16x16x32_bf16 v[144:147], v[160:163], v[172:175], v[144:147]\n"
    "v_mfma_f32_16x16x32_bf16 v[148:151], v[160:163], v[176:179], v[148:151]\n"
    "ds_read_b64_tr_b16 v[168:169], v198 offset:128\n"
    "ds_read_b64_tr_b16 v[170:171], v198 offset:2688\n"
    "s_waitcnt lgkmcnt(14)\n"
    "v_mfma_f32_16x16x32_bf16 v[152:155], v[104:107], v[172:175], v[152:155]\n"
    "v_mfma_f32_16x16x32_bf16 v[156:159], v[104:107], v[176:179], v[156:159]\n"
    "ds_read_b64_tr_b16 v[160:161], v196 offset:40960\n"
    "ds_read_b64_tr_b16 v[162:163], v196 offset:43520\n"
    "s_waitcnt lgkmcnt(14)\n"
    "v_mfma_f32_16x16x32_bf16 v[120:123], v[108:111], v[180:183], v[120:123]\n"
    "v_mfma_f32_16x16x32_bf16 v[124:127], v[108:111], v[184:187], v[124:127]\n"
    "ds_read_b64_tr_b16 v[104:105], v196 offset:40992\n"
    "ds_read_b64_tr_b16 v[106:107], v196 offset:43552\n"
    "s_waitcnt lgkmcnt(14)\n"
    "v_mfma_f32_16x16x32_bf16 v[128:131], v[112:115], v[180:183], v[128:131]\n"
    "v_mfma_f32_16x16x32_bf16 v[132:135], v[112:115], v[184:187], v[132:135]\n"
    "ds_read_b64_tr_b16 v[108:109], v196 offset:41024\n"
    "ds_read_b64_tr_b16 v[110:111], v196 offset:43584\n"
    "s_waitcnt vmcnt(2)\n"
    "v_cmp_ne_u32 vcc, s37, v206\n"
    "s_nop 0\n"
    "s_or_b32 s40, vcc_lo, vcc_hi\n"
    "s_cbranch_scc1 GA_%=\n"
    "buffer_load_dwordx4 v[40:43], v21, s[8:11], s53 offen offset:0 sc1\n"
    "buffer_load_dwordx4 v[44:47], v21, s[8:11], s54 offen offset:0 sc1\n"
    "buffer_load_dwordx4 v[48:51], v21, s[8:11], s53 offen offset:1024 sc1\n"
    "buffer_load_dwordx4 v[52:55], v21, s[8:11], s54 offen offset:1024 sc1\n"
    "buffer_load_dwordx4 v[56:59], v21, s[8:11], s53 offen offset:2048 sc1\n"
    "buffer_load_dwordx4 v[60:63], v21, s[8:11], s54 offen offset:2048 sc1\n"
    "s_branch GB_%=\n"
    "GA_%=:\n"
    "buffer_load_dword v207, v14, s[8:11], s80 offen sc1\n"
    "GB_%=:\n"
    "s_waitcnt lgkmcnt(10)\n"
    "v_mfma_f32_16x16x32_bf16 v[136:139], v[116:119], v[180:183], v[136:139]\n"
    "v_mfma_f32_16x16x32_bf16 v[140:143], v[116:119], v[184:187], v[140:143]\n"
    "ds_read_b64_tr_b16 v[88:89], v197 offset:41024\n"
    "ds_read_b64_tr_b16 v[90:91], v197 offset:43584\n"
    "ds_read_b64_tr_b16 v[92:93], v197 offset:41056\n"
    "ds_read_b64_tr_b16 v[94:95], v197 offset:43616\n"
    "ds_read_b64_tr_b16 v[112:113], v196 offset:41056\n"
    "ds_read_b64_tr_b16 v[114:115], v196 offset:43616\n"
    "s_waitcnt lgkmcnt(14)\n"
    "v_mfma_f32_16x16x32_bf16 v[144:147], v[164:167], v[180:183], v[144:147]\n"
    "v_mfma_f32_16x16x32_bf16 v[148:151], v[164:167], v[184:187], v[148:151]\n"
    "ds_read_b64_tr_b16 v[116:117], v196 offset:41088\n"
    "ds_read_b64_tr_b16 v[118:119], v196 offset:43648\n"
    "s_waitcnt lgkmcnt(14)\n"
    "v_mfma_f32_16x16x32_bf16 v[152:155], v[168:171], v[180:183], v[152:155]\n"
    "v_mfma_f32_16x16x32_bf16 v[156:159], v[168:171], v[184:187], v[156:159]\n"
    "s_waitcnt lgkmcnt(12)\n"
    "v_mfma_f32_16x16x32_bf16 v[120:123], v[160:163], v[188:191], v[120:123]\n"
    "v_mfma_f32_16x16x32_bf16 v[124:127], v[160:163], v[192:195], v[124:127]\n"
    "ds_read_b64_tr_b16 v[96:97], v197 offset:41088\n"
    "ds_read_b64_tr_b16 v[98:99], v197 offset:43648\n"
    "s_waitcnt lgkmcnt(12)\n"
    "v_mfma_f32_16x16x32_bf16 v[128:131], v[104:107], v[188:191], v[128:131]\n"
    "v_mfma_f32_16x16x32_bf16 v[132:135], v[104:107], v[192:195], v[132:135]\n"
    "s_waitcnt lgkmcnt(10)\n"
    "v_mfma_f32_16x16x32_bf16 v[136:139], v[108:111], v[188:191], v[136:139]\n"
    "v_mfma_f32_16x16x32_bf16 v[140:143], v[108:111], v[192:195], v[140:143]\n"
    "s_waitcnt lgkmcnt(4)\n"
    "v_mfma_f32_16x16x32_bf16 v[144:147], v[112:115], v[188:191], v[144:147]\n"
    "v_mfma_f32_16x16x32_bf16 v[148:151], v[112:115], v[192:195], v[148:151]\n"
    "s_cmp_lg_u32 s40, 0\n"
    "s_cbranch_scc0 GC_%=\n"
    "s_waitcnt vmcnt(0)\n"
    "v_cmp_ne_u32 vcc, s37, v207\n"
    "s_nop 0\n"
    "s_or_b32 s40, vcc_lo, vcc_hi\n"
    "s_cbranch_scc1 GC_%=\n"
    "buffer_load_dwordx4 v[40:43], v21, s[8:11], s53 offen offset:0 sc1\n"
    "buffer_load_dwordx4 v[44:47], v21, s[8:11], s54 offen offset:0 sc1\n"
    "buffer_load_dwordx4 v[48:51], v21, s[8:11], s53 offen offset:1024 sc1\n"
    "buffer_load_dwordx4 v[52:55], v21, s[8:11], s54 offen offset:1024 sc1\n"
    "buffer_load_dwordx4 v[56:59], v21, s[8:11], s53 offen offset:2048 sc1\n"
    "buffer_load_dwordx4 v[60:63], v21, s[8:11], s54 offen offset:2048 sc1\n"
    "GC_%=:\n"
    "s_waitcnt lgkmcnt(2)\n"
    "v_mfma_f32_16x16x32_bf16 v[152:155], v[116:119], v[188:191], v[152:155]\n"
    "v_mfma_f32_16x16x32_bf16 v[156:159], v[116:119], v[192:195], v[156:159]\n"
    "s_setprio 3\n"
    "s_cmp_lg_u32 s40, 0\n"
    "s_cbranch_scc1 OP_%=\n"
    "buffer_load_dwordx4 v[64:67], v21, s[8:11], s53 offen offset:3072 sc1\n"
    "buffer_load_dwordx4 v[68:71], v21, s[8:11], s54 offen offset:3072 sc1\n"
    "s_waitcnt vmcnt(6) lgkmcnt(4)\n"
    "v_max3_u32 v200, v40, v41, v42\n"
    "v_max3_u32 v201, v43, v44, v45\n"
    "v_max3_u32 v200, v200, v46, v47\n"
    "v_max_u32 v200, v200, v201\n"
    "v_cmp_eq_u32 vcc, -1, v200\n"
    "s_mov_b32 s38, 0\n"
    "s_cbranch_vccnz FX_%=\n"
    "buffer_load_dwordx2 v[72:73], v18, s[8:11], s49 offen sc1\n"
    "buffer_load_dwordx2 v[74:75], v18, s[8:11], s49 offen offset:128 sc1\n"
    "buffer_load_dwordx2 v[76:77], v18, s[8:11], s50 offen sc1\n"
    "buffer_load_dwordx2 v[78:79], v18, s[8:11], s50 offen offset:128 sc1\n"
    "v_mfma_f32_16x16x32_bf16 v[120:123], v[216:219], v[40:43], v[120:123]\n"
    "ds_read_b64_tr_b16 v[100:101], v198 offset:20480\n"
    "v_mfma_f32_16x16x32_bf16 v[124:127], v[216:219], v[44:47], v[124:127]\n"
    "ds_read_b64_tr_b16 v[102:103], v198 offset:23040\n"
    "v_mfma_f32_16x16x32_bf16 v[128:131], v[220:223], v[40:43], v[128:131]\n"
    "ds_read_b64_tr_b16 v[104:105], v198 offset:20512\n"
    "v_mfma_f32_16x16x32_bf16 v[132:135], v[220:223], v[44:47], v[132:135]\n"
    "ds_read_b64_tr_b16 v[106:107], v198 offset:23072\n"
    "s_waitcnt vmcnt(8)\n"
    "v_mfma_f32_16x16x32_bf16 v[136:139], v[224:227], v[40:43], v[136:139]\n"
    "ds_read_b64_tr_b16 v[108:109], v198 offset:20544\n"
    "v_max3_u32 v200, v48, v49, v50\n"
    "v_mfma_f32_16x16x32_bf16 v[140:143], v[224:227], v[44:47], v[140:143]\n"
    "ds_read_b64_tr_b16 v[110:111], v198 offset:23104\n"
    "v_max3_u32 v201, v51, v52, v53\n"
    "v_mfma_f32_16x16x32_bf16 v[144:147], v[228:231], v[40:43], v[144:147]\n"
    "ds_read_b64_tr_b16 v[112:113], v198 offset:20576\n"
    "v_max3_u32 v200, v200, v54, v55\n"
    "v_mfma_f32_16x16x32_bf16 v[148:151], v[228:231], v[44:47], v[148:151]\n"
    "ds_read_b64_tr_b16 v[114:115], v198 offset:23136\n"
    "v_max_u32 v200, v200, v201\n"
    "v_mfma_f32_16x16x32_bf16 v[152:155], v[232:235], v[40:43], v[152:155]\n"
    "ds_read_b64_tr_b16 v[116:117], v198 offset:20608\n"
    "v_cmp_eq_u32 vcc, -1, v200\n"
    "v_mfma_f32_16x16x32_bf16 v[156:159], v[232:235], v[44:47], v[156:159]\n"
    "ds_read_b64_tr_b16 v[118:119], v198 offset:23168\n"
    "s_setprio 2\n"
    "s_mov_b32 s38, 1\n"
    "s_cbranch_vccnz FX_%=\n"
    "s_waitcnt lgkmcnt(4)\n"
    "v_mfma_f32_16x16x32_bf16 v[120:123], v[236:239], v[48:51], v[120:123]\n"
    "ds_read_b64_tr_b16 v[160:161], v196 offset:0\n"
    "v_mfma_f32_16x16x32_bf16 v[124:127], v[236:239], v[52:55], v[124:127]\n"
    "ds_read_b64_tr_b16 v[162:163], v196 offset:2560\n"
    "v_mfma_f32_16x16x32_bf16 v[128:131], v[240:243], v[48:51], v[128:131]\n"
    "ds_read_b64_tr_b16 v[164:165], v196 offset:32\n"
    "v_mfma_f32_16x16x32_bf16 v[132:135], v[240:243], v[52:55], v[132:135]\n"
    "ds_read_b64_tr_b16 v[166:167], v196 offset:2592\n"
    "s_waitcnt vmcnt(6)\n"
    "v_mfma_f32_16x16x32_bf16 v[136:139], v[244:247], v[48:51], v[136:139]\n"
    "ds_read_b64_tr_b16 v[168:169], v196 offset:64\n"
    "v_max3_u32 v200, v56, v57, v58\n"
    "v_mfma_f32_16x16x32_bf16 v[140:143], v[244:247], v[52:55], v[140:143]\n"
    "ds_read_b64_tr_b16 v[170:171], v196 offset:2624\n"
    "v_max3_u32 v201, v59, v60, v61\n"
    "v_mfma_f32_16x16x32_bf16 v[144:147], v[248:251], v[48:51], v[144:147]\n"
    "ds_read_b64_tr_b16 v[172:173], v196 offset:96\n"
    "v_max3_u32 v200, v200, v62, v63\n"
    "v_mfma_f32_16x16x32_bf16 v[148:151], v[248:251], v[52:55], v[148:151]\n"
    "ds_read_b64_tr_b16 v[174:175], v196 offset:2656\n"
    "v_max_u32 v200, v200, v201\n"
    "v_mfma_f32_16x16x32_bf16 v[152:155], v[252:255], v[48:51], v[152:155]\n"
    "ds_read_b64_tr_b16 v[176:177], v196 offset:128\n"
    "v_cmp_eq_u32 vcc, -1, v200\n"
    "v_mfma_f32_16x16x32_bf16 v[156:159], v[252:255], v[52:55], v[156:159]\n"
    "ds_read_b64_tr_b16 v[178:179], v196 offset:2688\n"
    "s_setprio 1\n"
    "s_mov_b32 s38, 2\n"
    "s_cbranch_vccnz FX_%=\n"
    "s_waitcnt lgkmcnt(4)\n"
    "v_mfma_f32_16x16x32_bf16 v[120:123], v[80:83], v[56:59], v[120:123]\n"
    "v_add_u32 v180, s55, v21\n"
    "v_mfma_f32_16x16x32_bf16 v[124:127], v[80:83], v[60:63], v[124:127]\n"
    "v_add_u32 v181, s88, v21\n"
    "v_mfma_f32_16x16x32_bf16 v[128:131], v[84:87], v[56:59], v[128:131]\n"
    "v_add_u32 v182, s89, v21\n"
    "s_waitcnt vmcnt(4)\n"
    "v_max3_u32 v200, v64, v65, v65\n"
    "v_mfma_f32_16x16x32_bf16 v[132:135], v[84:87], v[60:63], v[132:135]\n"
    "v_add_u32 v183, s96, v21\n"
    "v_max3_u32 v201, v66, v67, v67\n"
    "v_max3_u32 v200, v68, v69, v200\n"
    "v_mfma_f32_16x16x32_bf16 v[136:139], v[88:91], v[56:59], v[136:139]\n"
    "v_max3_u32 v201, v70, v71, v201\n"
    "s_waitcnt vmcnt(0)\n"
    "v_mfma_f32_16x16x32_bf16 v[140:143], v[88:91], v[60:63], v[140:143]\n"
    "v_add_u32 v184, s56, v21\n"
    "v_max3_u32 v200, v72, v73, v200\n"
    "v_max3_u32 v201, v74, v75, v201\n"
    "v_mfma_f32_16x16x32_bf16 v[144:147], v[92:95], v[56:59], v[144:147]\n"
    "v_xor_b32 v185, 0x1000, v184\n"
    "v_max3_u32 v200, v76, v77, v200\n"
    "v_max3_u32 v201, v78, v79, v201\n"
    "v_mfma_f32_16x16x32_bf16 v[148:151], v[92:95], v[60:63], v[148:151]\n"
    "v_bfe_u32 v214, v21, 8, 1\n"
    "v_max_u32 v200, v200, v201\n"
    "v_cmp_eq_u32 vcc, -1, v200\n"
    "v_mfma_f32_16x16x32_bf16 v[152:155], v[96:99], v[56:59], v[152:155]\n"
    "v_mad_u32_u24 v214, v214, 24, v16\n"
    "v_mfma_f32_16x16x32_bf16 v[156:159], v[96:99], v[60:63], v[156:159]\n"
    "s_setprio 0\n"
    "s_waitcnt lgkmcnt(0)\n"
    "s_barrier\n"
    "s_cbranch_vccnz OR3_%=\n"
    "OM3_%=:\n"
    "v_mfma_f32_16x16x32_bf16 v[156:159], v[116:119], v[68:71], v[156:159]\n"
    "v_lshlrev_b32 v216, 16, v208\n"
    "v_and_b32 v217, 0xffff0000, v208\n"
    "v_mfma_f32_16x16x32_bf16 v[152:155], v[116:119], v[64:67], v[152:155]\n"
    "v_lshlrev_b32 v218, 16, v209\n"
    "v_and_b32 v219, 0xffff0000, v209\n"
    "v_mfma_f32_16x16x32_bf16 v[156:159], v[176:179], v[76:79], v[156:159]\n"
    "v_lshlrev_b32 v220, 16, v210\n"
    "v_and_b32 v221, 0xffff0000, v210\n"
    "v_mfma_f32_16x16x32_bf16 v[152:155], v[176:179], v[72:75], v[152:155]\n"
    "v_lshlrev_b32 v222, 16, v211\n"
    "v_and_b32 v223, 0xffff0000, v211\n"
    "v_mfma_f32_16x16x32_bf16 v[124:127], v[100:103], v[68:71], v[124:127]\n"
    "v_lshlrev_b32 v224, 16, v212\n"
    "v_and_b32 v225, 0xffff0000, v212\n"
    "v_mfma_f32_16x16x32_bf16 v[132:135], v[104:107], v[68:71], v[132:135]\n"
    "v_lshlrev_b32 v226, 16, v213\n"
    "v_and_b32 v227, 0xffff0000, v213\n"
    "v_mfma_f32_16x16x32_bf16 v[124:127], v[160:163], v[76:79], v[124:127]\n"
    "v_mfma_f32_16x16x32_bf16 v[132:135], v[164:167], v[76:79], v[132:135]\n"
    "ds_write_b128 v185, v[156:159]\n"
    "v_mfma_f32_16x16x32_bf16 v[136:139], v[108:111], v[64:67], v[136:139]\n"
    "ds_write_b128 v184, v[152:155]\n"
    "v_mfma_f32_16x16x32_bf16 v[144:147], v[112:115], v[64:67], v[144:147]\n"
    "v_mfma_f32_16x16x32_bf16 v[136:139], v[168:171], v[72:75], v[136:139]\n"
    "v_mfma_f32_16x16x32_bf16 v[144:147], v[172:175], v[72:75], v[144:147]\n"
    "ds_write_b128 v181, v[124:127] offset:0\n"
    "v_mfma_f32_16x16x32_bf16 v[140:143], v[108:111], v[68:71], v[140:143]\n"
    "ds_write_b128 v181, v[132:135] offset:1024\n"
    "v_mfma_f32_16x16x32_bf16 v[148:151], v[112:115], v[68:71], v[148:151]\n"
    "v_mfma_f32_16x16x32_bf16 v[140:143], v[168:171], v[76:79], v[140:143]\n"
    "v_mfma_f32_16x16x32_bf16 v[148:151], v[172:175], v[76:79], v[148:151]\n"
    "ds_write_b128 v182, v[136:139] offset:2048\n"
    "v_mfma_f32_16x16x32_bf16 v[120:123], v[100:103], v[64:67], v[120:123]\n"
    "ds_write_b128 v182, v[144:147] offset:3072\n"
    "v_mfma_f32_16x16x32_bf16 v[128:131], v[104:107], v[64:67], v[128:131]\n"
    "v_mfma_f32_16x16x32_bf16 v[120:123], v[160:163], v[72:75], v[120:123]\n"
    "v_mfma_f32_16x16x32_bf16 v[128:131], v[164:167], v[72:75], v[128:131]\n"
    "ds_write_b128 v183, v[140:143] offset:4096\n"
    "s_nop 3\n"
    "ds_write_b128 v183, v[148:151] offset:5120\n"
    "v_and_b32 v184, 0xfffff3ff, v184\n"
    "s_cmp_lt_u32 s42, 2\n"
    "s_waitcnt lgkmcnt(0)\n"
    "s_barrier\n"
    "ds_read_b128 v[40:43], v180 offset:0\n"
    "ds_read_b128 v[44:47], v180 offset:2048\n"
    "ds_read_b128 v[48:51], v180 offset:4096\n"
    "ds_read_b128 v[52:55], v180 offset:1024\n"
    "ds_read_b128 v[56:59], v180 offset:3072\n"
    "ds_read_b128 v[60:63], v180 offset:5120\n"
    "ds_read_b128 v[152:155], v184\n"
    "ds_read_b128 v[64:67], v184 offset:1024\n"
    "ds_read_b128 v[68:71], v184 offset:2048\n"
    "ds_read_b128 v[72:75], v184 offset:3072\n"
    "v_pk_add_f32 v[120:121], v[120:121], v[216:217]\n"
    "v_pk_add_f32 v[122:123], v[122:123], v[218:219]\n"
    "v_pk_add_f32 v[128:129], v[128:129], v[220:221]\n"
    "v_pk_add_f32 v[130:131], v[130:131], v[222:223]\n"
    "s_waitcnt lgkmcnt(7)\n"
    "v_pk_add_f32 v[120:121], v[120:121], v[40:41]\n"
    "v_pk_add_f32 v[44:45], v[44:45], v[48:49]\n"
    "v_pk_add_f32 v[122:123], v[122:123], v[42:43]\n"
    "v_pk_add_f32 v[46:47], v[46:47], v[50:51]\n"
    "v_pk_add_f32 v[120:121], v[120:121], v[44:45]\n"
    "v_pk_add_f32 v[122:123], v[122:123], v[46:47]\n"
    "v_cvt_pk_bf16_f32 v200, v120, v121\n"
    "v_cvt_pk_bf16_f32 v201, v122, v123\n"
    "s_waitcnt lgkmcnt(4)\n"
    "v_pk_add_f32 v[128:129], v[128:129], v[52:53]\n"
    "v_pk_add_f32 v[56:57], v[56:57], v[60:61]\n"
    "v_pk_add_f32 v[130:131], v[130:131], v[54:55]\n"
    "v_pk_add_f32 v[58:59], v[58:59], v[62:63]\n"
    "v_pk_add_f32 v[128:129], v[128:129], v[56:57]\n"
    "v_pk_add_f32 v[130:131], v[130:131], v[58:59]\n"
    "v_cvt_pk_bf16_f32 v202, v128, v129\n"
    "v_cvt_pk_bf16_f32 v203, v130, v131\n"
    "s_nop 0\n"
    "v_permlane16_swap_b32 v200, v202\n"
    "v_permlane16_swap_b32 v201, v203\n"
    "buffer_store_dwordx4 v[200:203], v214, s[24:27], s47 offen\n"
    "s_cbranch_scc0 RN_%=\n"
    "s_waitcnt lgkmcnt(0)\n"
    "v_pk_add_f32 v[152:153], v[152:153], v[224:225]\n"
    "v_pk_add_f32 v[154:155], v[154:155], v[226:227]\n"
    "v_pk_add_f32 v[152:153], v[152:153], v[64:65]\n"
    "v_pk_add_f32 v[68:69], v[68:69], v[72:73]\n"
    "v_pk_add_f32 v[154:155], v[154:155], v[66:67]\n"
    "v_pk_add_f32 v[70:71], v[70:71], v[74:75]\n"
    "v_pk_add_f32 v[152:153], v[152:153], v[68:69]\n"
    "v_pk_add_f32 v[154:155], v[154:155], v[70:71]\n"
    "v_cvt_pk_bf16_f32 v64, v152, v153\n"
    "v_cvt_pk_bf16_f32 v65, v154, v155\n"
    "buffer_store_dwordx2 v[64:65], v16, s[24:27], s48 offen offset:128\n"
    "RN_%=:\n"
    "s_endpgm\n"
    "OP_%=:\n"
    "s_mov_b32 s38, 3\n"
    "FX_%=:\n"
    "s_waitcnt lgkmcnt(12)\n"
    "ds_read_b64_tr_b16 v[100:101], v198 offset:20480\n"
    "ds_read_b64_tr_b16 v[102:103], v198 offset:23040\n"
    "s_waitcnt lgkmcnt(12)\n"
    "ds_read_b64_tr_b16 v[104:105], v198 offset:20512\n"
    "ds_read_b64_tr_b16 v[106:107], v198 offset:23072\n"
    "s_waitcnt lgkmcnt(12)\n"
    "ds_read_b64_tr_b16 v[108:109], v198 offset:20544\n"
    "ds_read_b64_tr_b16 v[110:111], v198 offset:23104\n"
    "s_waitcnt lgkmcnt(12)\n"
    "ds_read_b64_tr_b16 v[112:113], v198 offset:20576\n"
    "ds_read_b64_tr_b16 v[114:115], v198 offset:23136\n"
    "s_waitcnt lgkmcnt(12)\n"
    "ds_read_b64_tr_b16 v[116:117], v198 offset:20608\n"
    "ds_read_b64_tr_b16 v[118:119], v198 offset:23168\n"
    "s_waitcnt lgkmcnt(12)\n"
    "ds_read_b64_tr_b16 v[160:161], v196 offset:0\n"
    "ds_read_b64_tr_b16 v[162:163], v196 offset:2560\n"
    "s_waitcnt lgkmcnt(12)\n"
    "ds_read_b64_tr_b16 v[164:165], v196 offset:32\n"
    "ds_read_b64_tr_b16 v[166:167], v196 offset:2592\n"
    "s_waitcnt lgkmcnt(12)\n"
    "ds_read_b64_tr_b16 v[168:169], v196 offset:64\n"
    "ds_read_b64_tr_b16 v[170:171], v196 offset:2624\n"
    "s_waitcnt lgkmcnt(12)\n"
    "ds_read_b64_tr_b16 v[172:173], v196 offset:96\n"
    "ds_read_b64_tr_b16 v[174:175], v196 offset:2656\n"
    "s_waitcnt lgkmcnt(12)\n"
    "ds_read_b64_tr_b16 v[176:177], v196 offset:128\n"
    "ds_read_b64_tr_b16 v[178:179], v196 offset:2688\n"
    "v_add_u32 v180, s55, v21\n"
    "v_add_u32 v181, s88, v21\n"
    "v_add_u32 v182, s89, v21\n"
    "v_add_u32 v183, s96, v21\n"
    "v_add_u32 v184, s56, v21\n"
    "v_xor_b32 v185, 0x1000, v184\n"
    "v_bfe_u32 v214, v21, 8, 1\n"
    "v_mad_u32_u24 v214, v214, 24, v16\n"
    "s_waitcnt lgkmcnt(0)\n"
    "s_barrier\n"
    "s_cmp_eq_u32 s38, 1\n"
    "s_cbranch_scc1 OR1_%=\n"
    "s_cmp_eq_u32 s38, 2\n"
    "s_cbranch_scc1 OR2_%=\n"
    "s_cmp_eq_u32 s38, 0\n"
    "s_cbranch_scc1 OR0_%=\n"
    "OQ_%=:\n"
    "buffer_load_dword v207, v14, s[8:11], s80 offen sc1\n"
    "s_sub_u32 s36, s36, 1\n"
    "s_cselect_b32 s36, 0, s36\n"
    "s_waitcnt vmcnt(0)\n"
    "v_cmp_ne_u32 vcc, s37, v207\n"
    "s_cbranch_scc1 OR0_%=\n"
    "s_cbranch_vccnz OQ_%=\n"
    "OR0_%=:\n"
    "buffer_load_dwordx4 v[40:43], v21, s[8:11], s53 offen offset:0 sc1\n"
    "buffer_load_dwordx4 v[44:47], v21, s[8:11], s54 offen offset:0 sc1\n"
    "buffer_load_dwordx4 v[48:51], v21, s[8:11], s53 offen offset:1024 sc1\n"
    "buffer_load_dwordx4 v[52:55], v21, s[8:11], s54 offen offset:1024 sc1\n"
    "buffer_load_dwordx4 v[56:59], v21, s[8:11], s53 offen offset:2048 sc1\n"
    "buffer_load_dwordx4 v[60:63], v21, s[8:11], s54 offen offset:2048 sc1\n"
    "buffer_load_dwordx4 v[64:67], v21, s[8:11], s53 offen offset:3072 sc1\n"
    "buffer_load_dwordx4 v[68:71], v21, s[8:11], s54 offen offset:3072 sc1\n"
    "buffer_load_dwordx2 v[72:73], v18, s[8:11], s49 offen sc1\n"
    "buffer_load_dwordx2 v[74:75], v18, s[8:11], s49 offen offset:128 sc1\n"
    "buffer_load_dwordx2 v[76:77], v18, s[8:11], s50 offen sc1\n"
    "buffer_load_dwordx2 v[78:79], v18, s[8:11], s50 offen offset:128 sc1\n"
    "s_sub_u32 s36, s36, 1\n"
    "s_cselect_b32 s36, 0, s36\n"
    "s_waitcnt vmcnt(10)\n"
    "v_max3_u32 v200, v40, v41, v42\n"
    "v_max3_u32 v201, v43, v44, v45\n"
    "v_max3_u32 v200, v200, v46, v47\n"
    "v_max_u32 v200, v200, v201\n"
    "v_cmp_eq_u32 vcc, -1, v200\n"
    "s_cbranch_scc1 OM0_%=\n"
    "s_cbranch_vccnz OR0_%=\n"
    "OM0_%=:\n"
    "v_mfma_f32_16x16x32_bf16 v[120:123], v[216:219], v[40:43], v[120:123]\n"
    "v_mfma_f32_16x16x32_bf16 v[124:127], v[216:219], v[44:47], v[124:127]\n"
    "v_mfma_f32_16x16x32_bf16 v[128:131], v[220:223], v[40:43], v[128:131]\n"
    "v_mfma_f32_16x16x32_bf16 v[132:135], v[220:223], v[44:47], v[132:135]\n"
    "s_waitcnt vmcnt(8)\n"
    "v_mfma_f32_16x16x32_bf16 v[136:139], v[224:227], v[40:43], v[136:139]\n"
    "v_max3_u32 v200, v48, v49, v50\n"
    "v_mfma_f32_16x16x32_bf16 v[140:143], v[224:227], v[44:47], v[140:143]\n"
    "v_max3_u32 v201, v51, v52, v53\n"
    "v_mfma_f32_16x16x32_bf16 v[144:147], v[228:231], v[40:43], v[144:147]\n"
    "v_max3_u32 v200, v200, v54, v55\n"
    "v_mfma_f32_16x16x32_bf16 v[148:151], v[228:231], v[44:47], v[148:151]\n"
    "v_max_u32 v200, v200, v201\n"
    "v_mfma_f32_16x16x32_bf16 v[152:155], v[232:235], v[40:43], v[152:155]\n"
    "v_cmp_eq_u32 vcc, -1, v200\n"
    "v_mfma_f32_16x16x32_bf16 v[156:159], v[232:235], v[44:47], v[156:159]\n"
    "s_setprio 2\n"
    "s_cbranch_vccnz OR1_%=\n"
    "OM1_%=:\n"
    "v_mfma_f32_16x16x32_bf16 v[120:123], v[236:239], v[48:51], v[120:123]\n"
    "v_mfma_f32_16x16x32_bf16 v[124:127], v[236:239], v[52:55], v[124:127]\n"
    "v_mfma_f32_16x16x32_bf16 v[128:131], v[240:243], v[48:51], v[128:131]\n"
    "v_mfma_f32_16x16x32_bf16 v[132:135], v[240:243], v[52:55], v[132:135]\n"
    "s_waitcnt vmcnt(6)\n"
    "v_mfma_f32_16x16x32_bf16 v[136:139], v[244:247], v[48:51], v[136:139]\n"
    "v_max3_u32 v200, v56, v57, v58\n"
    "v_mfma_f32_16x16x32_bf16 v[140:143], v[244:247], v[52:55], v[140:143]\n"
    "v_max3_u32 v201, v59, v60, v61\n"
    "v_mfma_f32_16x16x32_bf16 v[144:147], v[248:251], v[48:51], v[144:147]\n"
    "v_max3_u32 v200, v200, v62, v63\n"
    "v_mfma_f32_16x16x32_bf16 v[148:151], v[248:251], v[52:55], v[148:151]\n"
    "v_max_u32 v200, v200, v201\n"
    "v_mfma_f32_16x16x32_bf16 v[152:155], v[252:255], v[48:51], v[152:155]\n"
    "v_cmp_eq_u32 vcc, -1, v200\n"
    "v_mfma_f32_16x16x32_bf16 v[156:159], v[252:255], v[52:55], v[156:159]\n"
    "s_setprio 1\n"
    "s_cbranch_vccnz OR2_%=\n"
    "OM2_%=:\n"
    "v_mfma_f32_16x16x32_bf16 v[120:123], v[80:83], v[56:59], v[120:123]\n"
    "v_mfma_f32_16x16x32_bf16 v[124:127], v[80:83], v[60:63], v[124:127]\n"
    "v_mfma_f32_16x16x32_bf16 v[128:131], v[84:87], v[56:59], v[128:131]\n"
    "s_waitcnt vmcnt(4)\n"
    "v_max3_u32 v200, v64, v65, v65\n"
    "v_mfma_f32_16x16x32_bf16 v[132:135], v[84:87], v[60:63], v[132:135]\n"
    "v_max3_u32 v201, v66, v67, v67\n"
    "v_max3_u32 v200, v68, v69, v200\n"
    "v_mfma_f32_16x16x32_bf16 v[136:139], v[88:91], v[56:59], v[136:139]\n"
    "v_max3_u32 v201, v70, v71, v201\n"
    "s_waitcnt vmcnt(0)\n"
    "v_mfma_f32_16x16x32_bf16 v[140:143], v[88:91], v[60:63], v[140:143]\n"
    "v_max3_u32 v200, v72, v73, v200\n"
    "v_max3_u32 v201, v74, v75, v201\n"
    "v_mfma_f32_16x16x32_bf16 v[144:147], v[92:95], v[56:59], v[144:147]\n"
    "v_max3_u32 v200, v76, v77, v200\n"
    "v_max3_u32 v201, v78, v79, v201\n"
    "v_mfma_f32_16x16x32_bf16 v[148:151], v[92:95], v[60:63], v[148:151]\n"
    "v_max_u32 v200, v200, v201\n"
    "v_cmp_eq_u32 vcc, -1, v200\n"
    "v_mfma_f32_16x16x32_bf16 v[152:155], v[96:99], v[56:59], v[152:155]\n"
    "v_mfma_f32_16x16x32_bf16 v[156:159], v[96:99], v[60:63], v[156:159]\n"
    "s_setprio 0\n"
    "s_cbranch_vccnz OR3_%=\n"
    "s_branch OM3_%=\n"
    "OR1_%=:\n"
    "buffer_load_dwordx4 v[48:51], v21, s[8:11], s53 offen offset:1024 sc1\n"
    "buffer_load_dwordx4 v[52:55], v21, s[8:11], s54 offen offset:1024 sc1\n"
    "buffer_load_dwordx4 v[56:59], v21, s[8:11], s53 offen offset:2048 sc1\n"
    "buffer_load_dwordx4 v[60:63], v21, s[8:11], s54 offen offset:2048 sc1\n"
    "buffer_load_dwordx4 v[64:67], v21, s[8:11], s53 offen offset:3072 sc1\n"
    "buffer_load_dwordx4 v[68:71], v21, s[8:11], s54 offen offset:3072 sc1\n"
    "buffer_load_dwordx2 v[72:73], v18, s[8:11], s49 offen sc1\n"
    "buffer_load_dwordx2 v[74:75], v18, s[8:11], s49 offen offset:128 sc1\n"
    "buffer_load_dwordx2 v[76:77], v18, s[8:11], s50 offen sc1\n"
    "buffer_load_dwordx2 v[78:79], v18, s[8:11], s50 offen offset:128 sc1\n"
    "s_sub_u32 s36, s36, 1\n"
    "s_cselect_b32 s36, 0, s36\n"
    "s_waitcnt vmcnt(8)\n"
    "v_max3_u32 v200, v48, v49, v50\n"
    "v_max3_u32 v201, v51, v52, v53\n"
    "v_max3_u32 v200, v200, v54, v55\n"
    "v_max_u32 v200, v200, v201\n"
    "v_cmp_eq_u32 vcc, -1, v200\n"
    "s_cbranch_scc1 OM1_%=\n"
    "s_cbranch_vccnz OR1_%=\n"
    "s_branch OM1_%=\n"
    "OR2_%=:\n"
    "buffer_load_dwordx4 v[56:59], v21, s[8:11], s53 offen offset:2048 sc1\n"
    "buffer_load_dwordx4 v[60:63], v21, s[8:11], s54 offen offset:2048 sc1\n"
    "buffer_load_dwordx4 v[64:67], v21, s[8:11], s53 offen offset:3072 sc1\n"
    "buffer_load_dwordx4 v[68:71], v21, s[8:11], s54 offen offset:3072 sc1\n"
    "buffer_load_dwordx2 v[72:73], v18, s[8:11], s49 offen sc1\n"
    "buffer_load_dwordx2 v[74:75], v18, s[8:11], s49 offen offset:128 sc1\n"
    "buffer_load_dwordx2 v[76:77], v18, s[8:11], s50 offen sc1\n"
    "buffer_load_dwordx2 v[78:79], v18, s[8:11], s50 offen offset:128 sc1\n"
    "s_sub_u32 s36, s36, 1\n"
    "s_cselect_b32 s36, 0, s36\n"
    "s_waitcnt vmcnt(6)\n"
    "v_max3_u32 v200, v56, v57, v58\n"
    "v_max3_u32 v201, v59, v60, v61\n"
    "v_max3_u32 v200, v200, v62, v63\n"
    "v_max_u32 v200, v200, v201\n"
    "v_cmp_eq_u32 vcc, -1, v200\n"
    "s_cbranch_scc1 OM2_%=\n"
    "s_cbranch_vccnz OR2_%=\n"
    "s_branch OM2_%=\n"
    "OR3_%=:\n"
    "buffer_load_dwordx4 v[64:67], v21, s[8:11], s53 offen offset:3072 sc1\n"
    "buffer_load_dwordx4 v[68:71], v21, s[8:11], s54 offen offset:3072 sc1\n"
    "buffer_load_dwordx2 v[72:73], v18, s[8:11], s49 offen sc1\n"
    "buffer_load_dwordx2 v[74:75], v18, s[8:11], s49 offen offset:128 sc1\n"
    "buffer_load_dwordx2 v[76:77], v18, s[8:11], s50 offen sc1\n"
    "buffer_load_dwordx2 v[78:79], v18, s[8:11], s50 offen offset:128 sc1\n"
    "s_sub_u32 s36, s36, 1\n"
    "s_cselect_b32 s36, 0, s36\n"
    "s_waitcnt vmcnt(4)\n"
    "v_max3_u32 v200, v64, v65, v65\n"
    "v_max3_u32 v201, v66, v67, v67\n"
    "v_max3_u32 v200, v68, v69, v200\n"
    "v_max3_u32 v201, v70, v71, v201\n"
    "s_waitcnt vmcnt(0)\n"
    "v_max3_u32 v200, v72, v73, v200\n"
    "v_max3_u32 v201, v74, v75, v201\n"
    "v_max3_u32 v200, v76, v77, v200\n"
    "v_max3_u32 v201, v78, v79, v201\n"
    "v_max_u32 v200, v200, v201\n"
    "v_cmp_eq_u32 vcc, -1, v200\n"
    "s_cbranch_scc1 OM3_%=\n"
    "s_cbranch_vccnz OR3_%=\n"
    "s_branch OM3_%=\n"
    "EPA_%=:\n"
    "buffer_load_dwordx4 v[200:203], v21, s[8:11], 0 offen offset:2048 sc1\n"
    "buffer_load_dwordx4 v[204:207], v21, s[8:11], 0 offen offset:3072 sc1\n"
    "s_branch EPB_%=\n"
    "EPO_%=:\n"
    "s_mov_b32 s36, 0x10000\n"
    "EPL_%=:\n"
    "v_xor_b32 v200, s37, v200\n"
    "v_xor_b32 v202, s37, v202\n"
    "v_xor_b32 v204, s37, v204\n"
    "v_xor_b32 v206, s37, v206\n"
    "v_or3_b32 v200, v200, v202, v204\n"
    "v_or_b32 v200, v200, v206\n"
    "v_cmp_ne_u32 vcc, 0, v200\n"
    "s_nop 1\n"
    "s_cbranch_vccz EPS_%=\n"
    "buffer_load_dwordx4 v[200:203], v21, s[8:11], 0 offen offset:2048 sc1\n"
    "buffer_load_dwordx4 v[204:207], v21, s[8:11], 0 offen offset:3072 sc1\n"
    "s_sub_u32 s36, s36, 1\n"
    "s_cmp_lg_u32 s36, 0\n"
    "s_waitcnt vmcnt(0)\n"
    "s_cbranch_scc1 EPL_%=\n"
    "EPS_%=:\n"
    "v_mov_b32 v200, s37\n"
    "s_mov_b64 exec, 1\n"
    "buffer_store_dword v200, off, s[8:11], 0 sc1\n"
    "s_mov_b64 exec, -1\n"
    "s_branch EPR_%=\n"
    "SLOW_%=:\n"
    "s_mov_b32 s36, 1\n"
    "s_waitcnt vmcnt(0) lgkmcnt(0)\n"
    "s_barrier\n"
    "s_nop 15\n"
    "END_%=:\n"
    : "=&{s36}"(slow), "=&{s37}"(tag), "=&{s[8:9]}"(wsp), "=&{v[36:39]}"(q0), "=&{v[34:35]}"(q1), "+{v0}"(tid)
    : "{s[0:1]}"(__builtin_amdgcn_kernarg_segment_ptr()), "{s2}"(blockIdx.x)
    : "v1", "v2", "v3", "v4", "v5", "v6", "v7", "v8", "v9", "v10", "v11", "v12", "v13", "v14", "v15", "v16", "v17", "v18", "v19", "v20", "v21", "v23", "v24", "v25", "v26", "v27", "v28", "v29", "v30", "v31", "v32", "v33", "v40", "v41", "v42", "v43", "v44", "v45", "v46", "v47", "v48", "v49", "v50", "v51", "v52", "v53", "v54", "v55", "v56", "v57", "v58", "v59", "v60", "v61", "v62", "v63", "v64", "v65", "v66", "v67", "v68", "v69", "v70", "v71", "v72", "v73", "v74", "v75", "v76", "v77", "v78", "v79", "v80", "v81", "v82", "v83", "v84", "v85", "v86", "v87", "v88", "v89", "v90", "v91", "v92", "v93", "v94", "v95", "v96", "v97", "v98", "v99", "v100", "v101", "v102", "v103", "v104", "v105", "v106", "v107", "v108", "v109", "v110", "v111", "v112", "v113", "v114", "v115", "v116", "v117", "v118", "v119", "v120", "v121", "v122", "v123", "v124", "v125", "v126", "v127", "v128", "v129", "v130", "v131", "v132", "v133", "v134", "v135", "v136", "v137", "v138", "v139", "v140", "v141", "v142", "v143", "v144", "v145", "v146", "v147", "v148", "v149", "v150", "v151", "v152", "v153", "v154", "v155", "v156", "v157", "v158", "v159", "v160", "v161", "v162", "v163", "v164", "v165", "v166", "v167", "v168", "v169", "v170", "v171", "v172", "v173", "v174", "v175", "v176", "v177", "v178", "v179", "v180", "v181", "v182", "v183", "v184", "v185", "v186", "v187", "v188", "v189", "v190", "v191", "v192", "v193", "v194", "v195", "v196", "v197", "v198", "v199", "v200", "v201", "v202", "v203", "v204", "v205", "v206", "v207", "v208", "v209", "v210", "v211", "v212", "v213", "v214", "v215", "v216", "v217", "v218", "v219", "v220", "v221", "v222", "v223", "v224", "v225", "v226", "v227", "v228", "v229", "v230", "v231", "v232", "v233", "v234", "v235", "v236", "v237", "v238", "v239", "v240", "v241", "v242", "v243", "v244", "v245", "v246", "v247", "v248", "v249", "v250", "v251", "v252", "v253", "v254", "v255", "s4", "s5", "s6", "s7", "s10", "s11", "s12", "s13", "s14", "s15", "s16", "s17", "s18", "s19", "s20", "s21", "s22", "s23", "s24", "s25", "s26", "s27", "s28", "s29", "s30", "s31", "s38", "s39", "s40", "s41", "s42", "s43", "s44", "s45", "s46", "s47", "s48", "s49", "s50", "s51", "s52", "s53", "s54", "s55", "s56", "s57", "s58", "s59", "s60", "s61", "s62", "s63", "s64", "s65", "s66", "s67", "s68", "s69", "s70", "s71", "s72", "s73", "s74", "s75", "s76", "s77", "s78", "s79", "s80", "s81", "s82", "s83", "s84", "s85", "s86", "s87", "s88", "s89", "s90", "s91", "s92", "s93", "s94", "s95", "s96", "vcc", "scc", "memory");
  const u32 w = __builtin_amdgcn_readfirstlane(tid >> 6), l16 = (tid & 63) * 16;
  if (slow) {
    asm volatile("" ::"v"(smem) : "memory");
    const rsrc_t rsX = mkrs((u64)ax.p, ax.n), rsW = mkrs(wsp, WS_BYTES), rsD = mkrs((u64)ad.p, ad.n), rsO = mkrs((u64)ao.p, ao.n), rsOut = mkrs((u64)ay.p, ay.n);
    const u32 b = blockIdx.x, xcd = b & 7, bi = b >> 3, rb = (xcd >> 2) * 8 + (bi >> 2), cb = (xcd & 3) * 4 + (bi & 3);
    const u32 l16b = l16, ln = l16 >> 4, hh = ln >> 5, c32 = ln & 31, g = ln >> 4, c16 = ln & 15;
    const u32 kh = w & 1, tt = w >> 1, r64 = (4 * xcd + (bi >> 3)) ^ 16, c80 = bi & 7, th = w & 1, kq = w >> 1, tb0 = 4 * r64 + 2 * th;
    u32 vw[3], vo[3], vt[3];
#pragma unroll
    for (int k = 0; k < 3; ++k) {
      const u32 u = 128 * th + 64 * k + ln, r = (u * 205) >> 11, hi = r >> 4;
      vw[k] = r * 1280 + ((u - 10 * r) << 4) + 160 * c80 - k * 1024;
      vo[k] = vw[k] + ((r >> 3) & 1) * (12 * 1280) - hi * (8 * 1280); vt[k] = vw[k] + ((r >> 2) & 3) * (36 * 1280) + hi * (4 * 1280);
    }
    const u32 blk = c32 >> 3, otb = 8 * rb + 2 * tt + (g & 1);
    const u32 ka0 = blk * 768 + (((hh ^ (blk & 1)) * 8 + (c32 & 7)) << 4), ka1 = blk * 768 + 256 + (blk & 1) * 64 + (c32 & 7) * 8 + ((hh ^ (blk >> 1)) << 2);
    const u32 va = hh * 768 + 384 + vunit(c32) * 16, hpv = WS_HF + (r64 >> 1) * 128 + (4 * kq + (ln & 3)) * 8, wb = kq * 5120 + (ln >> 2) * 160 + (ln & 3) * 8;
    const u32 xrow = (16 * (tb0 + (kq & 1)) + c16) * 1280 + 160 * c80 + g * 8, xo0 = xrow + (kq >> 1) * 64, xo2 = kq < 2 ? xrow + 128 : OOB;
    const u32 otl = WS_OT + (tb0 * 32 + 8 * kq + 2 * g) * 128 + c16 * 8, osm = WS_OM + (otb * 16 + cb) * 1024 + (((2 * kh + hh) * 16 + c16) << 4);
    const u32 ost = hh == 0 ? WS_OT + (otb * 32 + 2 * cb + kh) * 128 + c16 * 8 : OOB;
    const u32 wm = kq * 5120 + th * 2048, wso = 384 * kq * 1280, oso = 160 * kq * 1280, tso = (160 * kq + 16) * 1280;
    const u32 olo = WS_OM + (tb0 * 16 + 4 * kq) * 1024, hso = WS_H + (tb0 * 48 + 12 * kq) * 1024;
    f32x16 o;
#pragma unroll
    for (int i = 0; i < 16; ++i) o[i] = 0.f;
    float mrun = -3.0e38f;
    const u32x2 qs = {q1[0], 0u};
#pragma unroll 1
    for (u32 t = 0; t < 64; ++t) {
      const u32 off = WS_KV + t * 3072;
      const bf16x8 k0 = BC(bf16x8, ld16c(rsW, off + ka0)), v0 = BC(bf16x8, ld16c(rsW, off + va)), v1 = BC(bf16x8, ld16c(rsW, off + 6144 + 1536 + va));
      const u32x2 k1 = {__builtin_amdgcn_raw_buffer_load_b32(rsW, off + ka1, 0, 16), 0u};
      f32x16 s;
#pragma unroll
      for (int j = 0; j < 16; ++j) s[j] = 0.f;
      s = __builtin_amdgcn_mfma_f32_32x32x8bf16_1k(BC(s16x4, k1), BC(s16x4, qs), s, 0, 0, 0);
      s = M32(k0, q0, s);
      float m = mrun;
#pragma unroll
      for (int j = 0; j < 16; ++j) m = fmaxf(m, s[j]);
      m = mx32(m);
      const float al = __builtin_amdgcn_exp2f(mrun - m);
      mrun = m;
      f32x8 p0, p1;
#pragma unroll
      for (int j = 0; j < 8; ++j) { p0[j] = __builtin_amdgcn_exp2f(s[j] - m); p1[j] = __builtin_amdgcn_exp2f(s[8 + j] - m); }
#pragma unroll
      for (int j = 0; j < 16; ++j) o[j] *= al;
      o = __builtin_amdgcn_smfmac_f32_32x32x32_bf16(v0, BC(bf16x16, p0), o, 0xDDDD, 0, 0);
      o = __builtin_amdgcn_smfmac_f32_32x32x32_bf16(v1, BC(bf16x16, p1), o, 0xDDDD, 0, 0);
    }
    const float il = __builtin_amdgcn_rcpf(o[12]);
    st16c(u32x4{pk(o[0] * il, o[1] * il), pk(o[2] * il, o[3] * il), pk(o[4] * il, o[5] * il), pk(o[6] * il, o[7] * il)}, rsW, osm);
    st8c(u32x2{pk(o[8] * il, o[9] * il), pk(o[10] * il, o[11] * il)}, rsW, ost);
    if (w == 0 && l16b == 0) __builtin_amdgcn_raw_buffer_store_b32(tag, rsW, WS_OF + (rb * 4 + (cb >> 2)) * 128 + (cb & 3) * 8, 0, 16);
    poll2(rsW, hpv, tag);
    if (b + w == 0) {
      for (int sp = 0; sp < SPIN_MAX; ++sp) {
        u32x4 f0 = ld16c(rsW, WS_HF + l16b), f1 = ld16c(rsW, WS_HF + 1024 + l16b);
        if (__all(f0[0] == tag && f0[2] == tag && f1[0] == tag && f1[2] == tag)) break;
        nap();
      }
      if (l16b == 0) __builtin_amdgcn_raw_buffer_store_b32(tag, rsW, WS_EPOCH, 0, 16);
    }
    f32x4 oc[10];
#pragma unroll
    for (int i = 0; i < 10; ++i) oc[i] = f32x4{0.f, 0.f, 0.f, 0.f};
#pragma unroll 1
    for (u32 j = 0; j < 17; ++j) {
      const u32 so = j < 12 ? wso + 32 * j * 1280 : j < 16 ? oso + 40 * (j - 12) * 1280 : tso;
      const u32 a0 = j < 12 ? vw[0] : j < 16 ? vo[0] : vt[0], a1 = j < 12 ? vw[1] : j < 16 ? vo[1] : vt[1], a2 = j < 12 ? vw[2] : j < 16 ? vo[2] : vt[2];
      if (j < 12) dma3(wm, a0, a1, a2, rsD, so); else dma3(wm, a0, a1, a2, rsO, so);
      u32x4 h[2];
      for (int sp = 0; sp < SPIN_MAX; ++sp) {
#pragma unroll
        for (int n = 0; n < 2; ++n) {
          if (j < 16) h[n] = ld16c(rsW, (j < 12 ? hso + n * 49152 + j * 1024 : olo + n * 16384 + (j - 12) * 1024) + l16b);
          else { const u32x2 t0 = ld8c(rsW, otl + n * 4096), t1 = ld8c(rsW, otl + n * 4096 + 128); h[n] = u32x4{t0[0], t0[1], t1[0], t1[1]}; }
        }
        const u32 mk = max(max(max(h[0][0], h[0][1]), max(h[0][2], h[0][3])), max(max(h[1][0], h[1][1]), max(h[1][2], h[1][3])));
        if (j < 12 || !__any(mk == ~0u)) break;
        nap();
      }
      waitbar<0>();
#pragma unroll
      for (int i = 0; i < 5; ++i) {
        const bf16x8 a = tr2(wb + 32 * i, wb + 2560 + 32 * i);
        oc[2 * i] = M16(a, BC(bf16x8, h[0]), oc[2 * i]); oc[2 * i + 1] = M16(a, BC(bf16x8, h[1]), oc[2 * i + 1]);
      }
      bar();
    }
#pragma unroll
    for (int i = 0; i < 10; ++i) *LDSP(f32x4, 81920 + (2 * kq + th) * 10240 + i * 1024 + l16b) = oc[i];
    bar();
#pragma unroll 1
    for (u32 k = 0; k < 3; ++k) {
      const u32 a = 81920 + th * 10240 + ((k < 2 ? 4 * (kq >> 1) + 2 * k : 8) + (kq & 1)) * 1024 + l16b, xo = k < 2 ? xo0 + 32 * k : xo2;
      const f32x4 v = *LDSP(f32x4, a) + *LDSP(f32x4, a + 20480) + *LDSP(f32x4, a + 40960) + *LDSP(f32x4, a + 61440);
      const u32x2 xr = __builtin_amdgcn_raw_buffer_load_b64(rsX, xo, 0, 0);
      __builtin_amdgcn_raw_buffer_store_b64(u32x2{pk(v[0] + blo(xr[0]), v[1] + bhi(xr[0])), pk(v[2] + blo(xr[1]), v[3] + bhi(xr[1]))}, rsOut, xo, 0, 0);
    }
  }
}

extern "C" void kernel_launch(void* const* d_in, const int* in_sizes, int n_in, void* d_out, int out_size, void* d_ws, size_t ws_size, hipStream_t stream) {
  const u32 F = 0x00020000u;
  fused_block<<<256, 512, 0, stream>>>(Buf{d_in[0], 2048 * 1280, F}, Buf{d_ws, WS_BYTES, F}, Buf{d_in[1], 640 * 1280, F}, Buf{d_in[8], 1536 * 1280, F},
                                       Buf{d_in[4], 640 * 1280, F}, Buf{d_out, 2048 * 1280, F}, Buf{d_in[6], 640 * 3072, F}, Buf{d_in[7], 640 * 3072, F},
                                       Buf{d_in[2], 25600, F}, Buf{d_in[3], 25600, F});
}
```
